```python
import torch

def _softmax(x, axis=-1):
    return torch.softmax(x, dim=axis)

def _gelu(x):
    return torch.nn.functional.gelu(x, approximate='tanh')

def _silu(x):
    return torch.nn.functional.silu(x)

def _sigmoid(x):
    return torch.sigmoid(x)

def _layernorm(x, axis=-1, eps=1e-05):
    m = x.mean(dim=axis, keepdim=True)
    v = x.var(dim=axis, keepdim=True, unbiased=False)
    return (x - m) / torch.sqrt(v + eps)

def _scatter_add(tgt, idx, src, axis=0):
    return tgt.clone().index_add_(axis, idx, src)

def _segment_sum(src, seg, n):
    out = torch.zeros((n,) + src.shape[1:], dtype=src.dtype)
    return out.index_add_(0, seg, src)

def _pad(x, pw):
    flat = tuple((v for pair in reversed(pw) for v in pair))
    return torch.nn.functional.pad(x, flat)
kernel_name = 'sequential_preLN__memory_aug_recurrent_mem_tr__glu_gated_bilinear__prompt'

def setup_inputs():

    _w = lambda a: (lambda t: t.to(torch.bfloat16) if t.is_floating_point() else t)(torch.as_tensor(a))

    def _A_setup():
        B, S, Dm = (1, 2048, 448)
        H, Dh = (16, 256)
        M, seg = (8, 32)
        seg = min(seg, S)
        S = S // seg * seg
        return {'x': _w((lambda *s: torch.randn(*s, dtype=torch.bfloat16) * 0.05)(B, S, Dm)), 'mem0': _w((lambda *s: torch.randn(*s, dtype=torch.bfloat16) * 0.2)(B, M, Dm)), 'Wq': _w((lambda *s: torch.randn(*s, dtype=torch.bfloat16) * 1.6)(Dm, H, Dh) * Dm ** (-0.5)), 'Wk': _w((lambda *s: torch.randn(*s, dtype=torch.bfloat16) * 1.6)(Dm, H, Dh) * Dm ** (-0.5)), 'Wv': _w((lambda *s: torch.randn(*s, dtype=torch.bfloat16) * 0.2)(Dm, H, Dh) * Dm ** (-0.5)), 'Wo': _w((lambda *s: torch.randn(*s, dtype=torch.bfloat16) * 0.2)(H, Dh, Dm) * (H * Dh) ** (-0.5)), 'seg_len': seg}
    _A_in = _A_setup()

    def _B_setup():
        B, S, D, F = (1, 2048, 448, 1792)
        return {'x': _w((lambda *s: torch.randn(*s, dtype=torch.bfloat16) * 0.05)(B, S, D)), 'w_gate': _w((lambda *s: torch.randn(*s, dtype=torch.bfloat16) * 0.2)(D, F) * D ** (-0.5)), 'w_up': _w((lambda *s: torch.randn(*s, dtype=torch.bfloat16) * 0.2)(D, F) * D ** (-0.5)), 'w_down': _w((lambda *s: torch.randn(*s, dtype=torch.bfloat16) * 0.2)(F, D) * F ** (-0.5))}
    _B_in = _B_setup()
    return {'A_x': _A_in['x'], 'A_mem0': _A_in['mem0'], 'A_Wq': _A_in['Wq'], 'A_Wk': _A_in['Wk'], 'A_Wv': _A_in['Wv'], 'A_Wo': _A_in['Wo'], 'A_seg_len': _A_in['seg_len'], 'B_x': _B_in['x'], 'B_w_gate': _B_in['w_gate'], 'B_w_up': _B_in['w_up'], 'B_w_down': _B_in['w_down']}

def reference(A_x, A_mem0, A_Wq, A_Wk, A_Wv, A_Wo, A_seg_len, B_x, B_w_gate, B_w_up, B_w_down):

    def _A_ref(x, mem0, Wq, Wk, Wv, Wo, seg_len):
        B, S, Dm = x.shape
        H, Dh = (Wq.shape[1], Wq.shape[2])
        M = mem0.shape[1]
        scale = Dh ** (-0.5)
        n_seg = S // seg_len
        L = M + seg_len
        idx = torch.arange(L)
        mask = (idx[:, None] < M) | (idx[:, None] >= idx[None, :])
        mem = mem0
        outs = []
        for s in range(n_seg):
            xs = x[:, s * seg_len:(s + 1) * seg_len, :].clone()
            h = torch.concatenate([mem, xs], axis=1)
            q = torch.einsum('btd,dhe->bhte', h, Wq)
            k = torch.einsum('btd,dhe->bhte', h, Wk)
            v = torch.einsum('btd,dhe->bhte', h, Wv)
            scores = torch.einsum('bhqe,bhke->bhqk', q, k) * scale
            scores = torch.where(mask[None, None], scores, -1000000000.0)
            p = _softmax(scores, axis=-1)
            o = torch.einsum('bhqk,bhke->bhqe', p, v)
            o = torch.einsum('bhte,hed->btd', o, Wo)
            mem = o[:, :M, :].clone()
            outs.append(o[:, M:, :])
        return torch.concatenate(outs, axis=1)

    def _B_ref(x, w_gate, w_up, w_down):
        g = torch.einsum('bsd,df->bsf', x, w_gate)
        u = torch.einsum('bsd,df->bsf', x, w_up)
        h = g * u
        return torch.einsum('bsf,fd->bsd', h, w_down)
    x = A_x
    h = x + _A_ref(_layernorm(x, axis=-1), A_mem0, A_Wq, A_Wk, A_Wv, A_Wo, A_seg_len)
    h2 = h + _B_ref(_layernorm(h, axis=-1), B_w_gate, B_w_up, B_w_down)
    return h2
```

```cpp
#include <hip/hip_runtime.h>
#include <hip/hip_bf16.h>
#include <stdint.h>

typedef short bf16x8 __attribute__((ext_vector_type(8)));
typedef short bf16x4 __attribute__((ext_vector_type(4)));
typedef float f32x4 __attribute__((ext_vector_type(4)));
typedef float f32x2 __attribute__((ext_vector_type(2)));
typedef __bf16 bf2_t __attribute__((ext_vector_type(2)));
typedef unsigned v4u __attribute__((ext_vector_type(4)));
typedef unsigned v2u __attribute__((ext_vector_type(2)));
typedef unsigned short u16;
#define DI __device__ __forceinline__

constexpr int DM = 448, NH = 16, DH = 256, NSEG = 64, FF = 1792, KS = 14;
constexpr unsigned SENT = 0xFFFFFFFFu;
constexpr int AUX_SC1 = 16;
constexpr int SPIN_MAX = 20000;

constexpr size_t OFF_CTR = 0;
constexpr size_t OFF_E2 = 65536;
constexpr size_t E2_STRIDE = 8192;
constexpr size_t OFF_E1 = OFF_E2 + 64 * E2_STRIDE;
constexpr size_t E1_STRIDE = 24576;
constexpr size_t OFF_XN = OFF_E1 + 64 * E1_STRIDE;
constexpr size_t OFF_WC = OFF_XN + (size_t)128 * 14 * 1024;
constexpr size_t OFF_KP = OFF_WC + (size_t)896 * 14 * 1024;
constexpr size_t OFF_VP = OFF_KP + (size_t)64 * 16 * 2 * 14 * 1024;
constexpr size_t OFF_KM = OFF_VP + (size_t)64 * 8 * 56 * 1024;
constexpr size_t OFF_VM = OFF_KM + (size_t)64 * 16 * 14 * 512;
constexpr size_t OFF_H = OFF_VM + (size_t)64 * 8 * 56 * 256;
constexpr size_t OFF_HN = OFF_H + (size_t)2048 * 448 * 2;
constexpr size_t OFF_AF = OFF_HN + (size_t)128 * 14 * 1024;
constexpr size_t OFF_WG = OFF_AF + (size_t)128 * 56 * 1024;
constexpr size_t OFF_WU = OFF_WG + (size_t)112 * 14 * 1024;
constexpr size_t OFF_WD = OFF_WU + (size_t)112 * 14 * 1024;
constexpr size_t WS_END = OFF_WD + (size_t)28 * 56 * 1024;
static_assert(WS_END < (size_t)200 * 1024 * 1024, "ws");

DI f32x4 mfma32(v4u a, v4u b, f32x4 c) {
  return __builtin_amdgcn_mfma_f32_16x16x32_bf16(__builtin_bit_cast(bf16x8, a), __builtin_bit_cast(bf16x8, b), c, 0, 0, 0);
}
DI f32x4 mfma16(v2u a, v2u b, f32x4 c) {
  return __builtin_amdgcn_mfma_f32_16x16x16bf16_1k(__builtin_bit_cast(bf16x4, a), __builtin_bit_cast(bf16x4, b), c, 0, 0, 0);
}
DI unsigned pk2(float a, float b) {
  f32x2 v = {a, b};
  return __builtin_bit_cast(unsigned, __builtin_convertvector(v, bf2_t));
}
DI v2u pk4(f32x4 a) { return v2u{pk2(a.x, a.y), pk2(a.z, a.w)}; }
DI v4u pk8(f32x4 a, f32x4 b) { return v4u{pk2(a.x, a.y), pk2(a.z, a.w), pk2(b.x, b.y), pk2(b.z, b.w)}; }
DI float bflo(unsigned u) { return __builtin_bit_cast(float, u << 16); }
DI float bfhi(unsigned u) { return __builtin_bit_cast(float, u & 0xFFFF0000u); }
DI float rbf(float a) { return bflo(pk2(a, 0.f) & 0xFFFFu); }
DI float bf2f(u16 b) { return __builtin_bit_cast(float, (unsigned)b << 16); }
DI int picol(int c, int cc) { return 32 * (c >> 2) + 16 * (cc >> 2) + 4 * (c & 3) + (cc & 3); }
DI int pidx(int ks, int kg, int jj) { return 32 * ks + 16 * (jj >> 2) + 4 * kg + (jj & 3); }
DI float xsum16(float v) { auto r = __builtin_amdgcn_permlane16_swap(__float_as_uint(v), __float_as_uint(v), false, false);
  const unsigned a = r[0], b = r[1]; return __uint_as_float(a) + __uint_as_float(b); }
DI float xsum32(float v) { auto r = __builtin_amdgcn_permlane32_swap(__float_as_uint(v), __float_as_uint(v), false, false);
  const unsigned a = r[0], b = r[1]; return __uint_as_float(a) + __uint_as_float(b); }
DI float xmax16(float v) { auto r = __builtin_amdgcn_permlane16_swap(__float_as_uint(v), __float_as_uint(v), false, false);
  const unsigned a = r[0], b = r[1]; return fmaxf(__uint_as_float(a), __uint_as_float(b)); }
DI float xmax32(float v) { auto r = __builtin_amdgcn_permlane32_swap(__float_as_uint(v), __float_as_uint(v), false, false);
  const unsigned a = r[0], b = r[1]; return fmaxf(__uint_as_float(a), __uint_as_float(b)); }
DI unsigned nosent_bf(unsigned v) { return v == SENT ? 0x7FC07FC0u : v; }
DI unsigned nosent_f(unsigned v) { return v == SENT ? 0x7FC00000u : v; }
DI bool valid4(v4u r) { return (r.x != SENT) & (r.y != SENT) & (r.z != SENT) & (r.w != SENT); }

struct Params {
  const u16 *x, *mem0, *Wq, *Wk, *Wv, *Wo, *wg, *wu, *wd;
  u16* out;
  unsigned char* ws;
};

DI float ln_den(float var) { return rbf(sqrtf(rbf(rbf(var) + 1e-5f))); }

constexpr int PW_M = 896, PW_N = 896, PW_LN = 512, PW_F = 1176, PW_I = 64;
constexpr int PREP_WAVES = PW_M + PW_N + PW_LN + PW_F + PW_I;
__global__ void __launch_bounds__(256) k_prep(Params p) {
  const int lane = threadIdx.x & 63, kg = lane >> 4, n = lane & 15;
  int wv = __builtin_amdgcn_readfirstlane(blockIdx.x * 4 + (threadIdx.x >> 6));
  unsigned char* ws = p.ws;
  if (wv < PW_M) {
    const int h = wv / 56, r = wv % 56, ks = r >> 2, jg = r & 3;
    v4u A[2][8];
#pragma unroll
    for (int hf = 0; hf < 2; ++hf)
#pragma unroll
      for (int ke = 0; ke < 8; ++ke) A[hf][ke] = *(const v4u*)(p.Wk + (size_t)(32 * ks + 16 * hf + n) * 4096 + h * 256 + 32 * ke + 8 * kg);
    for (int tt = 0; tt < 7; ++tt) {
      const int t = jg * 7 + tt;
      v4u B[8];
#pragma unroll
      for (int ke = 0; ke < 8; ++ke) B[ke] = *(const v4u*)(p.Wq + (size_t)(16 * t + n) * 4096 + h * 256 + 32 * ke + 8 * kg);
      f32x4 a0 = {0, 0, 0, 0}, a1 = {0, 0, 0, 0};
#pragma unroll
      for (int ke = 0; ke < 8; ++ke) { a0 = mfma32(A[0][ke], B[ke], a0); a1 = mfma32(A[1][ke], B[ke], a1); }
      *(v4u*)(ws + OFF_WC + ((size_t)(h * 28 + t) * 14 + ks) * 1024 + lane * 16) = pk8(a0, a1);
    }
    return;
  }
  wv -= PW_M;
  if (wv < PW_N) {
    const int h = wv / 56, r = wv % 56, cp = r >> 1, hf2 = r & 1;
    const int col = picol(2 * cp + (n >> 3), n & 7);
    v4u B[8];
#pragma unroll
    for (int ke = 0; ke < 8; ++ke) {
      unsigned e[8];
#pragma unroll
      for (int jj = 0; jj < 8; ++jj) e[jj] = p.Wo[(size_t)(h * 256 + 32 * ke + 8 * kg + jj) * 448 + col];
      B[ke] = v4u{e[0] | (e[1] << 16), e[2] | (e[3] << 16), e[4] | (e[5] << 16), e[6] | (e[7] << 16)};
    }
    for (int kk = 0; kk < 7; ++kk) {
      const int ks = hf2 * 7 + kk;
      v4u A[2][8];
#pragma unroll
      for (int hf = 0; hf < 2; ++hf)
#pragma unroll
        for (int ke = 0; ke < 8; ++ke) A[hf][ke] = *(const v4u*)(p.Wv + (size_t)(32 * ks + 16 * hf + n) * 4096 + h * 256 + 32 * ke + 8 * kg);
      f32x4 a0 = {0, 0, 0, 0}, a1 = {0, 0, 0, 0};
#pragma unroll
      for (int ke = 0; ke < 8; ++ke) { a0 = mfma32(A[0][ke], B[ke], a0); a1 = mfma32(A[1][ke], B[ke], a1); }
      const int tile = 448 + (h >> 1) * 56 + 2 * cp + (n >> 3);
      *(v4u*)(ws + OFF_WC + ((size_t)tile * 14 + ks) * 1024 + (kg * 16 + (h & 1) * 8 + (n & 7)) * 16) = pk8(a0, a1);
    }
    return;
  }
  wv -= PW_N;
  if (wv < PW_LN) {
    for (int rr = 0; rr < 4; ++rr) {
      const int row = wv * 4 + rr;
      const int c = lane < 56 ? lane : 0;
      const int ks = c >> 2, kc = c & 3;
      v2u a = *(const v2u*)(p.x + (size_t)row * 448 + 32 * ks + 4 * kc);
      v2u b = *(const v2u*)(p.x + (size_t)row * 448 + 32 * ks + 16 + 4 * kc);
      float v[8] = {bflo(a.x), bfhi(a.x), bflo(a.y), bfhi(a.y), bflo(b.x), bfhi(b.x), bflo(b.y), bfhi(b.y)};
      float s = 0.f;
#pragma unroll
      for (int j = 0; j < 8; ++j) s += v[j];
      if (lane >= 56) s = 0.f;
#pragma unroll
      for (int o = 1; o < 64; o <<= 1) s += __shfl_xor(s, o);
      const float mean = s * (1.f / 448.f);
      float q = 0.f;
#pragma unroll
      for (int j = 0; j < 8; ++j) { float d = v[j] - mean; q += d * d; }
      if (lane >= 56) q = 0.f;
#pragma unroll
      for (int o = 1; o < 64; o <<= 1) q += __shfl_xor(q, o);
      const float den = ln_den(q * (1.f / 448.f)), mb = rbf(mean);
      float o8[8];
#pragma unroll
      for (int j = 0; j < 8; ++j) o8[j] = rbf(v[j] - mb) / den;
      if (lane < 56)
        *(v4u*)(ws + OFF_XN + ((size_t)(row >> 4) * 14 + ks) * 1024 + (kc * 16 + (row & 15)) * 16) =
            v4u{pk2(o8[0], o8[1]), pk2(o8[2], o8[3]), pk2(o8[4], o8[5]), pk2(o8[6], o8[7])};
    }
    return;
  }
  wv -= PW_LN;
  if (wv < PW_F) {
    for (int ff = 0; ff < 4; ++ff) {
      const int f = wv * 4 + ff;
      const u16* src; int ldn, nt, ks; size_t dst;
      if (f < 3136) { const int m = f / 1568, r = f % 1568; nt = r / 14; ks = r % 14; src = m ? p.wu : p.wg; ldn = 1792; dst = (m ? OFF_WU : OFF_WG) + (size_t)r * 1024; }
      else { const int r = f - 3136; nt = r / 56; ks = r % 56; src = p.wd; ldn = 448; dst = OFF_WD + (size_t)r * 1024; }
      unsigned e[8];
#pragma unroll
      for (int jj = 0; jj < 8; ++jj) e[jj] = src[(size_t)pidx(ks, kg, jj) * ldn + 16 * nt + n];
      *(v4u*)(ws + dst + lane * 16) = v4u{e[0] | (e[1] << 16), e[2] | (e[3] << 16), e[4] | (e[5] << 16), e[6] | (e[7] << 16)};
    }
    return;
  }
  wv -= PW_F;
  if (wv < PW_I) {
    const v4u sent = {SENT, SENT, SENT, SENT};
    for (int i = 0; i < 24; ++i) *(v4u*)(ws + OFF_E1 + (size_t)wv * E1_STRIDE + i * 1024 + lane * 16) = sent;
    if (wv > 0) {
      for (int i = 0; i < 8; ++i) *(v4u*)(ws + OFF_E2 + (size_t)wv * E2_STRIDE + i * 1024 + lane * 16) = sent;
    } else {
      for (int i = 0; i < 7; ++i) {
        const int pc = i * 64 + lane, c = pc >> 3, row = pc & 7;
        const int ks = c >> 2, kc = c & 3;
        v2u a = *(const v2u*)(p.mem0 + (size_t)row * 448 + 32 * ks + 4 * kc);
        v2u b = *(const v2u*)(p.mem0 + (size_t)row * 448 + 32 * ks + 16 + 4 * kc);
        *(v4u*)(ws + OFF_E2 + pc * 16) = v4u{nosent_bf(a.x), nosent_bf(a.y), nosent_bf(b.x), nosent_bf(b.y)};
      }
    }
    *(v4u*)(ws + OFF_CTR + (size_t)wv * 1024 + lane * 16) = v4u{0, 0, 0, 0};
  }
}

DI void unit_kv(const Params& p, unsigned char* lds, int rb, int cg) {
  unsigned char* ws = p.ws;
  const int tid = threadIdx.x, lane = tid & 63, w = __builtin_amdgcn_readfirstlane(tid >> 6);
  const int nt0 = cg * 16 + 2 * w;
  v4u W[2][KS];
#pragma unroll
  for (int q = 0; q < 2; ++q)
#pragma unroll
    for (int ks = 0; ks < KS; ++ks) W[q][ks] = *(const v4u*)(ws + OFF_WC + ((size_t)(nt0 + q) * 14 + ks) * 1024 + lane * 16);
  {
    const unsigned char* src = ws + OFF_XN + (size_t)rb * 8 * 14 * 1024;
#pragma unroll
    for (int i = 0; i < 14; ++i) *(v4u*)(lds + (i * 512 + tid) * 16) = *(const v4u*)(src + (i * 512 + tid) * 16);
  }
  __syncthreads();
  f32x4 acc[8][2];
#pragma unroll
  for (int rt = 0; rt < 8; ++rt) { acc[rt][0] = f32x4{0, 0, 0, 0}; acc[rt][1] = f32x4{0, 0, 0, 0}; }
  if (cg < 28) {
#pragma unroll
    for (int ks = 0; ks < KS; ++ks)
#pragma unroll
      for (int rt = 0; rt < 8; ++rt) {
        const v4u a = *(const v4u*)(lds + (rt * 14 + ks) * 1024 + lane * 16);
        acc[rt][0] = mfma32(W[0][ks], a, acc[rt][0]);
        acc[rt][1] = mfma32(W[1][ks], a, acc[rt][1]);
      }
    const int h = nt0 / 28, ksj = (nt0 % 28) >> 1;
#pragma unroll
    for (int rt = 0; rt < 8; ++rt) {
      const int s = rb * 4 + (rt >> 1), u = rt & 1;
      *(v4u*)(ws + OFF_KP + ((((size_t)s * 16 + h) * 2 + u) * 14 + ksj) * 1024 + lane * 16) = pk8(acc[rt][0], acc[rt][1]);
    }
  } else {
#pragma unroll
    for (int ks = 0; ks < KS; ++ks)
#pragma unroll
      for (int rt = 0; rt < 8; ++rt) {
        const v4u a = *(const v4u*)(lds + (rt * 14 + ks) * 1024 + lane * 16);
        acc[rt][0] = mfma32(a, W[0][ks], acc[rt][0]);
        acc[rt][1] = mfma32(a, W[1][ks], acc[rt][1]);
      }
#pragma unroll
    for (int q = 0; q < 2; ++q) {
      const int idx = nt0 - 448 + q;
#pragma unroll
      for (int sg = 0; sg < 4; ++sg) {
        const int s = rb * 4 + sg;
        *(v4u*)(ws + OFF_VP + ((size_t)s * 448 + idx) * 1024 + lane * 16) = pk8(acc[2 * sg][q], acc[2 * sg + 1][q]);
      }
    }
  }
  __syncthreads();
}
__global__ void __launch_bounds__(512, 2) k_gemm_kv(Params p) {
  extern __shared__ __attribute__((aligned(16))) unsigned char smem[];
  const int b = blockIdx.x;
  const int xcd = b & 7, i = b >> 3;
  unit_kv(p, smem, i / 7, xcd * 7 + i % 7);
}

#define DELAY1 0
#define DELAY2 0
constexpr int L_MEM = 0;
constexpr int L_P1 = 16384;
constexpr int L_P2 = 20480;
__global__ void __launch_bounds__(512, 2) k_chain(Params p) {
  extern __shared__ __attribute__((aligned(16))) unsigned char smem[];
  unsigned char* ws = p.ws;
  const int tid = threadIdx.x, lane = tid & 63, w = __builtin_amdgcn_readfirstlane(tid >> 6);
  const int kg = lane >> 4, n = lane & 15;
  const int cu = blockIdx.x;
  const __amdgpu_buffer_rsrc_t rx = __builtin_amdgcn_make_buffer_rsrc((void*)ws, 0, (int)OFF_XN, 0x00020000);
  const __amdgpu_buffer_rsrc_t rkm = __builtin_amdgcn_make_buffer_rsrc((void*)(ws + OFF_KM), 0, (int)(OFF_H - OFF_KM), 0x00020000);
  auto gather_mem = [&](int s) {
    if (w < 7) {
      const int off = (int)(OFF_E2 + (size_t)s * E2_STRIDE) + w * 1024 + lane * 16;
      v4u v;
      for (int spin = 0; spin < SPIN_MAX; ++spin) {
        asm volatile("" ::: "memory");
        v = __builtin_amdgcn_raw_buffer_load_b128(rx, off, 0, AUX_SC1);
        if (__all(valid4(v))) break;
      }
      *(v4u*)(smem + L_MEM + (s & 1) * 7168 + w * 1024 + lane * 16) = v;
    }
    __syncthreads();
  };
  if (cu >= 56) {
    const int h = 2 * (cu - 56) + (w & 1);
    v4u K[2][KS];
    if (w < 2) {
#pragma unroll
      for (int u = 0; u < 2; ++u)
#pragma unroll
        for (int ks = 0; ks < KS; ++ks) K[u][ks] = *(const v4u*)(ws + OFF_KP + ((((size_t)0 * 16 + h) * 2 + u) * 14 + ks) * 1024 + lane * 16);
    }
    for (int s = 0; s < NSEG - 1; ++s) {
      gather_mem(s);
      if (w < 2) {
        const unsigned char* lm = smem + L_MEM + (s & 1) * 7168 + kg * 128 + (n & 7) * 16;
        f32x4 a0 = {0, 0, 0, 0}, a1 = {0, 0, 0, 0};
#pragma unroll
        for (int ks = 0; ks < KS; ++ks) {
          const v4u mf = *(const v4u*)(lm + ks * 512);
          a0 = mfma32(K[0][ks], mf, a0);
          a1 = mfma32(K[1][ks], mf, a1);
        }
        v4u o = pk8(a0, a1);
        o = v4u{nosent_bf(o.x), nosent_bf(o.y), nosent_bf(o.z), nosent_bf(o.w)};
        if (n < 8) __builtin_amdgcn_raw_buffer_store_b128(o, rx, (int)(OFF_E1 + (size_t)s * E1_STRIDE) + (h * 8 + n) * 64 + kg * 16, 0, AUX_SC1);
        if (s + 1 < NSEG - 1) {
#pragma unroll
          for (int u = 0; u < 2; ++u)
#pragma unroll
            for (int ks = 0; ks < KS; ++ks) K[u][ks] = *(const v4u*)(ws + OFF_KP + ((((size_t)(s + 1) * 16 + h) * 2 + u) * 14 + ks) * 1024 + lane * 16);
        }
      }
      for (int d = 0; d < DELAY1 + DELAY2 + 12; ++d) __builtin_amdgcn_s_sleep(1);
    }
    return;
  }
  const int c = cu;
  const int tile = 8 * c + w, hS = tile / 28, t = tile % 28;
  const int m7 = c / 7, r7 = c % 7;
  v4u Mf[KS], Nf[KS];
#pragma unroll
  for (int ks = 0; ks < KS; ++ks) {
    Mf[ks] = *(const v4u*)(ws + OFF_WC + ((size_t)tile * 14 + ks) * 1024 + lane * 16);
    Nf[ks] = *(const v4u*)(ws + OFF_WC + ((size_t)(448 + w * 56 + c) * 14 + ks) * 1024 + lane * 16);
  }
  const int g = n >> 3, i8 = n & 7, hO = 2 * w + g;
  for (int s = 0; s < NSEG; ++s) {
    v4u Vf = v4u{0, 0, 0, 0};
    if (s < NSEG - 1) Vf = *(const v4u*)(ws + OFF_VP + (((size_t)s * 8 + w) * 56 + c) * 1024 + lane * 16);
    gather_mem(s);
    const unsigned char* lm = smem + L_MEM + (s & 1) * 7168 + kg * 128 + i8 * 16;
    v4u mf[KS];
#pragma unroll
    for (int ks = 0; ks < KS; ++ks) mf[ks] = *(const v4u*)(lm + ks * 512);
    const v2u piece = *(const v2u*)(lm + (t >> 1) * 512 + (t & 1) * 8);
    f32x4 at = {0, 0, 0, 0};
#pragma unroll
    for (int ks = 0; ks < KS; ++ks) at = mfma32(Mf[ks], mf[ks], at);
    const v2u tb = pk4(at);
    if (s < NSEG - 1) {
      const f32x4 d2 = mfma16(tb, piece, f32x4{0, 0, 0, 0});
      if (kg < 2 && n < 8) *(f32x4*)(smem + L_P1 + w * 256 + n * 32 + kg * 16) = d2;
      __syncthreads();
      if (w == 7) {
        const int q = lane >> 4, ii = (lane >> 1) & 7, hf = lane & 1;
        f32x4 a = *(const f32x4*)(smem + L_P1 + (2 * q) * 256 + ii * 32 + hf * 16);
        f32x4 b = *(const f32x4*)(smem + L_P1 + (2 * q + 1) * 256 + ii * 32 + hf * 16);
        a += b;
        a.x = xsum16(a.x); a.y = xsum16(a.y); a.z = xsum16(a.z); a.w = xsum16(a.w);
        int hh, pp; bool st;
        if (r7 == 3) { hh = 2 * m7 + (q >> 1); pp = (q >> 1) ? 0 : 3; st = (q & 1) == 0; }
        else {
          a.x = xsum32(a.x); a.y = xsum32(a.y); a.z = xsum32(a.z); a.w = xsum32(a.w);
          hh = 2 * m7 + (r7 > 3 ? 1 : 0); pp = r7 > 3 ? r7 - 3 : r7; st = q == 0;
        }
        v4u o = __builtin_bit_cast(v4u, a);
        o = v4u{nosent_f(o.x), nosent_f(o.y), nosent_f(o.z), nosent_f(o.w)};
        if (st) __builtin_amdgcn_raw_buffer_store_b128(o, rx, (int)(OFF_E1 + (size_t)s * E1_STRIDE) + 8192 + ((hh * 4 + pp) * 8 + ii) * 32 + hf * 16, 0, AUX_SC1);
      }
    }
    f32x4 ay = {0, 0, 0, 0};
#pragma unroll
    for (int ks = 0; ks < KS; ++ks) ay = mfma32(mf[ks], Nf[ks], ay);
    const v2u yb = pk4(ay);
    if (n < 8) __builtin_amdgcn_raw_buffer_store_b64(tb, rkm, (((s * 16 + hS) * 14 + (t >> 1)) * 4 + kg) * 128 + n * 16 + (t & 1) * 8, 0, AUX_SC1);
    if (kg < 2) __builtin_amdgcn_raw_buffer_store_b64(yb, rkm, (int)(OFF_VM - OFF_KM) + ((s * 8 + w) * 56 + c) * 256 + kg * 128 + n * 8, 0, AUX_SC1);
    if (s == NSEG - 1) break;
    for (int d = 0; d < DELAY1; ++d) __builtin_amdgcn_s_sleep(1);
    const int e1 = (int)(OFF_E1 + (size_t)s * E1_STRIDE);
    const int ox = e1 + (hO * 8 + i8) * 64 + kg * 16;
    const int om = e1 + 8192 + ((hO * 4 + 2 * (kg >> 1)) * 8 + i8) * 32 + (kg & 1) * 16;
    v4u sx, s0, s1;
    for (int spin = 0; spin < SPIN_MAX; ++spin) {
        asm volatile("" ::: "memory");
      sx = __builtin_amdgcn_raw_buffer_load_b128(rx, ox, 0, AUX_SC1);
      s0 = __builtin_amdgcn_raw_buffer_load_b128(rx, om, 0, AUX_SC1);
      s1 = __builtin_amdgcn_raw_buffer_load_b128(rx, om + 256, 0, AUX_SC1);
      if (__all(valid4(sx) & valid4(s0) & valid4(s1))) break;
    }
    f32x4 sm = __builtin_bit_cast(f32x4, s0) + __builtin_bit_cast(f32x4, s1);
    sm.x = rbf(xsum32(sm.x)); sm.y = rbf(xsum32(sm.y)); sm.z = rbf(xsum32(sm.z)); sm.w = rbf(xsum32(sm.w));
    float xs[8] = {bflo(sx.x), bfhi(sx.x), bflo(sx.y), bfhi(sx.y), bflo(sx.z), bfhi(sx.z), bflo(sx.w), bfhi(sx.w)};
    float mx = fmaxf(fmaxf(sm.x, sm.y), fmaxf(sm.z, sm.w));
#pragma unroll
    for (int j = 0; j < 8; ++j) mx = fmaxf(mx, xs[j]);
    mx = xmax32(xmax16(mx));
    const float sc = 0.0625f * 1.4426950408889634f;
    float sum = 0.f;
#pragma unroll
    for (int j = 0; j < 8; ++j) { xs[j] = __builtin_amdgcn_exp2f((xs[j] - mx) * sc); sum += xs[j]; }
    f32x4 em;
    em.x = __builtin_amdgcn_exp2f((sm.x - mx) * sc); em.y = __builtin_amdgcn_exp2f((sm.y - mx) * sc);
    em.z = __builtin_amdgcn_exp2f((sm.z - mx) * sc); em.w = __builtin_amdgcn_exp2f((sm.w - mx) * sc);
    if (kg >= 2) em = f32x4{0, 0, 0, 0};
    sum += (em.x + em.y) + (em.z + em.w);
    sum = xsum32(xsum16(sum));
    const float inv = 1.f / sum;
    const v4u px = v4u{pk2(xs[0] * inv, xs[1] * inv), pk2(xs[2] * inv, xs[3] * inv), pk2(xs[4] * inv, xs[5] * inv), pk2(xs[6] * inv, xs[7] * inv)};
    const v2u pm = pk4(em * inv);
    f32x4 d = mfma32(Vf, px, f32x4{0, 0, 0, 0});
    d = mfma16(yb, pm, d);
    if ((kg >> 1) == g) *(f32x4*)(smem + L_P2 + (w * 2 + g) * 256 + i8 * 32 + (kg & 1) * 16) = d;
    __syncthreads();
    if (w == 7) {
      const int q = lane >> 4, ii = (lane >> 1) & 7, hf = lane & 1;
      const unsigned char* pp = smem + L_P2 + (4 * q) * 256 + ii * 32 + hf * 16;
      f32x4 a = (*(const f32x4*)(pp) + *(const f32x4*)(pp + 256)) + (*(const f32x4*)(pp + 512) + *(const f32x4*)(pp + 768));
      a.x = xsum32(xsum16(a.x)); a.y = xsum32(xsum16(a.y)); a.z = xsum32(xsum16(a.z)); a.w = xsum32(xsum16(a.w));
      v2u o = pk4(a);
      o = v2u{nosent_bf(o.x), nosent_bf(o.y)};
      if (q == 0) __builtin_amdgcn_raw_buffer_store_b64(o, rx, (int)(OFF_E2 + (size_t)(s + 1) * E2_STRIDE) + c * 128 + ii * 16 + hf * 8, 0, AUX_SC1);
    }
    for (int d2 = 0; d2 < DELAY2; ++d2) __builtin_amdgcn_s_sleep(1);
  }
}

constexpr int A_XN = 0;
constexpr int A_PX = 28672;
constexpr int A_PM = A_PX + 32768;
constexpr int A_ST = A_PM + 8192;
DI void unit_attn(const Params& p, unsigned char* lds, int s) {
  unsigned char* ws = p.ws;
  const int tid = threadIdx.x, lane = tid & 63, w = __builtin_amdgcn_readfirstlane(tid >> 6);
  const int kg = lane >> 4, n = lane & 15;
  {
    const unsigned char* src = ws + OFF_XN + (size_t)(2 * s) * 14 * 1024;
    for (int i = tid; i < 1792; i += 512) *(v4u*)(lds + A_XN + i * 16) = *(const v4u*)(src + i * 16);
  }
  __syncthreads();
  {
    f32x4 aS[2][3], aM[2];
#pragma unroll
    for (int g = 0; g < 2; ++g) { aS[g][0] = f32x4{0, 0, 0, 0}; aS[g][1] = f32x4{0, 0, 0, 0}; aS[g][2] = f32x4{0, 0, 0, 0}; aM[g] = f32x4{0, 0, 0, 0}; }
#pragma unroll 2
    for (int ks = 0; ks < KS; ++ks) {
      const v4u x0 = *(const v4u*)(lds + A_XN + ks * 1024 + lane * 16);
      const v4u x1 = *(const v4u*)(lds + A_XN + (14 + ks) * 1024 + lane * 16);
#pragma unroll
      for (int g = 0; g < 2; ++g) {
        const v4u k0 = *(const v4u*)(ws + OFF_KP + ((((size_t)s * 16 + 2 * w + g) * 2 + 0) * 14 + ks) * 1024 + lane * 16);
        const v4u k1 = *(const v4u*)(ws + OFF_KP + ((((size_t)s * 16 + 2 * w + g) * 2 + 1) * 14 + ks) * 1024 + lane * 16);
        aS[g][0] = mfma32(k0, x0, aS[g][0]);
        aS[g][1] = mfma32(k0, x1, aS[g][1]);
        aS[g][2] = mfma32(k1, x1, aS[g][2]);
      }
      const v4u tm = *(const v4u*)(ws + OFF_KM + (((size_t)s * 16 + 2 * w + (n >> 3)) * 14 + ks) * 512 + kg * 128 + (n & 7) * 16);
      aM[0] = mfma32(tm, x0, aM[0]);
      aM[1] = mfma32(tm, x1, aM[1]);
    }
    const float sc = 0.0625f * 1.4426950408889634f;
    const float NEG = -3.0e38f;
#pragma unroll
    for (int v = 0; v < 2; ++v) {
      float mm[4] = {rbf(aM[v].x), rbf(aM[v].y), rbf(aM[v].z), rbf(aM[v].w)};
      float mxg[2], invg[2];
      float ex[2][8];
#pragma unroll
      for (int g = 0; g < 2; ++g) {
        float xs[8];
        const f32x4 lo = v ? aS[g][1] : aS[g][0];
#pragma unroll
        for (int r = 0; r < 4; ++r) {
          const bool vis0 = v ? true : (4 * kg + r <= n);
          xs[r] = vis0 ? rbf(lo[r]) : NEG;
          const bool vis1 = v ? (4 * kg + r <= n) : false;
          xs[4 + r] = vis1 ? rbf(aS[g][2][r]) : NEG;
        }
        float mx = NEG;
#pragma unroll
        for (int j = 0; j < 8; ++j) mx = fmaxf(mx, xs[j]);
        if ((kg >> 1) == g) mx = fmaxf(mx, fmaxf(fmaxf(mm[0], mm[1]), fmaxf(mm[2], mm[3])));
        mx = xmax32(xmax16(mx));
        float sum = 0.f;
#pragma unroll
        for (int j = 0; j < 8; ++j) { ex[g][j] = xs[j] == NEG ? 0.f : __builtin_amdgcn_exp2f((xs[j] - mx) * sc); sum += ex[g][j]; }
        if ((kg >> 1) == g) {
#pragma unroll
          for (int r = 0; r < 4; ++r) sum += __builtin_amdgcn_exp2f((mm[r] - mx) * sc);
        }
        sum = xsum32(xsum16(sum));
        mxg[g] = mx; invg[g] = 1.f / sum;
        const float inv = invg[g];
        *(v4u*)(lds + A_PX + ((2 * w + g) * 2 + v) * 1024 + lane * 16) =
            v4u{pk2(ex[g][0] * inv, ex[g][1] * inv), pk2(ex[g][2] * inv, ex[g][3] * inv), pk2(ex[g][4] * inv, ex[g][5] * inv), pk2(ex[g][6] * inv, ex[g][7] * inv)};
      }
      const float mxo = (kg >> 1) ? mxg[1] : mxg[0], ino = (kg >> 1) ? invg[1] : invg[0];
      f32x4 pm;
#pragma unroll
      for (int r = 0; r < 4; ++r) pm[r] = __builtin_amdgcn_exp2f((mm[r] - mxo) * sc) * ino;
      *(v2u*)(lds + A_PM + (w * 2 + v) * 512 + lane * 8) = pk4(pm);
    }
  }
  __syncthreads();
  f32x4 acc[4][2];
#pragma unroll
  for (int q = 0; q < 4; ++q) { acc[q][0] = f32x4{0, 0, 0, 0}; acc[q][1] = f32x4{0, 0, 0, 0}; }
  const int nq = w < 4 ? 4 : 3;
#pragma unroll 2
  for (int w2 = 0; w2 < 8; ++w2) {
    v4u px[2][2]; v2u pm[2];
#pragma unroll
    for (int g = 0; g < 2; ++g)
#pragma unroll
      for (int v = 0; v < 2; ++v) px[g][v] = *(const v4u*)(lds + A_PX + ((2 * w2 + g) * 2 + v) * 1024 + lane * 16);
#pragma unroll
    for (int v = 0; v < 2; ++v) pm[v] = *(const v2u*)(lds + A_PM + (w2 * 2 + v) * 512 + lane * 8);
#pragma unroll
    for (int q = 0; q < 4; ++q) {
      if (q < nq) {
        const int ct = w + 8 * q, c = 2 * ct + (n >> 3);
        const size_t vb = OFF_VP + (((size_t)s * 8 + w2) * 56 + c) * 1024 + (kg * 16 + (n & 7)) * 16;
        const v4u v0 = *(const v4u*)(ws + vb), v1 = *(const v4u*)(ws + vb + 128);
        const v2u yf = *(const v2u*)(ws + OFF_VM + (((size_t)s * 8 + w2) * 56 + c) * 256 + (kg & 1) * 128 + ((kg >> 1) * 8 + (n & 7)) * 8);
#pragma unroll
        for (int v = 0; v < 2; ++v) {
          acc[q][v] = mfma32(v0, px[0][v], acc[q][v]);
          acc[q][v] = mfma32(v1, px[1][v], acc[q][v]);
          acc[q][v] = mfma16(yf, pm[v], acc[q][v]);
        }
      }
    }
  }
  float hv[4][2][4];
  float rs[2] = {0.f, 0.f};
#pragma unroll
  for (int q = 0; q < 4; ++q)
#pragma unroll
    for (int v = 0; v < 2; ++v) {
      if (q < nq) {
        const int ct = w + 8 * q, c = 2 * ct + (kg >> 1);
        const int col = 32 * (c >> 2) + 16 * (kg & 1) + 4 * (c & 3);
        const int row = 32 * s + 16 * v + n;
        const v2u xv = *(const v2u*)(p.x + (size_t)row * 448 + col);
        const float xf[4] = {bflo(xv.x), bfhi(xv.x), bflo(xv.y), bfhi(xv.y)};
#pragma unroll
        for (int r = 0; r < 4; ++r) { hv[q][v][r] = rbf(xf[r] + rbf(acc[q][v][r])); rs[v] += hv[q][v][r]; }
        *(v2u*)(ws + OFF_H + ((size_t)row * 448 + col) * 2) = v2u{pk2(hv[q][v][0], hv[q][v][1]), pk2(hv[q][v][2], hv[q][v][3])};
      } else {
#pragma unroll
        for (int r = 0; r < 4; ++r) hv[q][v][r] = 0.f;
      }
    }
  float* st = (float*)(lds + A_ST);
#pragma unroll
  for (int v = 0; v < 2; ++v) { rs[v] = xsum32(xsum16(rs[v])); if (kg == 0) st[w * 32 + 16 * v + n] = rs[v]; }
  __syncthreads();
  float mean[2];
#pragma unroll
  for (int v = 0; v < 2; ++v) {
    float t = 0.f;
#pragma unroll
    for (int ww = 0; ww < 8; ++ww) t += st[ww * 32 + 16 * v + n];
    mean[v] = t * (1.f / 448.f);
    float qq = 0.f;
#pragma unroll
    for (int q = 0; q < 4; ++q) if (q < nq) {
#pragma unroll
      for (int r = 0; r < 4; ++r) { const float d = hv[q][v][r] - mean[v]; qq += d * d; }
    }
    qq = xsum32(xsum16(qq));
    if (kg == 0) st[256 + w * 32 + 16 * v + n] = qq;
  }
  __syncthreads();
#pragma unroll
  for (int v = 0; v < 2; ++v) {
    float t = 0.f;
#pragma unroll
    for (int ww = 0; ww < 8; ++ww) t += st[256 + ww * 32 + 16 * v + n];
    const float den = ln_den(t * (1.f / 448.f)), mb = rbf(mean[v]);
#pragma unroll
    for (int q = 0; q < 4; ++q) if (q < nq) {
      const int ct = w + 8 * q, c = 2 * ct + (kg >> 1);
      float o[4];
#pragma unroll
      for (int r = 0; r < 4; ++r) o[r] = rbf(hv[q][v][r] - mb) / den;
      *(v2u*)(ws + OFF_HN + ((size_t)(2 * s + v) * 14 + (c >> 2)) * 1024 + ((c & 3) * 16 + n) * 16 + (kg & 1) * 8) = v2u{pk2(o[0], o[1]), pk2(o[2], o[3])};
    }
  }
  __syncthreads();
}
__global__ void __launch_bounds__(512, 2) k_attn(Params p) {
  extern __shared__ __attribute__((aligned(16))) unsigned char smem[];
  unit_attn(p, smem, blockIdx.x);
}

DI void unit_ffn1(const Params& p, unsigned char* lds, int rb, int fs) {
  unsigned char* ws = p.ws;
  const int tid = threadIdx.x, lane = tid & 63, w = __builtin_amdgcn_readfirstlane(tid >> 6);
  {
    const unsigned char* src = ws + OFF_HN + (size_t)rb * 8 * 14 * 1024;
#pragma unroll
    for (int i = 0; i < 14; ++i) *(v4u*)(lds + (i * 512 + tid) * 16) = *(const v4u*)(src + (i * 512 + tid) * 16);
  }
  __syncthreads();
  const int ft0 = fs * 16 + 2 * w;
  f32x4 ag[8][2], au[8][2];
#pragma unroll
  for (int rt = 0; rt < 8; ++rt) { ag[rt][0] = ag[rt][1] = au[rt][0] = au[rt][1] = f32x4{0, 0, 0, 0}; }
#pragma unroll 2
  for (int ks = 0; ks < KS; ++ks) {
    const v4u g0 = *(const v4u*)(ws + OFF_WG + ((size_t)ft0 * 14 + ks) * 1024 + lane * 16);
    const v4u g1 = *(const v4u*)(ws + OFF_WG + ((size_t)(ft0 + 1) * 14 + ks) * 1024 + lane * 16);
    const v4u u0 = *(const v4u*)(ws + OFF_WU + ((size_t)ft0 * 14 + ks) * 1024 + lane * 16);
    const v4u u1 = *(const v4u*)(ws + OFF_WU + ((size_t)(ft0 + 1) * 14 + ks) * 1024 + lane * 16);
#pragma unroll
    for (int rt = 0; rt < 8; ++rt) {
      const v4u a = *(const v4u*)(lds + (rt * 14 + ks) * 1024 + lane * 16);
      ag[rt][0] = mfma32(g0, a, ag[rt][0]); ag[rt][1] = mfma32(g1, a, ag[rt][1]);
      au[rt][0] = mfma32(u0, a, au[rt][0]); au[rt][1] = mfma32(u1, a, au[rt][1]);
    }
  }
#pragma unroll
  for (int rt = 0; rt < 8; ++rt) {
    f32x4 a0, a1;
#pragma unroll
    for (int r = 0; r < 4; ++r) { a0[r] = rbf(ag[rt][0][r]) * rbf(au[rt][0][r]); a1[r] = rbf(ag[rt][1][r]) * rbf(au[rt][1][r]); }
    *(v4u*)(ws + OFF_AF + ((size_t)(rb * 8 + rt) * 56 + (ft0 >> 1)) * 1024 + lane * 16) = pk8(a0, a1);
  }
  __syncthreads();
}
__global__ void __launch_bounds__(512, 2) k_ffn1(Params p) {
  extern __shared__ __attribute__((aligned(16))) unsigned char smem[];
  unit_ffn1(p, smem, blockIdx.x / 7, blockIdx.x % 7);
}

DI void unit_ffn2(const Params& p, unsigned char* lds, int rb, int cg) {
  unsigned char* ws = p.ws;
  const int tid = threadIdx.x, lane = tid & 63, w = __builtin_amdgcn_readfirstlane(tid >> 6);
  const int kg = lane >> 4, n = lane & 15;
  f32x4 acc[8][4];
#pragma unroll
  for (int rt = 0; rt < 8; ++rt)
#pragma unroll
    for (int q = 0; q < 4; ++q) acc[rt][q] = f32x4{0, 0, 0, 0};
  for (int kk = 0; kk < 7; ++kk) {
    const int kf = 7 * w + kk;
    v4u wd[4], a[8];
#pragma unroll
    for (int q = 0; q < 4; ++q) wd[q] = *(const v4u*)(ws + OFF_WD + ((size_t)(cg * 4 + q) * 56 + kf) * 1024 + lane * 16);
#pragma unroll
    for (int rt = 0; rt < 8; ++rt) a[rt] = *(const v4u*)(ws + OFF_AF + ((size_t)(rb * 8 + rt) * 56 + kf) * 1024 + lane * 16);
#pragma unroll
    for (int rt = 0; rt < 8; ++rt)
#pragma unroll
      for (int q = 0; q < 4; ++q) acc[rt][q] = mfma32(wd[q], a[rt], acc[rt][q]);
  }
#pragma unroll
  for (int rd = 0; rd < 2; ++rd) {
#pragma unroll
    for (int r4 = 0; r4 < 4; ++r4)
#pragma unroll
      for (int q = 0; q < 4; ++q) *(f32x4*)(lds + ((w * 16 + r4 * 4 + q) * 64 + lane) * 16) = acc[rd * 4 + r4][q];
    __syncthreads();
#pragma unroll
    for (int tt = 0; tt < 2; ++tt) {
      const int tl = 2 * w + tt, r4 = tl >> 2, q = tl & 3;
      f32x4 sum = {0, 0, 0, 0};
#pragma unroll
      for (int ww = 0; ww < 8; ++ww) sum += *(const f32x4*)(lds + ((ww * 16 + tl) * 64 + lane) * 16);
      const int row = rb * 128 + (rd * 4 + r4) * 16 + n, col = (cg * 4 + q) * 16 + 4 * kg;
      const v2u hv = *(const v2u*)(ws + OFF_H + ((size_t)row * 448 + col) * 2);
      const float o0 = bflo(hv.x) + rbf(sum.x), o1 = bfhi(hv.x) + rbf(sum.y), o2 = bflo(hv.y) + rbf(sum.z), o3 = bfhi(hv.y) + rbf(sum.w);
      *(v2u*)(p.out + (size_t)row * 448 + col) = v2u{pk2(o0, o1), pk2(o2, o3)};
    }
    __syncthreads();
  }
}
__global__ void __launch_bounds__(512, 2) k_ffn2(Params p) {
  extern __shared__ __attribute__((aligned(16))) unsigned char smem[];
  unit_ffn2(p, smem, blockIdx.x / 7, blockIdx.x % 7);
}

extern "C" void kernel_launch(void* const* d_in, const int* in_sizes, int n_in, void* d_out, int out_size, void* d_ws, size_t ws_size,
                              hipStream_t stream) {
  Params p;
  p.x = (const u16*)d_in[0]; p.mem0 = (const u16*)d_in[1]; p.Wq = (const u16*)d_in[2]; p.Wk = (const u16*)d_in[3];
  p.Wv = (const u16*)d_in[4]; p.Wo = (const u16*)d_in[5];
  p.wg = (const u16*)d_in[8]; p.wu = (const u16*)d_in[9]; p.wd = (const u16*)d_in[10];
  p.out = (u16*)d_out; p.ws = (unsigned char*)d_ws;
  hipFuncSetAttribute((const void*)k_gemm_kv, hipFuncAttributeMaxDynamicSharedMemorySize, 114688);
  hipFuncSetAttribute((const void*)k_chain, hipFuncAttributeMaxDynamicSharedMemorySize, 98304);
  hipFuncSetAttribute((const void*)k_attn, hipFuncAttributeMaxDynamicSharedMemorySize, 73728);
  hipFuncSetAttribute((const void*)k_ffn1, hipFuncAttributeMaxDynamicSharedMemorySize, 114688);
  hipFuncSetAttribute((const void*)k_ffn2, hipFuncAttributeMaxDynamicSharedMemorySize, 131072);
  k_prep<<<(PREP_WAVES + 3) / 4, 256, 0, stream>>>(p);
  k_gemm_kv<<<896, 512, 114688, stream>>>(p);
  k_chain<<<64, 512, 98304, stream>>>(p);
  k_attn<<<64, 512, 73728, stream>>>(p);
  k_ffn1<<<112, 512, 114688, stream>>>(p);
  k_ffn2<<<112, 512, 131072, stream>>>(p);
}
```

```cpp
#include <hip/hip_runtime.h>
#include <hip/hip_bf16.h>
#include <stdint.h>

typedef short bf16x8 __attribute__((ext_vector_type(8)));
typedef float f32x4 __attribute__((ext_vector_type(4)));
typedef float f32x2 __attribute__((ext_vector_type(2)));
typedef __bf16 bf2_t __attribute__((ext_vector_type(2)));
typedef unsigned v4u __attribute__((ext_vector_type(4)));
typedef unsigned v2u __attribute__((ext_vector_type(2)));
typedef unsigned short u16;
typedef __amdgpu_buffer_rsrc_t rsrc_t;
#define DI __device__ __forceinline__

constexpr int NSEG = 64, KS = 14;
constexpr unsigned SENT = 0xFFFFFFFFu;
constexpr int AUX_SC1 = 16;
constexpr int SPIN_MAX = 20000;
constexpr int OOB = 0x7FFFFFF0;

constexpr size_t OFF_CTR = 0;
constexpr int CTR_KV = 0, CTR_M = 4096, OFF_PTAB = 16384, CTR_FX = 16640, CTR_FS = 16896;
constexpr size_t OFF_EX = 65536;
constexpr size_t EX_STEP = 5 * 16384;
constexpr size_t OFF_PX = OFF_EX + 64 * EX_STEP;
constexpr size_t OFF_PM = OFF_PX + (size_t)128 * 16 * 1024;
constexpr size_t OFF_HF = OFF_PM + (size_t)128 * 4 * 1024;
constexpr size_t OFF_AF = OFF_HF + (size_t)128 * 14 * 1024;
constexpr size_t OFF_XN = OFF_AF + (size_t)128 * 56 * 1024;
constexpr size_t OFF_WC = OFF_XN + (size_t)128 * 14 * 1024;
constexpr size_t OFF_KP = OFF_WC + (size_t)896 * 14 * 1024;
constexpr size_t OFF_VP = OFF_KP + (size_t)64 * 16 * 2 * 14 * 1024;
constexpr size_t OFF_KM = OFF_VP + (size_t)64 * 448 * 1024;
constexpr size_t OFF_VM = OFF_KM + (size_t)64 * 16 * 14 * 512;
constexpr size_t WS_END = OFF_VM + (size_t)64 * 16 * 28 * 256;
constexpr size_t FILL_BYTES = OFF_XN - OFF_EX;
static_assert(WS_END < (size_t)200 * 1024 * 1024, "ws");
static_assert(FILL_BYTES % 16384 == 0, "fill");

DI f32x4 mfma32(v4u a, v4u b, f32x4 c) {
  return __builtin_amdgcn_mfma_f32_16x16x32_bf16(__builtin_bit_cast(bf16x8, a), __builtin_bit_cast(bf16x8, b), c, 0, 0, 0);
}
DI unsigned pk2(float a, float b) {
  f32x2 v = {a, b};
  return __builtin_bit_cast(unsigned, __builtin_convertvector(v, bf2_t));
}
DI v2u pk4(f32x4 a) { return v2u{pk2(a.x, a.y), pk2(a.z, a.w)}; }
DI v4u pk8(f32x4 a, f32x4 b) { return v4u{pk2(a.x, a.y), pk2(a.z, a.w), pk2(b.x, b.y), pk2(b.z, b.w)}; }
DI float bflo(unsigned u) { return __uint_as_float(u << 16); }
DI float bfhi(unsigned u) { return __uint_as_float(u & 0xFFFF0000u); }
DI float rbf(float a) { return bflo(pk2(a, 0.f) & 0xFFFFu); }
DI int picol(int c, int cc) { return 32 * (c >> 2) + 16 * (cc >> 2) + 4 * (c & 3) + (cc & 3); }
DI int pidx(int ks, int kg, int jj) { return 32 * ks + 16 * (jj >> 2) + 4 * kg + (jj & 3); }
DI float xsum16(float v) { auto r = __builtin_amdgcn_permlane16_swap(__float_as_uint(v), __float_as_uint(v), false, false);
  const unsigned a = r[0], b = r[1]; return __uint_as_float(a) + __uint_as_float(b); }
DI float xsum32(float v) { auto r = __builtin_amdgcn_permlane32_swap(__float_as_uint(v), __float_as_uint(v), false, false);
  const unsigned a = r[0], b = r[1]; return __uint_as_float(a) + __uint_as_float(b); }
DI float xmax16(float v) { auto r = __builtin_amdgcn_permlane16_swap(__float_as_uint(v), __float_as_uint(v), false, false);
  const unsigned a = r[0], b = r[1]; return fmaxf(__uint_as_float(a), __uint_as_float(b)); }
DI float xmax32(float v) { auto r = __builtin_amdgcn_permlane32_swap(__float_as_uint(v), __float_as_uint(v), false, false);
  const unsigned a = r[0], b = r[1]; return fmaxf(__uint_as_float(a), __uint_as_float(b)); }
DI unsigned nosent(unsigned v) { return min(v, 0xFFFFFFFEu); }
DI v2u nosent2(v2u o) { return v2u{nosent(o.x), nosent(o.y)}; }
DI v4u nosent4(v4u o) { return v4u{nosent(o.x), nosent(o.y), nosent(o.z), nosent(o.w)}; }
DI unsigned umax3(unsigned a, unsigned b, unsigned c) { return max(max(a, b), c); }
DI unsigned umax4(v4u r) { return max(umax3(r.x, r.y, r.z), r.w); }
#define KEEP4(x) asm volatile("" :: "v"(x))
#define BAR() do { asm volatile("s_waitcnt lgkmcnt(0)" ::: "memory"); __builtin_amdgcn_s_barrier(); asm volatile("" ::: "memory"); } while (0)
#define DRAIN() asm volatile("s_waitcnt vmcnt(0)" ::: "memory")
DI void sleep_units(int n) { for (int d = 0; d < n; ++d) __builtin_amdgcn_s_sleep(1); }
DI v4u ld_sc1(rsrc_t r, int off) { return __builtin_amdgcn_raw_buffer_load_b128(r, off, 0, AUX_SC1); }
DI unsigned ld1_sc1(rsrc_t r, int off) { return __builtin_amdgcn_raw_buffer_load_b32(r, off, 0, AUX_SC1); }
DI void st4_sc1(v4u v, rsrc_t r, int off) { __builtin_amdgcn_raw_buffer_store_b128(v, r, off, 0, AUX_SC1); }
DI void st2_sc1(v2u v, rsrc_t r, int off) { __builtin_amdgcn_raw_buffer_store_b64(v, r, off, 0, AUX_SC1); }
DI void st1_sc1(unsigned v, rsrc_t r, int off) { __builtin_amdgcn_raw_buffer_store_b32(v, r, off, 0, AUX_SC1); }
constexpr int FLAG_D = 32768;
DI unsigned arrive_issue(unsigned char* ws, int off) { return __hip_atomic_fetch_add((unsigned*)(ws + off), 1u, __ATOMIC_RELAXED, __HIP_MEMORY_SCOPE_AGENT); }
DI void arrive_finish(rsrc_t r, unsigned old, int off, unsigned total) { if (old == total - 1u) st1_sc1(1u, r, off + FLAG_D); }
DI void arrive(unsigned char* ws, rsrc_t r, int off, unsigned total) { arrive_finish(r, arrive_issue(ws, off), off, total); }
DI void ctr_wait(rsrc_t r, int off, unsigned want, int nap) {
  off += FLAG_D; want = 1u;
#pragma nounroll
  for (int spin = 0; spin < SPIN_MAX; ++spin) {
    asm volatile("" ::: "memory");
    const unsigned v = __builtin_amdgcn_readfirstlane(ld1_sc1(r, off));
    if (v >= want) break;
    sleep_units(nap);
  }
  asm volatile("" ::: "memory");
}

struct Params {
  const u16 *x, *mem0, *Wq, *Wk, *Wv, *Wo, *wg, *wu, *wd;
  u16* out;
  unsigned char* ws;
};

DI float ln_den(float var) { return rbf(sqrtf(rbf(rbf(var) + 1e-5f))); }

#define TL(idx)
constexpr int PREP_LDS = 288 * 528;
constexpr int FILL_KB = (int)(FILL_BYTES / 1024);
__global__ void __launch_bounds__(512, 2) k_prep(Params p) {
  extern __shared__ __attribute__((aligned(16))) unsigned char lds[];
  const int tid = threadIdx.x, lane = tid & 63, kg = lane >> 4, n = lane & 15;
  const int w = __builtin_amdgcn_readfirstlane(tid >> 6);
  unsigned char* ws = p.ws;
  const int b = blockIdx.x, half = b >> 8, bb = b & 255;
  const v4u sent = {SENT, SENT, SENT, SENT};
  if (bb < 224) {
    const int h = bb / 14, r = bb % 14, ab = r / 7, jb = r % 7;
    const u16* asrc = (half ? p.Wk : p.Wv) + (size_t)(224 * ab) * 4096 + h * 256;
    v4u sa[14], sb[4];
#pragma unroll
    for (int i = 0; i < 14; ++i) { const int pc = i * 512 + tid; sa[i] = *(const v4u*)(asrc + (size_t)(pc >> 5) * 4096 + (pc & 31) * 8); }
    if (half) {
      const u16* bsrc = p.Wq + (size_t)(64 * jb) * 4096 + h * 256;
#pragma unroll
      for (int i = 0; i < 4; ++i) { const int pc = i * 512 + tid; sb[i] = *(const v4u*)(bsrc + (size_t)(pc >> 5) * 4096 + (pc & 31) * 8); }
    } else {
      const u16* bsrc = p.Wo + (size_t)(h * 256) * 448 + 64 * jb;
#pragma unroll
      for (int i = 0; i < 4; ++i) { const int pc = i * 512 + tid; sb[i] = *(const v4u*)(bsrc + (size_t)(pc >> 3) * 448 + (pc & 7) * 8); }
    }
#pragma unroll
    for (int i = 0; i < 14; ++i) { const int pc = i * 512 + tid; *(v4u*)(lds + (pc >> 5) * 528 + (pc & 31) * 16) = sa[i]; }
    unsigned char* lb = lds + 224 * 528;
    if (half) {
#pragma unroll
      for (int i = 0; i < 4; ++i) { const int pc = i * 512 + tid; *(v4u*)(lb + (pc >> 5) * 528 + (pc & 31) * 16) = sb[i]; }
    } else {
#pragma unroll
      for (int i = 0; i < 4; ++i) {
        const int pc = i * 512 + tid, e = pc >> 3, x0 = (pc & 7) * 8;
#pragma unroll
        for (int j = 0; j < 8; ++j) {
          const int x = x0 + j, m = x >> 5, xl = x & 31, cl = (xl >> 2) & 3;
          const int slot = (2 * m + (cl >> 1)) * 16 + (cl & 1) * 8 + (xl >> 4) * 4 + (xl & 3);
          const unsigned wv = sb[i][j >> 1];
          *(u16*)(lb + slot * 528 + e * 2) = (u16)((j & 1) ? (wv >> 16) : (wv & 0xFFFFu));
        }
      }
    }
    __syncthreads();
    if (w < 7) {
      const int ks = ab * 7 + w;
      const unsigned char* pa = lds + (32 * w + n) * 528 + kg * 16;
      const unsigned char* pb = lb + n * 528 + kg * 16;
      v4u A[2][8];
#pragma unroll
      for (int hf = 0; hf < 2; ++hf)
#pragma unroll
        for (int ke = 0; ke < 8; ++ke) A[hf][ke] = *(const v4u*)(pa + hf * 16 * 528 + ke * 64);
#pragma unroll
      for (int t = 0; t < 4; ++t) {
        f32x4 a0 = {0, 0, 0, 0}, a1 = {0, 0, 0, 0};
#pragma unroll
        for (int ke = 0; ke < 8; ++ke) {
          const v4u B = *(const v4u*)(pb + t * 16 * 528 + ke * 64);
          a0 = mfma32(A[0][ke], B, a0); a1 = mfma32(A[1][ke], B, a1);
        }
        const int tile = (half ? 0 : 448) + h * 28 + jb * 4 + t;
        *(v4u*)(ws + OFF_WC + ((size_t)tile * 14 + ks) * 1024 + lane * 16) = pk8(a0, a1);
      }
    }
    return;
  }
  {
    const int row0 = half * 1024 + (bb - 224) * 32 + w * 4;
    const int c = lane < 56 ? lane : 0;
    const int ks = c >> 2, kc = c & 3;
    v2u a4[4], b4[4];
#pragma unroll
    for (int rr = 0; rr < 4; ++rr) {
      a4[rr] = *(const v2u*)(p.x + (size_t)(row0 + rr) * 448 + 32 * ks + 4 * kc);
      b4[rr] = *(const v2u*)(p.x + (size_t)(row0 + rr) * 448 + 32 * ks + 16 + 4 * kc);
    }
#pragma unroll
    for (int rr = 0; rr < 4; ++rr) {
      const int row = row0 + rr;
      const v2u a = a4[rr], b2 = b4[rr];
      float v[8] = {bflo(a.x), bfhi(a.x), bflo(a.y), bfhi(a.y), bflo(b2.x), bfhi(b2.x), bflo(b2.y), bfhi(b2.y)};
      float s = 0.f;
#pragma unroll
      for (int j = 0; j < 8; ++j) s += v[j];
      if (lane >= 56) s = 0.f;
#pragma unroll
      for (int o = 1; o < 64; o <<= 1) s += __shfl_xor(s, o);
      const float mean = s * (1.f / 448.f);
      float q = 0.f;
#pragma unroll
      for (int j = 0; j < 8; ++j) { float d = v[j] - mean; q += d * d; }
      if (lane >= 56) q = 0.f;
#pragma unroll
      for (int o = 1; o < 64; o <<= 1) q += __shfl_xor(q, o);
      const float den = ln_den(q * (1.f / 448.f)), mb = rbf(mean);
      float o8[8];
#pragma unroll
      for (int j = 0; j < 8; ++j) o8[j] = rbf(v[j] - mb) / den;
      if (lane < 56)
        *(v4u*)(ws + OFF_XN + ((size_t)(row >> 4) * 14 + ks) * 1024 + (kc * 16 + (row & 15)) * 16) =
            v4u{pk2(o8[0], o8[1]), pk2(o8[2], o8[3]), pk2(o8[4], o8[5]), pk2(o8[6], o8[7])};
    }
  }
  if (b == 224) {
    if (w < 7) {
      const int pc = w * 64 + lane;
      const int c = (pc >> 4) * 2 + ((pc >> 2) & 1), row = ((pc >> 3) & 1) * 4 + (pc & 3);
      const int ks = c >> 2, kc = c & 3;
      v2u a = *(const v2u*)(p.mem0 + (size_t)row * 448 + 32 * ks + 4 * kc);
      v2u b2 = *(const v2u*)(p.mem0 + (size_t)row * 448 + 32 * ks + 16 + 4 * kc);
      const v4u v = v4u{nosent(a.x), nosent(a.y), nosent(b2.x), nosent(b2.y)};
      *(v4u*)(ws + OFF_EX + pc * 16) = v;
      *(v4u*)(ws + OFF_EX + 8192 + pc * 16) = v;
    } else {
      *(v4u*)(ws + OFF_EX + 7168 + lane * 16) = sent;
      *(v4u*)(ws + OFF_EX + 8192 + 7168 + lane * 16) = sent;
    }
  }
  if (b == 225) {
#pragma unroll
    for (int i = 0; i < 8; ++i) *(v4u*)(ws + OFF_CTR + (size_t)(i * 512 + tid) * 16) = v4u{0, 0, 0, 0};
  }
}
#define STAMP(k)
#define STAMPV(k, v)
#define STAMP_INIT()
#define STAMP_DUMP()
constexpr int L_MEM = 0;
constexpr int L_P1 = 16384;
constexpr int L_YB = 24576;
constexpr int N_S = 56, N_O = 56, N_X = 8, N_CHAIN = N_S + N_O + N_X;
constexpr int L_TAB2 = 22528;
constexpr int L_VOTE = 23808;
#define L_DUMMY (14336 + lane * 16)
#define ex_off(s, c) __builtin_amdgcn_readfirstlane(*(const int*)(smem + L_TAB2 + ((s) * 5 + (c)) * 4))
#define T_A 120
#define T_B 110
#define NREP 4
#define T_OG 0
#define T_PEN 60
#define T_UP 0
#define T_OKN 1000
constexpr int L_SCH = 23872;
DI unsigned rt32() { unsigned long long t; asm volatile("s_memrealtime %0\n\ts_waitcnt lgkmcnt(0)" : "=s"(t) :: "memory"); return (unsigned)t; }
DI unsigned wait_until(unsigned t) {
  int d = (int)(t - rt32());
  if (d < -2) return t - d;
  if (d > 3000) d = 3000;
  if (d > 24) { sleep_units(((d - 16) * 3) >> 3); d = (int)(t - rt32()); }
  if (d > 0 && d < 64) sleep_units((d * 3) >> 3);
  return t;
}
struct Sched { unsigned tE1, tE2; int m1, m2, ok, nf; };
DI void sched_step(Sched& y, unsigned word, int lane, int s) {
  if (s == 0) { y.tE1 = rt32(); y.tE2 = y.tE1; return; }
  int dv = lane < NREP ? (int)((word & ~3u) - (y.tE1 << 2)) >> 2 : -0x40000000;
  unsigned fl = lane < NREP ? (word & 3u) : 0u;
  if (s > 1 && __all(lane >= NREP || word == (y.tE1 << 2))) { dv = 0; fl = 0; }
  else {
#pragma unroll
    for (int o = 1; o < NREP; o <<= 1) { dv = max(dv, __shfl_xor(dv, o)); fl |= (unsigned)__shfl_xor((int)fl, o); }
    dv = __builtin_amdgcn_readfirstlane(dv); fl = __builtin_amdgcn_readfirstlane(fl);
  }
  unsigned base = y.tE2;
  if (dv > 0 || s == 1) base = y.tE1 + dv + T_B + y.m2 + T_PEN;
  if (s > 2) {
    if (fl & 1u) { y.m2 += T_UP; y.ok = 0; }
    else if (fl & 2u) { y.m1 += T_UP; y.ok = 0; }
    else if (++y.ok >= T_OKN) { y.ok = 0; y.m1 -= 1; y.m2 -= 1; }
  }
  y.tE1 = base + T_A + y.m1;
  y.tE2 = y.tE1 + T_B + y.m2;
}
#define FAIL_DUMP(v)

DI int gather_poll(__amdgpu_buffer_rsrc_t rx, unsigned char* dst0, unsigned char* dst1, int o0, int o1, int ot, unsigned& word) {
  int spin = 0;
#pragma nounroll
  for (;;) {
    asm volatile("" ::: "memory");
    const v4u v0 = ld_sc1(rx, o0), v1 = ld_sc1(rx, o1);
    word = __builtin_amdgcn_raw_buffer_load_b32(rx, ot, 0, AUX_SC1);
    *(v4u*)dst0 = v0;
    *(v4u*)dst1 = v1;
    if (__builtin_expect(__all(umax3(umax4(v0), umax4(v1), word) != SENT), 1)) break;
    if (++spin >= SPIN_MAX) break;
    __builtin_amdgcn_s_sleep(2);
  }
  return spin;
}

DI void probe_place(const Params& p, const rsrc_t rx) {
  unsigned char* ws = p.ws;
  const int lane = threadIdx.x & 63, w = __builtin_amdgcn_readfirstlane(threadIdx.x >> 6);
  const unsigned a0 = (unsigned)(size_t)(ws + OFF_EX);
    if (w >= 4) {
      const int r = w - 4;
      int pr = -1;
      for (int k = 0; k < 64; ++k) if (pr < 0 && (int)(((a0 + (unsigned)k * 81920u) >> 21) - (a0 >> 21)) == r) pr = k * 5;
      unsigned res = 2;
      if (pr >= 0) {
        const int base = (int)OFF_EX + pr * 16384 + 7168;
        const v4u sent = {SENT, SENT, SENT, SENT};
        __builtin_amdgcn_raw_buffer_store_b128(sent, rx, base + lane * 16, 0, AUX_SC1);
        __builtin_amdgcn_raw_buffer_store_b128(sent, rx, base + 8192 + lane * 16, 0, AUX_SC1);
        asm volatile("s_waitcnt vmcnt(0)" ::: "memory");
        unsigned best[2] = {0xFFFFFFFFu, 0xFFFFFFFFu};
#pragma unroll
        for (int k = 0; k < 8; ++k) {
          unsigned long long t0, t1;
          asm volatile("s_memtime %0\n\ts_waitcnt lgkmcnt(0)" : "=s"(t0) :: "memory");
          unsigned v = __builtin_amdgcn_raw_buffer_load_b32(rx, base + (k & 1) * 8192 + (k >> 1) * 128, 0, AUX_SC1);
          asm volatile("s_waitcnt vmcnt(0)" :: "v"(v) : "memory");
          asm volatile("s_memtime %0\n\ts_waitcnt lgkmcnt(0)" : "=s"(t1) :: "memory");
          best[k & 1] = min(best[k & 1], (unsigned)(t1 - t0));
        }
        const unsigned b13 = ((a0 + (unsigned)pr * 16384u) >> 13) & 1;
        res = 2 + ((best[1] < best[0]) ? (b13 ^ 1) : b13);
      }
      if (lane == 0) __builtin_amdgcn_raw_buffer_store_b32(res, rx, (int)OFF_PTAB + r * 4, 0, AUX_SC1);
    }
}

DI void chain_role(const Params& p, unsigned char* smem, const rsrc_t rx, const int cu) {
  unsigned char* ws = p.ws;
  const int tid = threadIdx.x, lane = tid & 63, w = __builtin_amdgcn_readfirstlane(tid >> 6);
  const int kg = lane >> 4, n = lane & 15;
  const float sc = 0.0625f * 1.4426950408889634f;
  STAMP_INIT();
  auto startup = [&]() {
  {
    const v4u sent = {SENT, SENT, SENT, SENT};
    constexpr int EX_KB = (int)(64 * EX_STEP / 1024);
    for (int i = 16 + cu * 8 + w; i < EX_KB; i += N_CHAIN * 8) st4_sc1(sent, rx, (int)OFF_EX + i * 1024 + lane * 16);
    DRAIN();
    __syncthreads();
    if (tid == 448) arrive(ws, rx, CTR_FX, (unsigned)N_CHAIN);
  }
  {
    const unsigned a0 = (unsigned)(size_t)(ws + OFF_EX);
    if (w == 0) {
      unsigned v = 0;
      for (int spin = 0; spin < SPIN_MAX; ++spin) {
        asm volatile("" ::: "memory");
        v = __builtin_amdgcn_raw_buffer_load_b32(rx, (int)OFF_PTAB + (lane & 3) * 4, 0, AUX_SC1);
        if (__all(v != 0)) break;
        __builtin_amdgcn_s_sleep(4);
      }
      ctr_wait(rx, CTR_FX, (unsigned)N_CHAIN, 4);
      if (lane < 4) *(unsigned*)(smem + L_VOTE + lane * 4) = v;
      if (lane < 2) *(unsigned*)(smem + L_SCH + lane * 4) = 0u;
    }
    __syncthreads();
    const unsigned a = a0 + (unsigned)tid * 16384u;
    const int reg = (int)((a >> 21) - (a0 >> 21)) & 3, b13 = (a >> 13) & 1;
    if (tid < 320) { const int nearbit = *(const int*)(smem + L_VOTE + reg * 4) & 1; *(int*)(smem + L_TAB2 + tid * 4) = (int)OFF_EX + tid * 16384 + ((nearbit != b13) ? 8192 : 0); }
    __syncthreads();
  }
  };
  const int lmoff = (kg >> 1) * 256 + ((n & 7) >> 2) * 128 + (kg & 1) * 64 + (n & 3) * 16;
  const int goff = (w - 4) * 2048 + lane * 16;
  if (cu < N_S) {
    const int c = cu, m7 = c / 7, r7 = c % 7;
    if (w < 4) {
      const int tile = 8 * c + 2 * w, hS = tile / 28, ksj = (tile % 28) >> 1;
      v4u Mf[2][KS];
#pragma unroll
      for (int q = 0; q < 2; ++q)
#pragma unroll
        for (int ks = 0; ks < KS; ++ks) Mf[q][ks] = *(const v4u*)(ws + OFF_WC + ((size_t)(tile + q) * 14 + ks) * 1024 + lane * 16);
      startup();
#pragma unroll
      for (int ks = 0; ks < KS; ++ks) { KEEP4(Mf[0][ks]); KEEP4(Mf[1][ks]); }
      unsigned char* pdst = smem + ((kg < 2 && n < 8) ? L_P1 + (kg * 8 + n) * 16 + w * 4 : L_DUMMY);
      const int kmo = (int)OFF_KM + ((hS * 14 + ksj) * 4 + kg) * 128 + n * 16;
#pragma nounroll
      for (int s = 0; s < NSEG; ++s) {
        const unsigned char* lm = smem + L_MEM + (s & 1) * 7168 + lmoff;
        BAR();
        STAMP(2);
        v4u mf[KS];
#pragma unroll
        for (int ks = 0; ks < KS; ++ks) mf[ks] = *(const v4u*)(lm + ks * 512);
        const v4u piece = *(const v4u*)(lm + ksj * 512);
        __builtin_amdgcn_sched_barrier(0);
        f32x4 a0 = {0, 0, 0, 0}, a1 = {0, 0, 0, 0};
#pragma unroll
        for (int ks = 0; ks < KS; ++ks) { a0 = mfma32(Mf[0][ks], mf[ks], a0); a1 = mfma32(Mf[1][ks], mf[ks], a1); }
        const v4u tb = pk8(a0, a1);
        const f32x4 d2 = mfma32(tb, piece, f32x4{0, 0, 0, 0});
        *(float*)(pdst) = d2.x; *(float*)(pdst + 256) = d2.y; *(float*)(pdst + 512) = d2.z; *(float*)(pdst + 768) = d2.w;
        STAMP(3);
        BAR();
        st4_sc1(tb, rx, n < 8 ? kmo + s * (16 * 14 * 512) : OOB);
        DRAIN();
      }
      BAR();
    } else {
      startup();
      Sched sd = {0u, 0u, 0, 0, 0, 0};
      const bool strad = r7 == 3;
      const int ii = lane & 7, key = ((lane >> 3) & 1) * 4 + (lane >> 4);
      const int hh = 2 * m7 + (r7 > 3 ? 1 : 0), pp = r7 > 3 ? r7 - 3 : r7;
      const int so1 = ((hh * 4 + pp) * 4 + (ii & 3)) * 32 + key * 4;
      const int so2 = (((2 * m7 + 1) * 4 + 0) * 4 + (ii & 3)) * 32 + key * 4;
      const unsigned char* psrc = smem + L_P1 + lane * 16;
#pragma nounroll
      for (int s = 0; s < NSEG; ++s) {
        const int o0 = ex_off(s, 0) + goff;
        const int eo2 = ex_off(s, 2), eo4 = ex_off(s, 4);
        const int eb = (ii >> 2) ? eo4 : eo2;
        const int sto1 = (w == 4 && s < NSEG - 1) ? eb + so1 : OOB;
        const int sto2 = (w == 4 && s < NSEG - 1 && strad) ? eb + so2 : OOB;
        unsigned char* d0 = smem + L_MEM + (s & 1) * 7168 + goff;
        unsigned char* d1 = w == 7 ? smem + L_DUMMY : d0 + 1024;
        const int ot = (s > 0 && lane < NREP) ? o0 - goff + 7168 + lane * 128 : OOB;
        unsigned word;
        KEEP4(sto1); KEEP4(sto2);
        if (s > 1) wait_until(sd.tE2);
        STAMP(0);
        const int spin = gather_poll(rx, d0, d1, o0, w == 7 ? OOB : o0 + 1024, ot, word);
        BAR();
        STAMP(1);
        if (w == 7) TL(s);
        BAR();
        const f32x4 a = *(const f32x4*)psrc;
        const float u0 = a.x + a.y, u1 = a.z + a.w;
        const float v1 = strad ? u0 : u0 + u1;
        __builtin_amdgcn_raw_buffer_store_b32(nosent(__float_as_uint(v1)), rx, sto1, 0, AUX_SC1);
        __builtin_amdgcn_raw_buffer_store_b32(nosent(__float_as_uint(u1)), rx, sto2, 0, AUX_SC1);
        __builtin_amdgcn_sched_barrier(0);
        STAMP(5);
        STAMPV(10, spin);
        sched_step(sd, word, lane, s);
        sd.nf += (s > 1 && spin) ? 1 : 0;
        if (w == 6 && lane == 0 && s > 0) arrive(ws, rx, CTR_M + (s - 1) * 128, 84u);
      }
      BAR();
      if (w == 6 && lane == 0) arrive(ws, rx, CTR_M + (NSEG - 1) * 128, 84u);
      FAIL_DUMP(sd.nf);
    }
  } else if (cu < N_S + N_O) {
    const int o_ = cu - N_S, rg = o_ / 28, ct = o_ % 28;
    v4u Nf[2][KS];
#pragma unroll
    for (int q = 0; q < 2; ++q)
#pragma unroll
      for (int ks = 0; ks < KS; ++ks) Nf[q][ks] = *(const v4u*)(ws + OFF_WC + ((size_t)(448 + (2 * w + q) * 28 + ct) * 14 + ks) * 1024 + lane * 16);
    startup();
#pragma unroll
    for (int ks = 0; ks < KS; ++ks) { KEEP4(Nf[0][ks]); KEEP4(Nf[1][ks]); }
    const int vmo = (int)OFF_VM + ((2 * w) * 28 + ct) * 256 + n * 16 + kg * 8;
    auto compute_y = [&](int s) {
      const unsigned char* lm = smem + L_MEM + (s & 1) * 7168 + lmoff;
      v4u mf[KS];
#pragma unroll
      for (int ks = 0; ks < KS; ++ks) mf[ks] = *(const v4u*)(lm + ks * 512);
      __builtin_amdgcn_sched_barrier(0);
      f32x4 a0 = {0, 0, 0, 0}, a1 = {0, 0, 0, 0};
#pragma unroll
      for (int ks = 0; ks < KS; ++ks) { a0 = mfma32(mf[ks], Nf[0][ks], a0); a1 = mfma32(mf[ks], Nf[1][ks], a1); }
      v2u y0 = pk4(a0), y1 = pk4(a1);
      const int vo = (rg == 0 && kg < 2) ? vmo + s * (16 * 28 * 256) : OOB;
      st2_sc1(y0, rx, vo);
      st2_sc1(y1, rx, (rg == 0 && kg < 2) ? vo + 28 * 256 : OOB);
      unsigned char* yd = smem + (kg < 2 ? L_YB + ((2 * w) * 4 + 2 * kg) * 64 + n * 4 : L_DUMMY);
      *(unsigned*)(yd) = y0.x; *(unsigned*)(yd + 64) = y0.y;
      *(unsigned*)(yd + 256) = y1.x; *(unsigned*)(yd + 320) = y1.y;
    };
    if (w < 4) {
      int nf = 0;
      v4u Vf[4];
      ctr_wait(rx, CTR_KV, 56u, 4);
#pragma unroll
      for (int g2 = 0; g2 < 4; ++g2) Vf[g2] = ld_sc1(rx, (int)OFF_VP + ((4 * w + g2) * 28 + ct) * 1024 + lane * 16);
      const int g = n >> 2, i4 = n & 3, hP = 4 * w + g;
      unsigned char* pdst = smem + L_P1 + (kg * 4 + i4) * 80 + (w * 4 + g) * 4;
      const unsigned char* ys = smem + L_YB + ((4 * w) * 4 + 2 * (kg & 1) + (kg >> 1)) * 64 + n * 4;
#pragma nounroll
      for (int s = 0; s < NSEG; ++s) {
        const int eb = ex_off(s, 1 + 2 * rg);
        const int oe = eb + (hP * 4 + i4) * 64 + kg * 16;
        const int ol = eb + 4096 + (hP * 4 + i4) * 4;
        const int om = ex_off(s, 2 + 2 * rg) + ((hP * 4 + 2 * (kg >> 1)) * 4 + i4) * 32 + (kg & 1) * 16;
#pragma unroll
        for (int g2 = 0; g2 < 4; ++g2) KEEP4(Vf[g2]);
        KEEP4(oe); KEEP4(ol); KEEP4(om);
        BAR();
        STAMP(2);
        compute_y(s);
        if (s == NSEG - 1) break;
        unsigned tiss;
        for (;;) {
          const v2u tv = *(volatile const v2u*)(smem + L_SCH);
          tiss = __builtin_amdgcn_readfirstlane(tv.x);
          if (__builtin_amdgcn_readfirstlane(tv.y) == (unsigned)(s + 1)) break;
        }
        STAMP(3);
        if (s > 0) tiss = wait_until(tiss);
        STAMP(6);
        int spin = 0;
        f32x4 dres;
        v4u Yf = v4u{0, 0, 0, 0};
#pragma nounroll
        for (;;) {
          asm volatile("" ::: "memory");
          const v4u q0 = ld_sc1(rx, om), q1 = ld_sc1(rx, om + 128);
          const unsigned ls = __builtin_amdgcn_raw_buffer_load_b32(rx, ol, 0, AUX_SC1);
          const v4u ex = ld_sc1(rx, oe);
          __builtin_amdgcn_sched_barrier(0);
          if (spin == 0) {
            BAR();
            Yf = v4u{*(const unsigned*)(ys), *(const unsigned*)(ys + 256), *(const unsigned*)(ys + 512), *(const unsigned*)(ys + 768)};
            KEEP4(Yf);
          }
          __builtin_amdgcn_sched_barrier(0);
          const f32x4 sq = __builtin_bit_cast(f32x4, q0) + __builtin_bit_cast(f32x4, q1);
          auto ra = __builtin_amdgcn_permlane32_swap(__float_as_uint(sq.x), __float_as_uint(sq.z), false, false);
          auto rb = __builtin_amdgcn_permlane32_swap(__float_as_uint(sq.y), __float_as_uint(sq.w), false, false);
          const unsigned ra0 = ra[0], ra1 = ra[1], rb0 = rb[0], rb1 = rb[1];
          const float sm0 = __uint_as_float(ra0) + __uint_as_float(ra1), sm1 = __uint_as_float(rb0) + __uint_as_float(rb1);
          const float lse = __uint_as_float(ls);
          const float mx = xmax32(xmax16(fmaxf(sm0, sm1)));
          const float nb = -fmaxf(mx * sc, lse);
          const float e0 = __builtin_amdgcn_exp2f(__builtin_fmaf(sm0, sc, nb)), e1 = __builtin_amdgcn_exp2f(__builtin_fmaf(sm1, sc, nb));
          const float wx = __builtin_amdgcn_exp2f(lse + nb);
          const unsigned pk = pk2(e0, e1);
          const v4u pb = v4u{g == 0 ? pk : 0u, g == 1 ? pk : 0u, g == 2 ? pk : 0u, g == 3 ? pk : 0u};
          const f32x4 dx0 = mfma32(Vf[0], ex, f32x4{0, 0, 0, 0});
          const f32x4 dx1 = mfma32(Vf[1], ex, f32x4{0, 0, 0, 0});
          const f32x4 dx2 = mfma32(Vf[2], ex, f32x4{0, 0, 0, 0});
          const f32x4 dx3 = mfma32(Vf[3], ex, f32x4{0, 0, 0, 0});
          const f32x4 dm = mfma32(Yf, pb, f32x4{0, 0, 0, 0});
          const float inv = __builtin_amdgcn_rcpf(wx + xsum32(xsum16(e0 + e1)));
          const unsigned mxu = umax3(umax3(umax4(q0), umax4(q1), ls), umax4(ex), 0u);
          const f32x4 dxa = g < 2 ? (g == 0 ? dx0 : dx1) : (g == 2 ? dx2 : dx3);
          dres = (dxa * wx + dm) * inv;
          *(float*)(pdst) = dres.x; *(float*)(pdst + 1280) = dres.y; *(float*)(pdst + 2560) = dres.z; *(float*)(pdst + 3840) = dres.w;
          if (__builtin_expect(__all(mxu != SENT), 1)) break;
          if (++spin >= SPIN_MAX) break;
          __builtin_amdgcn_s_sleep(2);
          tiss = rt32();
        }
        *(unsigned*)(smem + L_SCH + 16 + w * 4) = (tiss << 2) | (spin ? 2u : 0u);
        DRAIN();
        STAMP(8);
        BAR();
        STAMPV(11, spin);
        nf += (s > 1 && spin) ? 1 : 0;
        const int sn = s + 1 < NSEG - 1 ? s + 1 : s;
        if ((sn & 1) == 0) ctr_wait(rx, CTR_KV + (sn >> 1) * 128, 56u, 4);
#pragma unroll
        for (int g2 = 0; g2 < 4; ++g2) Vf[g2] = ld_sc1(rx, (int)OFF_VP + (sn * 448 + (4 * w + g2) * 28 + ct) * 1024 + lane * 16);
        if (w == 0 && lane == 0 && rg == 0) arrive(ws, rx, CTR_M + s * 128, 84u);
      }
      DRAIN();
      BAR();
      FAIL_DUMP(nf);
    } else {
      Sched sd = {0u, 0u, 0, 0, 0, 0};
      const unsigned char* pp = smem + L_P1 + lane * 80;
      const bool st = w == 4 && (kg & 1) == 0;
      const int eoff = ct * 256 + rg * 128 + ((((n >> 3) & 1) * 4 + (n & 3)) * 8 + ((n >> 2) & 1) * 4 + kg) * 2;
#pragma nounroll
      for (int s = 0; s < NSEG; ++s) {
        const int o0 = ex_off(s, 0) + goff;
        unsigned char* d0 = smem + L_MEM + (s & 1) * 7168 + goff;
        unsigned char* d1 = w == 7 ? smem + L_DUMMY : d0 + 1024;
        const int ot = (s > 0 && lane < NREP) ? o0 - goff + 7168 + lane * 128 : OOB;
        unsigned word;
        if (s > 1) wait_until(sd.tE2 + T_OG);
        STAMP(0);
        const int spin = gather_poll(rx, d0, d1, o0, w == 7 ? OOB : o0 + 1024, ot, word);
        BAR();
        STAMP(1);
        sched_step(sd, word, lane, s);
        if (w == 4) *(volatile v2u*)(smem + L_SCH) = v2u{sd.tE1, (unsigned)(s + 1)};
        sd.nf += (s > 1 && spin) ? 1 : 0;
        *(unsigned*)(smem + L_SCH + 32 + (w - 4) * 4) = (s > 1 && spin) ? 1u : 0u;
        compute_y(s);
        if (s == NSEG - 1) break;
        const int eon = ex_off(s + 1, 0);
        const int sto = st ? eon + eoff : ((w == 5 && lane == 0 && o_ % (56 / NREP) == 0) ? eon + 7168 + (o_ / (56 / NREP)) * 128 : OOB);
        BAR();
        const v4u gf = *(const v4u*)(smem + L_SCH + 32);
        const unsigned gfl = gf.x | gf.y | gf.z | gf.w;
        const unsigned tref = sd.tE1 << 2;
        KEEP4(sto); KEEP4(gfl);
        DRAIN();
        if (w == 4) {
          BAR();
          STAMP(4);
          const f32x4 a = (*(const f32x4*)(pp) + *(const f32x4*)(pp + 16)) + (*(const f32x4*)(pp + 32) + *(const f32x4*)(pp + 48));
          const float v = (a.x + a.y) + (a.z + a.w);
          auto r = __builtin_amdgcn_permlane16_swap(__float_as_uint(v), __float_as_uint(v), false, false);
          const unsigned r0 = r[0], r1 = r[1];
          __builtin_amdgcn_raw_buffer_store_b32(nosent(pk2(__uint_as_float(r0), __uint_as_float(r1))), rx, sto, 0, AUX_SC1);
        } else {
          BAR();
          const v4u tw = *(const v4u*)(smem + L_SCH + 16);
          const unsigned tm = tref + (unsigned)max(max((int)(tw.x - tref), (int)(tw.y - tref)), max((int)(tw.z - tref), (int)(tw.w - tref)));
          const unsigned wd = nosent(tm | ((tw.x | tw.y | tw.z | tw.w) & 3u) | gfl);
          __builtin_amdgcn_raw_buffer_store_b32(wd, rx, sto, 0, AUX_SC1);
        }
        __builtin_amdgcn_sched_barrier(0);
        STAMP(5);
        STAMPV(10, spin);
      }
      DRAIN();
      BAR();
      if (w == 6 && lane == 0 && rg == 0) arrive(ws, rx, CTR_M + (NSEG - 1) * 128, 84u);
      FAIL_DUMP(w == 7 ? sd.m1 * 1000 + sd.m2 : sd.nf);
    }
  } else {
    startup();
    if (w < 2) {
      const int h = 2 * (cu - N_S - N_O) + w;
      v4u K[2][KS];
      ctr_wait(rx, CTR_KV, 56u, 4);
#pragma unroll
      for (int u = 0; u < 2; ++u)
#pragma unroll
        for (int ks = 0; ks < KS; ++ks) K[u][ks] = ld_sc1(rx, (int)OFF_KP + ((h * 2 + u) * 14 + ks) * 1024 + lane * 16);
#pragma nounroll
      for (int s = 0; s < NSEG - 1; ++s) {
        const int eo1 = ex_off(s, 1), eo3 = ex_off(s, 3);
        const int eb = ((n >> 2) & 1) ? eo3 : eo1;
        const int so = n < 8 ? eb + (h * 4 + (n & 3)) * 64 + kg * 16 : OOB;
        const int lo = lane < 8 ? eb + 4096 + (h * 4 + (n & 3)) * 4 : OOB;
        const unsigned char* lm = smem + L_MEM + (s & 1) * 7168 + lmoff;
#pragma unroll
        for (int ks = 0; ks < KS; ++ks) { KEEP4(K[0][ks]); KEEP4(K[1][ks]); }
        BAR();
        STAMP(2);
        v4u mf[KS];
#pragma unroll
        for (int ks = 0; ks < KS; ++ks) mf[ks] = *(const v4u*)(lm + ks * 512);
        __builtin_amdgcn_sched_barrier(0);
        f32x4 a0 = {0, 0, 0, 0}, a1 = {0, 0, 0, 0};
#pragma unroll
        for (int ks = 0; ks < KS; ++ks) {
          a0 = mfma32(K[0][ks], mf[ks], a0);
          a1 = mfma32(K[1][ks], mf[ks], a1);
        }
        float xs[8] = {rbf(a0.x), rbf(a0.y), rbf(a0.z), rbf(a0.w), rbf(a1.x), rbf(a1.y), rbf(a1.z), rbf(a1.w)};
        float mx = xs[0];
#pragma unroll
        for (int j = 1; j < 8; ++j) mx = fmaxf(mx, xs[j]);
        mx = xmax32(xmax16(mx));
        const float nb = -mx * sc;
        float sum = 0.f;
#pragma unroll
        for (int j = 0; j < 8; ++j) { xs[j] = __builtin_amdgcn_exp2f(__builtin_fmaf(xs[j], sc, nb)); sum += xs[j]; }
        sum = xsum32(xsum16(sum));
        const float inv = __builtin_amdgcn_rcpf(sum);
        v4u o = v4u{pk2(xs[0] * inv, xs[1] * inv), pk2(xs[2] * inv, xs[3] * inv), pk2(xs[4] * inv, xs[5] * inv), pk2(xs[6] * inv, xs[7] * inv)};
        __builtin_amdgcn_raw_buffer_store_b128(nosent4(o), rx, so, 0, AUX_SC1);
        const float lse = __builtin_amdgcn_logf(sum) - nb;
        __builtin_amdgcn_raw_buffer_store_b32(nosent(__float_as_uint(lse)), rx, lo, 0, AUX_SC1);
        __builtin_amdgcn_sched_barrier(0);
        STAMP(5);
        const int sn = s + 1 < NSEG - 1 ? s + 1 : s;
        if ((sn & 1) == 0) ctr_wait(rx, CTR_KV + (sn >> 1) * 128, 56u, 4);
#pragma unroll
        for (int u = 0; u < 2; ++u)
#pragma unroll
          for (int ks = 0; ks < KS; ++ks) K[u][ks] = ld_sc1(rx, (int)OFF_KP + (((sn * 16 + h) * 2 + u) * 14 + ks) * 1024 + lane * 16);
      }
    } else if (w < 4) {
#pragma nounroll
      for (int s = 0; s < NSEG - 1; ++s) BAR();
    } else {
      Sched sd = {0u, 0u, 0, 0, 0, 0};
#pragma nounroll
      for (int s = 0; s < NSEG - 1; ++s) {
        const int o0 = ex_off(s, 0) + goff;
        unsigned char* d0 = smem + L_MEM + (s & 1) * 7168 + goff;
        unsigned char* d1 = w == 7 ? smem + L_DUMMY : d0 + 1024;
        const int ot = (s > 0 && lane < NREP) ? o0 - goff + 7168 + lane * 128 : OOB;
        unsigned word;
        if (s > 1) wait_until(sd.tE2);
        STAMP(0);
        const int spin = gather_poll(rx, d0, d1, o0, w == 7 ? OOB : o0 + 1024, ot, word);
        BAR();
        STAMP(1);
        STAMPV(10, spin);
        sched_step(sd, word, lane, s);
        sd.nf += (s > 1 && spin) ? 1 : 0;
      }
      FAIL_DUMP(sd.nf);
    }
  }
  STAMP_DUMP();
}
DI void phase_kv(const Params& p, unsigned char* lds, rsrc_t rw, int cg, int par) {
  unsigned char* ws = p.ws;
  const int tid = threadIdx.x, lane = tid & 63, w = __builtin_amdgcn_readfirstlane(tid >> 6);
  const int nt0 = cg * 16 + 2 * w;
  v4u st[7];
  {
    const unsigned char* src = ws + OFF_XN + (size_t)par * 57344 + tid * 16;
#pragma unroll
    for (int i = 0; i < 7; ++i) st[i] = *(const v4u*)(src + i * 8192);
  }
  v4u W[2][KS];
#pragma unroll
  for (int q = 0; q < 2; ++q)
#pragma unroll
    for (int ks = 0; ks < KS; ++ks) W[q][ks] = *(const v4u*)(ws + OFF_WC + ((size_t)(nt0 + q) * 14 + ks) * 1024 + lane * 16);
#pragma unroll
  for (int i = 0; i < 7; ++i) *(v4u*)(lds + i * 8192 + tid * 16) = st[i];
#pragma unroll
  for (int ks = 0; ks < KS; ++ks) { KEEP4(W[0][ks]); KEEP4(W[1][ks]); }
  BAR();
  const bool isK = cg < 28;
  const int kbase = (int)OFF_KP + (((nt0 / 28) * 2) * 14 + ((nt0 % 28) >> 1)) * 1024 + lane * 16;
  const int vbase = (int)OFF_VP + (nt0 - 448) * 1024 + lane * 16;
#pragma nounroll
  for (int it = 0; it < 16; ++it) {
    const int hb = par + 2 * it;
    const unsigned char* cur = lds + (it & 1) * 57344 + lane * 16;
    {
      const int so = (int)OFF_XN + (it + 1 < 16 ? hb + 2 : hb) * 57344;
#pragma unroll
      for (int i = 0; i < 7; ++i) st[i] = __builtin_amdgcn_raw_buffer_load_b128(rw, tid * 16, so + i * 8192, 0);
    }
    __builtin_amdgcn_sched_barrier(0);
    f32x4 acc[4][2];
#pragma unroll
    for (int rt = 0; rt < 4; ++rt) { acc[rt][0] = f32x4{0, 0, 0, 0}; acc[rt][1] = f32x4{0, 0, 0, 0}; }
    if (isK) {
#pragma unroll
      for (int ks = 0; ks < KS; ++ks)
#pragma unroll
        for (int rt = 0; rt < 4; ++rt) {
          const v4u a = *(const v4u*)(cur + (rt * 14 + ks) * 1024);
          acc[rt][0] = mfma32(W[0][ks], a, acc[rt][0]);
          acc[rt][1] = mfma32(W[1][ks], a, acc[rt][1]);
        }
#pragma unroll
      for (int rt = 0; rt < 4; ++rt) {
        const int s = hb * 2 + (rt >> 1), u = rt & 1;
        st4_sc1(pk8(acc[rt][0], acc[rt][1]), rw, kbase + (s * 32 + u) * 14336);
      }
    } else {
#pragma unroll
      for (int ks = 0; ks < KS; ++ks)
#pragma unroll
        for (int rt = 0; rt < 4; ++rt) {
          const v4u a = *(const v4u*)(cur + (rt * 14 + ks) * 1024);
          acc[rt][0] = mfma32(a, W[0][ks], acc[rt][0]);
          acc[rt][1] = mfma32(a, W[1][ks], acc[rt][1]);
        }
#pragma unroll
      for (int q = 0; q < 2; ++q)
#pragma unroll
        for (int sg = 0; sg < 2; ++sg) st4_sc1(pk8(acc[2 * sg][q], acc[2 * sg + 1][q]), rw, vbase + ((hb * 2 + sg) * 448 + q) * 1024);
    }
    __builtin_amdgcn_sched_barrier(0);
#pragma unroll
    for (int i = 0; i < 7; ++i) *(v4u*)(lds + ((it + 1) & 1) * 57344 + i * 8192 + tid * 16) = st[i];
    if (it == 0 || it == 15) DRAIN(); else asm volatile("s_waitcnt vmcnt(4)" ::: "memory");
    BAR();
    if (w == 0) TL(200 + it);
    if (tid == (it & 7) * 64) {
      if (it == 0 || it == 15) arrive(ws, rw, CTR_KV + hb * 128, 56u);
      if (it >= 2) arrive(ws, rw, CTR_KV + (hb - 2) * 128, 56u);
    }
  }
}

DI void gate_wait(rsrc_t rw, int off, int nap) {
#pragma nounroll
  for (int spin = 0; spin < SPIN_MAX; ++spin) {
    asm volatile("" ::: "memory");
    const v4u v = ld_sc1(rw, off);
    if (__all(umax4(v) != SENT)) break;
    sleep_units(nap);
  }
}
#define NAP 6

DI void stage_a1(const Params& p, rsrc_t rw, int h) {
  unsigned char* ws = p.ws;
  const int tid = threadIdx.x, lane = tid & 63, w = __builtin_amdgcn_readfirstlane(tid >> 6);
  const int kg = lane >> 4, n = lane & 15, v = w & 1;
  const float sc = 0.0625f * 1.4426950408889634f;
  const float NEG = -3.0e38f;
#pragma nounroll
  for (int rt = w; rt < 128; rt += 8) {
    const int s = rt >> 1;
    v4u xf[KS], kf[KS];
#pragma unroll
    for (int ks = 0; ks < KS; ++ks) xf[ks] = *(const v4u*)(ws + OFF_XN + ((size_t)rt * 14 + ks) * 1024 + lane * 16);
    ctr_wait(rw, CTR_KV + (s >> 1) * 128, 56u, NAP);
    const int kb = (int)OFF_KP + ((s * 16 + h) * 2) * 14336 + lane * 16;
#pragma unroll
    for (int ks = 0; ks < KS; ++ks) kf[ks] = ld_sc1(rw, kb + ks * 1024);
    f32x4 a0 = {0, 0, 0, 0}, a1 = {0, 0, 0, 0}, am = {0, 0, 0, 0};
#pragma unroll
    for (int ks = 0; ks < KS; ++ks) a0 = mfma32(kf[ks], xf[ks], a0);
    if (v) {
#pragma unroll
      for (int ks = 0; ks < KS; ++ks) kf[ks] = ld_sc1(rw, kb + 14336 + ks * 1024);
#pragma unroll
      for (int ks = 0; ks < KS; ++ks) a1 = mfma32(kf[ks], xf[ks], a1);
    }
    float xs[8];
#pragma unroll
    for (int r = 0; r < 4; ++r) {
      const bool diag = 4 * kg + r <= n;
      xs[r] = (v || diag) ? rbf(a0[r]) : NEG;
      xs[4 + r] = (v && diag) ? rbf(a1[r]) : NEG;
    }
    float mx = NEG;
#pragma unroll
    for (int j = 0; j < 8; ++j) mx = fmaxf(mx, xs[j]);
    ctr_wait(rw, CTR_M + s * 128, 84u, NAP);
    const int tb = (int)OFF_KM + (s * 16 + h) * 7168 + kg * 128 + (n & 7) * 16;
#pragma unroll
    for (int ks = 0; ks < KS; ++ks) kf[ks] = ld_sc1(rw, tb + ks * 512);
#pragma unroll
    for (int ks = 0; ks < KS; ++ks) am = mfma32(kf[ks], xf[ks], am);
    float mm[4];
#pragma unroll
    for (int r = 0; r < 4; ++r) { mm[r] = rbf(am[r]); mx = fmaxf(mx, mm[r]); }
    mx = xmax32(xmax16(mx));
    const float nb = -mx * sc;
    float sum = 0.f;
#pragma unroll
    for (int j = 0; j < 8; ++j) { xs[j] = xs[j] == NEG ? 0.f : __builtin_amdgcn_exp2f(__builtin_fmaf(xs[j], sc, nb)); sum += xs[j]; }
#pragma unroll
    for (int r = 0; r < 4; ++r) { mm[r] = __builtin_amdgcn_exp2f(__builtin_fmaf(mm[r], sc, nb)); if (kg < 2) sum += mm[r]; }
    sum = xsum32(xsum16(sum));
    const float inv = 1.f / sum;
    st4_sc1(nosent4(v4u{pk2(xs[0] * inv, xs[1] * inv), pk2(xs[2] * inv, xs[3] * inv), pk2(xs[4] * inv, xs[5] * inv), pk2(xs[6] * inv, xs[7] * inv)}),
            rw, (int)OFF_PX + (rt * 16 + h) * 1024 + lane * 16);
    st2_sc1(nosent2(v2u{pk2(mm[0] * inv, mm[1] * inv), pk2(mm[2] * inv, mm[3] * inv)}), rw,
            kg < 2 ? (int)OFF_PM + (rt * 4 + (h >> 2)) * 1024 + ((h & 3) * 16 + n) * 16 + kg * 8 : OOB);
    TL(rt);
  }
}

DI void stage_a2(const Params& p, rsrc_t rw, int ct) {
  const int tid = threadIdx.x, lane = tid & 63, w = __builtin_amdgcn_readfirstlane(tid >> 6);
  const int kg = lane >> 4, n = lane & 15;
  const int c = 2 * ct + (kg >> 1);
  const int col = 32 * (c >> 2) + 16 * (kg & 1) + 4 * (c & 3);
  const int hoff = (c >> 2) * 1024 + ((c & 3) * 16 + n) * 16 + (kg & 1) * 8;
#pragma nounroll
  for (int rt = w; rt < 128; rt += 8) {
    const int s = rt >> 1;
    const v2u xv = *(const v2u*)(p.x + (size_t)(rt * 16 + n) * 448 + col);
    ctr_wait(rw, CTR_KV + (s >> 1) * 128, 56u, NAP);
    v4u vf[16], yf[4];
#pragma unroll
    for (int h = 0; h < 16; ++h) vf[h] = ld_sc1(rw, (int)OFF_VP + (s * 448 + h * 28 + ct) * 1024 + lane * 16);
    ctr_wait(rw, CTR_M + s * 128, 84u, NAP);
#pragma unroll
    for (int q = 0; q < 4; ++q) yf[q] = ld_sc1(rw, (int)OFF_VM + ((s * 16 + 4 * q + kg) * 28 + ct) * 256 + n * 16);
    const int pb = (int)OFF_PX + rt * 16384 + lane * 16, mb = (int)OFF_PM + rt * 4096 + lane * 16;
    gate_wait(rw, pb + (ct & 15) * 1024, NAP);
    f32x4 acc;
#pragma nounroll
    for (int spin = 0; spin < SPIN_MAX; ++spin) {
      asm volatile("" ::: "memory");
      v4u px[16], pm[4];
#pragma unroll
      for (int h = 0; h < 16; ++h) px[h] = ld_sc1(rw, pb + h * 1024);
#pragma unroll
      for (int q = 0; q < 4; ++q) pm[q] = ld_sc1(rw, mb + q * 1024);
      unsigned mxu = 0;
      f32x4 a0 = {0, 0, 0, 0}, a1 = {0, 0, 0, 0};
#pragma unroll
      for (int h = 0; h < 16; h += 2) {
        mxu = umax3(mxu, umax4(px[h]), umax4(px[h + 1]));
        a0 = mfma32(vf[h], px[h], a0); a1 = mfma32(vf[h + 1], px[h + 1], a1);
      }
#pragma unroll
      for (int q = 0; q < 4; q += 2) {
        mxu = umax3(mxu, umax4(pm[q]), umax4(pm[q + 1]));
        a0 = mfma32(yf[q], pm[q], a0); a1 = mfma32(yf[q + 1], pm[q + 1], a1);
      }
      acc = a0 + a1;
      if (__all(mxu != SENT)) break;
      sleep_units(2);
    }
    const float h0 = bflo(xv.x) + rbf(acc.x), h1 = bfhi(xv.x) + rbf(acc.y), h2 = bflo(xv.y) + rbf(acc.z), h3 = bfhi(xv.y) + rbf(acc.w);
    st2_sc1(nosent2(v2u{pk2(h0, h1), pk2(h2, h3)}), rw, (int)OFF_HF + rt * 14336 + hoff);
    TL(rt);
  }
}

DI void stage_f1(const Params& p, unsigned char* lds, rsrc_t rw, int kf) {
  const int tid = threadIdx.x, lane = tid & 63, w = __builtin_amdgcn_readfirstlane(tid >> 6);
  const int kg = lane >> 4, n = lane & 15;
  for (int f = w; f < 56; f += 8) {
    const int t = f / 14, ks = f % 14;
    const u16* src = ((t >> 1) ? p.wu : p.wg) + 32 * kf + 16 * (t & 1) + n;
    unsigned e[8];
#pragma unroll
    for (int jj = 0; jj < 8; ++jj) e[jj] = src[(size_t)pidx(ks, kg, jj) * 1792];
    *(v4u*)(lds + f * 1024 + lane * 16) = v4u{e[0] | (e[1] << 16), e[2] | (e[3] << 16), e[4] | (e[5] << 16), e[6] | (e[7] << 16)};
  }
  BAR();
  const v4u ones = {0x3F803F80u, 0x3F803F80u, 0x3F803F80u, 0x3F803F80u};
  f32x4 cs[4];
#pragma unroll
  for (int t = 0; t < 4; ++t) {
    cs[t] = f32x4{0, 0, 0, 0};
#pragma unroll
    for (int ks = 0; ks < KS; ++ks) cs[t] = mfma32(*(const v4u*)(lds + (t * 14 + ks) * 1024 + lane * 16), ones, cs[t]);
  }
#pragma nounroll
  for (int rt = w; rt < 128; rt += 8) {
    const int hb = (int)OFF_HF + rt * 14336 + lane * 16;
    gate_wait(rw, hb + (kf % 14) * 1024, NAP);
    v4u hf[KS];
#pragma nounroll
    for (int spin = 0; spin < SPIN_MAX; ++spin) {
      asm volatile("" ::: "memory");
      unsigned mxu = 0;
#pragma unroll
      for (int ks = 0; ks < KS; ++ks) hf[ks] = ld_sc1(rw, hb + ks * 1024);
#pragma unroll
      for (int ks = 0; ks < KS; ++ks) mxu = max(mxu, umax4(hf[ks]));
      if (__all(mxu != SENT)) break;
      sleep_units(2);
    }
    f32x4 acc[4], sm = {0, 0, 0, 0}, gr = {0, 0, 0, 0};
#pragma unroll
    for (int t = 0; t < 4; ++t) acc[t] = f32x4{0, 0, 0, 0};
#pragma unroll
    for (int ks = 0; ks < KS; ++ks) {
      sm = mfma32(ones, hf[ks], sm);
      gr = mfma32(hf[ks], hf[ks], gr);
#pragma unroll
      for (int t = 0; t < 4; ++t) acc[t] = mfma32(*(const v4u*)(lds + (t * 14 + ks) * 1024 + lane * 16), hf[ks], acc[t]);
    }
    float dg = (kg == (n >> 2)) ? ((n & 2) ? ((n & 1) ? gr.w : gr.z) : ((n & 1) ? gr.y : gr.x)) : 0.f;
    dg = xsum32(xsum16(dg));
    const float mean = sm.x * (1.f / 448.f);
    const float var = fmaxf(dg * (1.f / 448.f) - mean * mean, 0.f);
    const float mb = rbf(mean), rden = 1.f / ln_den(var);
    f32x4 o0, o1;
#pragma unroll
    for (int r = 0; r < 4; ++r) {
      o0[r] = rbf((acc[0][r] - mb * cs[0][r]) * rden) * rbf((acc[2][r] - mb * cs[2][r]) * rden);
      o1[r] = rbf((acc[1][r] - mb * cs[1][r]) * rden) * rbf((acc[3][r] - mb * cs[3][r]) * rden);
    }
    st4_sc1(nosent4(pk8(o0, o1)), rw, (int)OFF_AF + (rt * 56 + kf) * 1024 + lane * 16);
    TL(rt);
  }
}

DI void stage_f2(const Params& p, unsigned char* lds, rsrc_t rw, int ct) {
  const int tid = threadIdx.x, lane = tid & 63, w = __builtin_amdgcn_readfirstlane(tid >> 6);
  const int kg = lane >> 4, n = lane & 15;
  for (int kf = w; kf < 56; kf += 8) {
    const u16* src = p.wd + 16 * ct + n;
    unsigned e[8];
#pragma unroll
    for (int jj = 0; jj < 8; ++jj) e[jj] = src[(size_t)pidx(kf, kg, jj) * 448];
    *(v4u*)(lds + kf * 1024 + lane * 16) = v4u{e[0] | (e[1] << 16), e[2] | (e[3] << 16), e[4] | (e[5] << 16), e[6] | (e[7] << 16)};
  }
  BAR();
#pragma nounroll
  for (int rt = w; rt < 128; rt += 8) {
    const int ab = (int)OFF_AF + rt * 57344 + lane * 16;
    gate_wait(rw, ab + (2 * ct) * 1024, NAP);
    v2u hv = v2u{0, 0};
    f32x4 a0 = {0, 0, 0, 0}, a1 = {0, 0, 0, 0};
#pragma nounroll
    for (int b = 0; b < 2; ++b) {
      v4u af[28];
#pragma nounroll
      for (int spin = 0; spin < SPIN_MAX; ++spin) {
        asm volatile("" ::: "memory");
        hv = __builtin_amdgcn_raw_buffer_load_b64(rw, (int)OFF_HF + rt * 14336 + (ct >> 1) * 1024 + lane * 16 + (ct & 1) * 8, 0, AUX_SC1);
#pragma unroll
        for (int k = 0; k < 28; ++k) af[k] = ld_sc1(rw, ab + (b * 28 + k) * 1024);
        unsigned mxu = max(hv.x, hv.y);
#pragma unroll
        for (int k = 0; k < 28; ++k) mxu = max(mxu, umax4(af[k]));
        if (__all(mxu != SENT)) break;
        sleep_units(2);
      }
#pragma unroll
      for (int k = 0; k < 28; k += 2) {
        a0 = mfma32(*(const v4u*)(lds + (b * 28 + k) * 1024 + lane * 16), af[k], a0);
        a1 = mfma32(*(const v4u*)(lds + (b * 28 + k + 1) * 1024 + lane * 16), af[k + 1], a1);
      }
    }
    const f32x4 d = a0 + a1;
    const float o0 = bflo(hv.x) + rbf(d.x), o1 = bfhi(hv.x) + rbf(d.y), o2 = bflo(hv.y) + rbf(d.z), o3 = bfhi(hv.y) + rbf(d.w);
    *(v2u*)(p.out + (size_t)(rt * 16 + n) * 448 + 16 * ct + 4 * kg) = v2u{pk2(o0, o1), pk2(o2, o3)};
    TL(rt);
  }
}

DI void arm_stage(const Params& p, rsrc_t rw, int k, int nwg, int off0, int nkb, int ctr) {
  const int tid = threadIdx.x, lane = tid & 63, w = __builtin_amdgcn_readfirstlane(tid >> 6);
  const v4u sent = {SENT, SENT, SENT, SENT};
  for (int i = k * 8 + w; i < nkb; i += nwg * 8) st4_sc1(sent, rw, off0 + i * 1024 + lane * 16);
  DRAIN();
  __syncthreads();
  if (tid == 0) arrive(p.ws, rw, ctr, (unsigned)nwg);
}

constexpr int MAIN_LDS = 114688;
__global__ void __launch_bounds__(512, 2) k_main(Params p) {
  extern __shared__ __attribute__((aligned(16))) unsigned char smem[];
  const rsrc_t rw = __builtin_amdgcn_make_buffer_rsrc((void*)p.ws, 0, (int)WS_END, 0x00020000);
  const int b = blockIdx.x;
  if (threadIdx.x < 64) TL(255);
  const int j = (b >> 3) * 4 + (b & 3);
  if ((b & 7) < 4) {
    if (j < N_CHAIN) chain_role(p, smem, rw, j);
    else { if (j == N_CHAIN) probe_place(p, rw); sleep_units(400); arm_stage(p, rw, j - N_CHAIN, 8, (int)OFF_HF, (int)((OFF_XN - OFF_HF) / 1024), CTR_FS + 128); }
    return;
  }
  if (j < 112) phase_kv(p, smem, rw, j % 56, j / 56);
  else arm_stage(p, rw, j - 112, 16, (int)OFF_PX, (int)((OFF_HF - OFF_PX) / 1024), CTR_FS);
  ctr_wait(rw, CTR_FS, 16u, 8);
  if (j < 112) ctr_wait(rw, CTR_FS + 128, 8u, 8);
  if (j < 56) stage_f1(p, smem, rw, j);
  else if (j < 84) stage_f2(p, smem, rw, j - 56);
  else if (j < 112) stage_a2(p, rw, j - 84);
  else stage_a1(p, rw, j - 112);
}

extern "C" void kernel_launch(void* const* d_in, const int* in_sizes, int n_in, void* d_out, int out_size, void* d_ws, size_t ws_size,
                              hipStream_t stream) {
  Params p;
  p.x = (const u16*)d_in[0]; p.mem0 = (const u16*)d_in[1]; p.Wq = (const u16*)d_in[2]; p.Wk = (const u16*)d_in[3];
  p.Wv = (const u16*)d_in[4]; p.Wo = (const u16*)d_in[5];
  p.wg = (const u16*)d_in[8]; p.wu = (const u16*)d_in[9]; p.wd = (const u16*)d_in[10];
  p.out = (u16*)d_out; p.ws = (unsigned char*)d_ws;
  (void)hipFuncSetAttribute((const void*)k_main, hipFuncAttributeMaxDynamicSharedMemorySize, MAIN_LDS);
  (void)hipFuncSetAttribute((const void*)k_prep, hipFuncAttributeMaxDynamicSharedMemorySize, PREP_LDS);
  k_prep<<<512, 512, PREP_LDS, stream>>>(p);
  k_main<<<256, 512, MAIN_LDS, stream>>>(p);
}
```

```cpp
#include <hip/hip_runtime.h>
#include <hip/hip_bf16.h>
#include <stdint.h>

typedef short bf16x8 __attribute__((ext_vector_type(8)));
typedef float f32x4 __attribute__((ext_vector_type(4)));
typedef float f32x2 __attribute__((ext_vector_type(2)));
typedef __bf16 bf2_t __attribute__((ext_vector_type(2)));
typedef unsigned v4u __attribute__((ext_vector_type(4)));
typedef unsigned v2u __attribute__((ext_vector_type(2)));
typedef unsigned short u16;
typedef __amdgpu_buffer_rsrc_t rsrc_t;
#define DI __device__ __forceinline__

constexpr int NSEG = 64, KS = 14;
#define S_FAST 44
constexpr unsigned SENT = 0xFFFFFFFFu;
constexpr int AUX_SC1 = 16;
constexpr int SPIN_MAX = 20000;
constexpr int OOB = 0x7FFFFFF0;

constexpr size_t OFF_CTR = 0;
constexpr int CTR_KV = 0, CTR_M = 4096, OFF_PTAB = 16384, CTR_FX = 16640, CTR_FS = 16896, OFF_TT = 20480;
constexpr size_t OFF_EX = 65536;
constexpr size_t EX_STEP = 5 * 16384;
constexpr size_t OFF_PX = OFF_EX + 64 * EX_STEP;
constexpr size_t OFF_PM = OFF_PX + (size_t)128 * 16 * 1024;
constexpr size_t OFF_HF = OFF_PM + (size_t)128 * 4 * 1024;
constexpr size_t OFF_AF = OFF_HF + (size_t)128 * 14 * 1024;
constexpr size_t OFF_XN = OFF_AF + (size_t)128 * 56 * 1024;
constexpr size_t OFF_WC = OFF_XN + (size_t)128 * 14 * 1024;
constexpr size_t OFF_KP = OFF_WC + (size_t)896 * 14 * 1024;
constexpr size_t OFF_VP = OFF_KP + (size_t)64 * 16 * 2 * 14 * 1024;
constexpr size_t OFF_KM = OFF_VP + (size_t)64 * 448 * 1024;
constexpr size_t OFF_VM = OFF_KM + (size_t)64 * 16 * 14 * 512;
constexpr size_t WS_END = OFF_VM + (size_t)64 * 16 * 28 * 256;
constexpr size_t FILL_BYTES = OFF_XN - OFF_EX;
static_assert(WS_END < (size_t)200 * 1024 * 1024, "ws");
static_assert(FILL_BYTES % 16384 == 0, "fill");

DI f32x4 mfma32(v4u a, v4u b, f32x4 c) {
  return __builtin_amdgcn_mfma_f32_16x16x32_bf16(__builtin_bit_cast(bf16x8, a), __builtin_bit_cast(bf16x8, b), c, 0, 0, 0);
}
DI unsigned pk2(float a, float b) {
  f32x2 v = {a, b};
  return __builtin_bit_cast(unsigned, __builtin_convertvector(v, bf2_t));
}
DI v2u pk4(f32x4 a) { return v2u{pk2(a.x, a.y), pk2(a.z, a.w)}; }
DI v4u pk8(f32x4 a, f32x4 b) { return v4u{pk2(a.x, a.y), pk2(a.z, a.w), pk2(b.x, b.y), pk2(b.z, b.w)}; }
DI float bflo(unsigned u) { return __uint_as_float(u << 16); }
DI float bfhi(unsigned u) { return __uint_as_float(u & 0xFFFF0000u); }
DI float rbf(float a) { return bflo(pk2(a, 0.f) & 0xFFFFu); }
DI int picol(int c, int cc) { return 32 * (c >> 2) + 16 * (cc >> 2) + 4 * (c & 3) + (cc & 3); }
DI int pidx(int ks, int kg, int jj) { return 32 * ks + 16 * (jj >> 2) + 4 * kg + (jj & 3); }
DI float xsum16(float v) { auto r = __builtin_amdgcn_permlane16_swap(__float_as_uint(v), __float_as_uint(v), false, false);
  const unsigned a = r[0], b = r[1]; return __uint_as_float(a) + __uint_as_float(b); }
DI float xsum32(float v) { auto r = __builtin_amdgcn_permlane32_swap(__float_as_uint(v), __float_as_uint(v), false, false);
  const unsigned a = r[0], b = r[1]; return __uint_as_float(a) + __uint_as_float(b); }
DI float xmax16(float v) { auto r = __builtin_amdgcn_permlane16_swap(__float_as_uint(v), __float_as_uint(v), false, false);
  const unsigned a = r[0], b = r[1]; return fmaxf(__uint_as_float(a), __uint_as_float(b)); }
DI float xmax32(float v) { auto r = __builtin_amdgcn_permlane32_swap(__float_as_uint(v), __float_as_uint(v), false, false);
  const unsigned a = r[0], b = r[1]; return fmaxf(__uint_as_float(a), __uint_as_float(b)); }
DI unsigned nosent(unsigned v) { return min(v, 0xFFFFFFFEu); }
DI v2u nosent2(v2u o) { return v2u{nosent(o.x), nosent(o.y)}; }
DI v4u nosent4(v4u o) { return v4u{nosent(o.x), nosent(o.y), nosent(o.z), nosent(o.w)}; }
DI unsigned umax3(unsigned a, unsigned b, unsigned c) { return max(max(a, b), c); }
DI unsigned umax4(v4u r) { return max(umax3(r.x, r.y, r.z), r.w); }
#define KEEP4(x) asm volatile("" :: "v"(x))
#define BAR() do { asm volatile("s_waitcnt lgkmcnt(0)" ::: "memory"); __builtin_amdgcn_s_barrier(); asm volatile("" ::: "memory"); } while (0)
#define DRAIN() asm volatile("s_waitcnt vmcnt(0)" ::: "memory")
DI void sleep_units(int n) { for (int d = 0; d < n; ++d) __builtin_amdgcn_s_sleep(1); }
DI v4u ld_sc1(rsrc_t r, int off) { return __builtin_amdgcn_raw_buffer_load_b128(r, off, 0, AUX_SC1); }
DI unsigned ld1_sc1(rsrc_t r, int off) { return __builtin_amdgcn_raw_buffer_load_b32(r, off, 0, AUX_SC1); }
DI void st4_sc1(v4u v, rsrc_t r, int off) { __builtin_amdgcn_raw_buffer_store_b128(v, r, off, 0, AUX_SC1); }
DI void st2_sc1(v2u v, rsrc_t r, int off) { __builtin_amdgcn_raw_buffer_store_b64(v, r, off, 0, AUX_SC1); }
DI void st1_sc1(unsigned v, rsrc_t r, int off) { __builtin_amdgcn_raw_buffer_store_b32(v, r, off, 0, AUX_SC1); }
constexpr int FLAG_D = 32768;
DI unsigned arrive_issue(unsigned char* ws, int off) { return __hip_atomic_fetch_add((unsigned*)(ws + off), 1u, __ATOMIC_RELAXED, __HIP_MEMORY_SCOPE_AGENT); }
DI void arrive_finish(rsrc_t r, unsigned old, int off, unsigned total) { if (old == total - 1u) st1_sc1(1u, r, off + FLAG_D); }
DI void arrive(unsigned char* ws, rsrc_t r, int off, unsigned total) { arrive_finish(r, arrive_issue(ws, off), off, total); }
DI void ctr_wait(rsrc_t r, int off, unsigned want, int nap) {
  off += FLAG_D; want = 1u;
#pragma nounroll
  for (int spin = 0; spin < SPIN_MAX; ++spin) {
    asm volatile("" ::: "memory");
    const unsigned v = __builtin_amdgcn_readfirstlane(ld1_sc1(r, off));
    if (v >= want) break;
    sleep_units(nap);
  }
  asm volatile("" ::: "memory");
}

DI void kv_ensure(rsrc_t r, int hb, int& known, int nap) {
  if (hb < known) return;
  const int lane = threadIdx.x & 63;
#pragma nounroll
  for (int spin = 0; spin < SPIN_MAX; ++spin) {
    asm volatile("" ::: "memory");
    const unsigned v = ld1_sc1(r, lane < 32 ? CTR_KV + FLAG_D + lane * 128 : OOB);
    const unsigned long long m = __ballot(v != 0u);
    known = __builtin_ctzll(~m);
    if (hb < known) break;
    sleep_units(nap);
  }
  asm volatile("" ::: "memory");
}

DI unsigned rt32() { unsigned long long t; asm volatile("s_memrealtime %0\n\ts_waitcnt lgkmcnt(0)" : "=s"(t) :: "memory"); return (unsigned)t; }
DI unsigned wait_until(unsigned t) {
  int d = (int)(t - rt32());
  if (d < -2) return t - d;
  if (d > 3000) d = 3000;
  if (d > 24) { sleep_units(((d - 16) * 3) >> 3); d = (int)(t - rt32()); }
  if (d > 0 && d < 64) sleep_units((d * 3) >> 3);
  return t;
}

struct Params {
  const u16 *x, *mem0, *Wq, *Wk, *Wv, *Wo, *wg, *wu, *wd;
  u16* out;
  unsigned char* ws;
};

DI float ln_den(float var) { return rbf(sqrtf(rbf(rbf(var) + 1e-5f))); }

#define TL(idx)
constexpr int PREP_LDS = 288 * 528;
constexpr int FILL_KB = (int)(FILL_BYTES / 1024);
__global__ void __launch_bounds__(512, 2) k_prep(Params p) {
  extern __shared__ __attribute__((aligned(16))) unsigned char lds[];
  const int tid = threadIdx.x, lane = tid & 63, kg = lane >> 4, n = lane & 15;
  const int w = __builtin_amdgcn_readfirstlane(tid >> 6);
  unsigned char* ws = p.ws;
#define TLP(idx)
  const int xg = blockIdx.x & 7, ix = blockIdx.x >> 3;
  const int b = ix < 28 ? (2 * xg + ix / 14) * 14 + ix % 14 : 224 + xg * 4 + (ix - 28), bb = b;
  TLP(0);
  const v4u sent = {SENT, SENT, SENT, SENT};
  if (bb < 224) {
    const int h = bb / 14, r = bb % 14, ab = r / 7, jb = r % 7;
    v4u sa[2][14], sb[2][4];
#pragma unroll
    for (int half = 0; half < 2; ++half) {
      const u16* asrc = (half ? p.Wk : p.Wv) + (size_t)(224 * ab) * 4096 + h * 256;
      if (half) {
        const u16* bsrc = p.Wq + (size_t)(64 * jb) * 4096 + h * 256;
#pragma unroll
        for (int i = 0; i < 4; ++i) { const int pc = i * 512 + tid; sb[1][i] = *(const v4u*)(bsrc + (size_t)(pc >> 5) * 4096 + (pc & 31) * 8); }
      } else {
        const u16* bsrc = p.Wo + (size_t)(h * 256) * 448 + 64 * jb;
#pragma unroll
        for (int i = 0; i < 4; ++i) { const int pc = i * 512 + tid; sb[0][i] = *(const v4u*)(bsrc + (size_t)(pc >> 3) * 448 + (pc & 7) * 8); }
      }
#pragma unroll
      for (int i = 0; i < 14; ++i) { const int pc = i * 512 + tid; sa[half][i] = *(const v4u*)(asrc + (size_t)(pc >> 5) * 4096 + (pc & 31) * 8); }
    }
    __builtin_amdgcn_sched_barrier(0);
    unsigned char* lb = lds + 224 * 528;
#pragma unroll
    for (int half = 0; half < 2; ++half) {
      if (half) {
        __syncthreads();
#pragma unroll
        for (int i = 0; i < 4; ++i) { const int pc = i * 512 + tid; *(v4u*)(lb + (pc >> 5) * 528 + (pc & 31) * 16) = sb[1][i]; }
      } else {
#pragma unroll
        for (int i = 0; i < 4; ++i) {
          const int pc = i * 512 + tid, e = pc >> 3, x0 = (pc & 7) * 8;
#pragma unroll
          for (int j = 0; j < 8; ++j) {
            const int x = x0 + j, m = x >> 5, xl = x & 31, cl = (xl >> 2) & 3;
            const int slot = (2 * m + (cl >> 1)) * 16 + (cl & 1) * 8 + (xl >> 4) * 4 + (xl & 3);
            const unsigned wv = sb[0][i][j >> 1];
            *(u16*)(lb + slot * 528 + e * 2) = (u16)((j & 1) ? (wv >> 16) : (wv & 0xFFFFu));
          }
        }
      }
#pragma unroll
      for (int i = 0; i < 14; ++i) { const int pc = i * 512 + tid; *(v4u*)(lds + (pc >> 5) * 528 + (pc & 31) * 16) = sa[half][i]; }
      __syncthreads();
      TLP(1 + 2 * half);
      if (w < 7) {
        const int ks = ab * 7 + w;
        const unsigned char* pa = lds + (32 * w + n) * 528 + kg * 16;
        const unsigned char* pb = lb + n * 528 + kg * 16;
        f32x4 acc[4][2];
#pragma unroll
        for (int t = 0; t < 4; ++t) { acc[t][0] = f32x4{0, 0, 0, 0}; acc[t][1] = f32x4{0, 0, 0, 0}; }
#pragma unroll
        for (int ke = 0; ke < 8; ++ke) {
          const v4u A0 = *(const v4u*)(pa + ke * 64), A1 = *(const v4u*)(pa + 16 * 528 + ke * 64);
#pragma unroll
          for (int t = 0; t < 4; ++t) {
            const v4u B = *(const v4u*)(pb + t * 16 * 528 + ke * 64);
            acc[t][0] = mfma32(A0, B, acc[t][0]); acc[t][1] = mfma32(A1, B, acc[t][1]);
          }
        }
#pragma unroll
        for (int t = 0; t < 4; ++t) {
          const int tile = (half ? 0 : 448) + h * 28 + jb * 4 + t;
          *(v4u*)(ws + OFF_WC + ((size_t)tile * 14 + ks) * 1024 + lane * 16) = pk8(acc[t][0], acc[t][1]);
        }
      }
      TLP(2 + 2 * half);
    }
    return;
  }
#pragma unroll
  for (int half = 0; half < 2; ++half)
  {
    const int row0 = half * 1024 + (bb - 224) * 32 + w * 4;
    const int c = lane < 56 ? lane : 0;
    const int ks = c >> 2, kc = c & 3;
    v2u a4[4], b4[4];
#pragma unroll
    for (int rr = 0; rr < 4; ++rr) {
      a4[rr] = *(const v2u*)(p.x + (size_t)(row0 + rr) * 448 + 32 * ks + 4 * kc);
      b4[rr] = *(const v2u*)(p.x + (size_t)(row0 + rr) * 448 + 32 * ks + 16 + 4 * kc);
    }
#pragma unroll
    for (int rr = 0; rr < 4; ++rr) {
      const int row = row0 + rr;
      const v2u a = a4[rr], b2 = b4[rr];
      float v[8] = {bflo(a.x), bfhi(a.x), bflo(a.y), bfhi(a.y), bflo(b2.x), bfhi(b2.x), bflo(b2.y), bfhi(b2.y)};
      float s = 0.f;
#pragma unroll
      for (int j = 0; j < 8; ++j) s += v[j];
      if (lane >= 56) s = 0.f;
#pragma unroll
      for (int o = 1; o < 64; o <<= 1) s += __shfl_xor(s, o);
      const float mean = s * (1.f / 448.f);
      float q = 0.f;
#pragma unroll
      for (int j = 0; j < 8; ++j) { float d = v[j] - mean; q += d * d; }
      if (lane >= 56) q = 0.f;
#pragma unroll
      for (int o = 1; o < 64; o <<= 1) q += __shfl_xor(q, o);
      const float den = ln_den(q * (1.f / 448.f)), mb = rbf(mean);
      float o8[8];
#pragma unroll
      for (int j = 0; j < 8; ++j) o8[j] = rbf(v[j] - mb) / den;
      if (lane < 56)
        *(v4u*)(ws + OFF_XN + ((size_t)(row >> 4) * 14 + ks) * 1024 + (kc * 16 + (row & 15)) * 16) =
            v4u{pk2(o8[0], o8[1]), pk2(o8[2], o8[3]), pk2(o8[4], o8[5]), pk2(o8[6], o8[7])};
    }
  }
  TLP(4);
  if (b == 224) {
    if (w < 7) {
      const int pc = w * 64 + lane;
      const int c = (pc >> 4) * 2 + ((pc >> 2) & 1), row = ((pc >> 3) & 1) * 4 + (pc & 3);
      const int ks = c >> 2, kc = c & 3;
      v2u a = *(const v2u*)(p.mem0 + (size_t)row * 448 + 32 * ks + 4 * kc);
      v2u b2 = *(const v2u*)(p.mem0 + (size_t)row * 448 + 32 * ks + 16 + 4 * kc);
      const v4u v = v4u{nosent(a.x), nosent(a.y), nosent(b2.x), nosent(b2.y)};
      *(v4u*)(ws + OFF_EX + pc * 16) = v;
      *(v4u*)(ws + OFF_EX + 8192 + pc * 16) = v;
    } else {
      *(v4u*)(ws + OFF_EX + 7168 + lane * 16) = sent;
      *(v4u*)(ws + OFF_EX + 8192 + 7168 + lane * 16) = sent;
    }
  }
  if (b == 225) {
#pragma unroll
    for (int i = 0; i < 8; ++i) *(v4u*)(ws + OFF_CTR + (size_t)(i * 512 + tid) * 16) = v4u{0, 0, 0, 0};
  }
}
#define STAMP(k)
#define STAMPV(k, v)
#define STAMP_INIT()
#define STAMP_DUMP()
constexpr int L_MEM = 0;
constexpr int L_P1 = 16384;
constexpr int L_YB = 24576;
constexpr int N_S = 56, N_O = 56, N_X = 8, N_CHAIN = N_S + N_O + N_X;
constexpr int L_TAB2 = 22528;
constexpr int L_VOTE = 23808;
#define L_DUMMY (14336 + lane * 16)
#define ex_off(s, c) __builtin_amdgcn_readfirstlane(*(const int*)(smem + L_TAB2 + ((s) * 5 + (c)) * 4))
#define T_A 110
#define T_B 102
#define T_SW 26
#define T_DA 0
#define T_DB 0
#define T_R2 8
#define T_R1 0
#define T_Q1 15
#define T_Q2 70
#define PFD 4
#define T_EX 30
#define T_EXN 4
#define NREP 4
#define T_OG 0
#define T_PEN 35
#define T_UP 2
#define T_OKN 1000
constexpr int L_SCH = 23872;
struct Sched { unsigned tE1, tE2; int m1, m2, ok, nf, late; };
DI void sched_step(Sched& y, unsigned word, int lane, int s) {
  if (s == 0) { y.tE1 = rt32(); y.tE2 = y.tE1; return; }
  int dv = lane < NREP ? (int)((word & ~3u) - (y.tE1 << 2)) >> 2 : -0x40000000;
  unsigned fl = lane < NREP ? (word & 3u) : 0u;
  if (s > 1 && __all(lane >= NREP || word == (y.tE1 << 2))) { dv = 0; fl = 0; }
  else {
#pragma unroll
    for (int o = 1; o < NREP; o <<= 1) { dv = max(dv, __shfl_xor(dv, o)); fl |= (unsigned)__shfl_xor((int)fl, o); }
    dv = __builtin_amdgcn_readfirstlane(dv); fl = __builtin_amdgcn_readfirstlane(fl);
  }
  unsigned base = y.tE2;
  if (dv > 0 || s == 1) base = y.tE1 + dv + T_B + y.m2 + T_PEN;
  if (s > 2 && !y.late) {
    if (fl & 1u) { y.m2 += T_UP; y.ok = 0; }
    else if (fl & 2u) { y.m1 += T_UP; y.ok = 0; }
    else if (++y.ok >= T_OKN) { y.ok = 0; y.m1 -= 1; y.m2 -= 1; }
  }
  y.late = dv > 0 ? 1 : 0;
  if (s == T_SW) { y.m1 -= T_DA; y.m2 -= T_DB; }
  const int ex = s < T_EXN ? T_EX : 0;
  y.tE1 = base + T_A + y.m1 + ex;
  y.tE2 = y.tE1 + T_B + y.m2 + (ex >> 1);
}
#define FAIL_DUMP(v)

DI int gather_poll(__amdgpu_buffer_rsrc_t rx, unsigned char* dst0, unsigned char* dst1, int o0, int o1, int ot, unsigned& word) {
  int spin = 0;
#pragma nounroll
  for (;;) {
    asm volatile("" ::: "memory");
    const v4u v0 = ld_sc1(rx, o0), v1 = ld_sc1(rx, o1);
    word = __builtin_amdgcn_raw_buffer_load_b32(rx, ot, 0, AUX_SC1);
    *(v4u*)dst0 = v0;
    *(v4u*)dst1 = v1;
    if (__builtin_expect(__all(umax3(umax4(v0), umax4(v1), word) != SENT), 1)) break;
    if (spin == 0) { o0 ^= 8192; o1 ^= 8192; ot ^= 8192; sleep_units(T_R2); }
    if (++spin >= SPIN_MAX) break;
    __builtin_amdgcn_s_sleep(2);
  }
  return spin;
}

DI void probe_place(const Params& p, const rsrc_t rx) {
  unsigned char* ws = p.ws;
  const int lane = threadIdx.x & 63, w = __builtin_amdgcn_readfirstlane(threadIdx.x >> 6);
  const unsigned a0 = (unsigned)(size_t)(ws + OFF_EX);
    if (w >= 4) {
      const int r = w - 4;
      int pr = -1;
      for (int k = 0; k < 64; ++k) if (pr < 0 && (int)(((a0 + (unsigned)k * 81920u) >> 21) - (a0 >> 21)) == r) pr = k * 5;
      unsigned res = 2;
      if (pr >= 0) {
        const int base = (int)OFF_EX + pr * 16384 + 7168;
        const v4u sent = {SENT, SENT, SENT, SENT};
        __builtin_amdgcn_raw_buffer_store_b128(sent, rx, base + lane * 16, 0, AUX_SC1);
        __builtin_amdgcn_raw_buffer_store_b128(sent, rx, base + 8192 + lane * 16, 0, AUX_SC1);
        asm volatile("s_waitcnt vmcnt(0)" ::: "memory");
        unsigned best[2] = {0xFFFFFFFFu, 0xFFFFFFFFu};
#pragma unroll
        for (int k = 0; k < 8; ++k) {
          unsigned long long t0, t1;
          asm volatile("s_memtime %0\n\ts_waitcnt lgkmcnt(0)" : "=s"(t0) :: "memory");
          unsigned v = __builtin_amdgcn_raw_buffer_load_b32(rx, base + (k & 1) * 8192 + (k >> 1) * 128, 0, AUX_SC1);
          asm volatile("s_waitcnt vmcnt(0)" :: "v"(v) : "memory");
          asm volatile("s_memtime %0\n\ts_waitcnt lgkmcnt(0)" : "=s"(t1) :: "memory");
          best[k & 1] = min(best[k & 1], (unsigned)(t1 - t0));
        }
        const unsigned b13 = ((a0 + (unsigned)pr * 16384u) >> 13) & 1;
        res = 2 + ((best[1] < best[0]) ? (b13 ^ 1) : b13);
      }
      if (lane == 0) __builtin_amdgcn_raw_buffer_store_b32(res, rx, (int)OFF_PTAB + r * 4, 0, AUX_SC1);
    }
}

DI void chain_role(const Params& p, unsigned char* smem, const rsrc_t rx, const int cu) {
  unsigned char* ws = p.ws;
  const int tid = threadIdx.x, lane = tid & 63, w = __builtin_amdgcn_readfirstlane(tid >> 6);
  const int kg = lane >> 4, n = lane & 15;
  const float sc = 0.0625f * 1.4426950408889634f;
  STAMP_INIT();
  auto startup = [&]() {
  {
    const v4u sent = {SENT, SENT, SENT, SENT};
    constexpr int EX_KB = (int)(64 * EX_STEP / 1024);
    for (int i = 16 + cu * 8 + w; i < EX_KB; i += N_CHAIN * 8) st4_sc1(sent, rx, (int)OFF_EX + i * 1024 + lane * 16);
    DRAIN();
    __syncthreads();
    if (tid == 448) arrive(ws, rx, CTR_FX, (unsigned)N_CHAIN);
  }
  {
    const unsigned a0 = (unsigned)(size_t)(ws + OFF_EX);
    if (w == 0) {
      unsigned v = 0;
      for (int spin = 0; spin < SPIN_MAX; ++spin) {
        asm volatile("" ::: "memory");
        v = __builtin_amdgcn_raw_buffer_load_b32(rx, (int)OFF_PTAB + (lane & 3) * 4, 0, AUX_SC1);
        if (__all(v != 0)) break;
        __builtin_amdgcn_s_sleep(4);
      }
      ctr_wait(rx, CTR_FX, (unsigned)N_CHAIN, 4);
      if (lane < 4) *(unsigned*)(smem + L_VOTE + lane * 4) = v;
      if (lane < 2) *(unsigned*)(smem + L_SCH + lane * 4) = 0u;
    }
    __syncthreads();
    const unsigned a = a0 + (unsigned)tid * 16384u;
    const int reg = (int)((a >> 21) - (a0 >> 21)) & 3, b13 = (a >> 13) & 1;
    if (tid < 320) { const int nearbit = *(const int*)(smem + L_VOTE + reg * 4) & 1; *(int*)(smem + L_TAB2 + tid * 4) = (int)OFF_EX + tid * 16384 + ((nearbit != b13) ? 8192 : 0); }
    __syncthreads();
  }
  };
  const int lmoff = (kg >> 1) * 256 + ((n & 7) >> 2) * 128 + (kg & 1) * 64 + (n & 3) * 16;
  const int goff = (w - 4) * 2048 + lane * 16;
  if (cu < N_S) {
    const int c = cu, m7 = c / 7, r7 = c % 7;
    if (w < 4) {
      const int tile = 8 * c + 2 * w, hS = tile / 28, ksj = (tile % 28) >> 1;
      v4u Mf[2][KS];
#pragma unroll
      for (int q = 0; q < 2; ++q)
#pragma unroll
        for (int ks = 0; ks < KS; ++ks) Mf[q][ks] = *(const v4u*)(ws + OFF_WC + ((size_t)(tile + q) * 14 + ks) * 1024 + lane * 16);
      startup();
#pragma unroll
      for (int ks = 0; ks < KS; ++ks) { KEEP4(Mf[0][ks]); KEEP4(Mf[1][ks]); }
      unsigned char* pdst = smem + ((kg < 2 && n < 8) ? L_P1 + (kg * 8 + n) * 16 + w * 4 : L_DUMMY);
      const int kmo = (int)OFF_KM + ((hS * 14 + ksj) * 4 + kg) * 128 + n * 16;
#pragma nounroll
      for (int s = 0; s < NSEG; ++s) {
        const unsigned char* lm = smem + L_MEM + (s & 1) * 7168 + lmoff;
        BAR();
        STAMP(2);
        v4u mf[KS + 1];
#pragma unroll
        for (int ks = 0; ks < PFD; ++ks) mf[ks] = *(const v4u*)(lm + ks * 512);
        __builtin_amdgcn_sched_barrier(0);
        f32x4 a0 = {0, 0, 0, 0}, a1 = {0, 0, 0, 0};
#pragma unroll
        for (int ks = 0; ks < KS; ++ks) {
          if (ks + PFD < KS) mf[ks + PFD] = *(const v4u*)(lm + (ks + PFD) * 512);
          if (ks + PFD == KS) mf[KS] = *(const v4u*)(lm + ksj * 512);
          a0 = mfma32(Mf[0][ks], mf[ks], a0); a1 = mfma32(Mf[1][ks], mf[ks], a1);
          __builtin_amdgcn_sched_barrier(0);
        }
        const v4u piece = mf[KS];
        const v4u tb = pk8(a0, a1);
        const f32x4 d2 = mfma32(tb, piece, f32x4{0, 0, 0, 0});
        *(float*)(pdst) = d2.x; *(float*)(pdst + 256) = d2.y; *(float*)(pdst + 512) = d2.z; *(float*)(pdst + 768) = d2.w;
        STAMP(3);
        BAR();
        st4_sc1(nosent4(tb), rx, n < 8 ? kmo + s * (16 * 14 * 512) : OOB);
        DRAIN();
        if (s == S_FAST - 1) ctr_wait(rx, CTR_FS + 256, 8u, 4);
      }
      BAR();
    } else {
      startup();
      Sched sd = {0u, 0u, 0, 0, 0, 0, 1};
      const bool strad = r7 == 3;
      const int ii = lane & 7, key = ((lane >> 3) & 1) * 4 + (lane >> 4);
      const int hh = 2 * m7 + (r7 > 3 ? 1 : 0), pp = r7 > 3 ? r7 - 3 : r7;
      const int so1 = ((hh * 4 + pp) * 4 + (ii & 3)) * 32 + key * 4;
      const int so2 = (((2 * m7 + 1) * 4 + 0) * 4 + (ii & 3)) * 32 + key * 4;
      const unsigned char* psrc = smem + L_P1 + lane * 16;
#pragma nounroll
      for (int s = 0; s < NSEG; ++s) {
        const int o0 = ex_off(s, 0) + goff;
        const int eo2 = ex_off(s, 2), eo4 = ex_off(s, 4);
        const int eb = (ii >> 2) ? eo4 : eo2;
        const int sto1 = (w == 4 && s < NSEG - 1) ? eb + so1 : OOB;
        const int sto2 = (w == 4 && s < NSEG - 1 && strad) ? eb + so2 : OOB;
        unsigned char* d0 = smem + L_MEM + (s & 1) * 7168 + goff;
        unsigned char* d1 = w == 7 ? smem + L_DUMMY : d0 + 1024;
        const int ot = (s > 0 && lane < NREP) ? o0 - goff + 7168 + lane * 128 : OOB;
        unsigned word;
        KEEP4(sto1); KEEP4(sto2);
        if (s > 1) wait_until(sd.tE2);
        else if (s == 1) { int kn = 0; kv_ensure(rx, 0, kn, 8); sleep_units(T_Q2); }
        STAMP(0);
        const int spin = gather_poll(rx, d0, d1, o0, w == 7 ? OOB : o0 + 1024, ot, word);
        BAR();
        STAMP(1);
        if (w == 7) TL(s);
        BAR();
        const f32x4 a = *(const f32x4*)psrc;
        const float u0 = a.x + a.y, u1 = a.z + a.w;
        const float v1 = strad ? u0 : u0 + u1;
        st1_sc1(nosent(__float_as_uint(v1)), rx, sto1);
        st1_sc1(nosent(__float_as_uint(u1)), rx, sto2);
        st1_sc1(nosent(__float_as_uint(v1)), rx, sto1 ^ 8192);
        st1_sc1(nosent(__float_as_uint(u1)), rx, sto2 ^ 8192);
        __builtin_amdgcn_sched_barrier(0);
        STAMP(5);
        STAMPV(10, spin);
        sched_step(sd, word, lane, s);
        sd.nf += (s > 1 && spin) ? 1 : 0;
        if (w == 6 && lane == 0 && s > 0) arrive(ws, rx, CTR_M + (s - 1) * 128, 84u);
        if (cu == 0 && w == 5 && lane == 0 && s >= S_FAST - 1 && s < NSEG - 1) st1_sc1(sd.tE2 | 1u, rx, OFF_TT + (s + 1) * 128);
      }
      BAR();
      if (w == 6 && lane == 0) arrive(ws, rx, CTR_M + (NSEG - 1) * 128, 84u);
      FAIL_DUMP(sd.nf);
    }
  } else if (cu < N_S + N_O) {
    const int o_ = cu - N_S, rg = o_ / 28, ct = o_ % 28;
    v4u Nf[2][KS];
#pragma unroll
    for (int q = 0; q < 2; ++q)
#pragma unroll
      for (int ks = 0; ks < KS; ++ks) Nf[q][ks] = *(const v4u*)(ws + OFF_WC + ((size_t)(448 + (2 * w + q) * 28 + ct) * 14 + ks) * 1024 + lane * 16);
    startup();
#pragma unroll
    for (int ks = 0; ks < KS; ++ks) { KEEP4(Nf[0][ks]); KEEP4(Nf[1][ks]); }
    auto publish_y = [&](int s) {
      const int h = 4 * (w - 4) + kg;
      const unsigned char* ys = smem + L_YB + (h * 4) * 64 + n * 4;
      const v4u y = v4u{*(const unsigned*)(ys), *(const unsigned*)(ys + 64), *(const unsigned*)(ys + 128), *(const unsigned*)(ys + 192)};
      st4_sc1(nosent4(y), rx, rg == 0 ? (int)OFF_VM + ((s * 16 + h) * 28 + ct) * 256 + n * 16 : OOB);
    };
    auto compute_y = [&](int s) {
      const unsigned char* lm = smem + L_MEM + (s & 1) * 7168 + lmoff;
      v4u mf[KS];
#pragma unroll
      for (int ks = 0; ks < PFD; ++ks) mf[ks] = *(const v4u*)(lm + ks * 512);
      __builtin_amdgcn_sched_barrier(0);
      f32x4 a0 = {0, 0, 0, 0}, a1 = {0, 0, 0, 0};
#pragma unroll
      for (int ks = 0; ks < KS; ++ks) {
        if (ks + PFD < KS) mf[ks + PFD] = *(const v4u*)(lm + (ks + PFD) * 512);
        a0 = mfma32(mf[ks], Nf[0][ks], a0); a1 = mfma32(mf[ks], Nf[1][ks], a1);
        __builtin_amdgcn_sched_barrier(0);
      }
      v2u y0 = pk4(a0), y1 = pk4(a1);
      unsigned char* yd = smem + (kg < 2 ? L_YB + ((2 * w) * 4 + 2 * kg) * 64 + n * 4 : L_DUMMY);
      *(unsigned*)(yd) = y0.x; *(unsigned*)(yd + 64) = y0.y;
      *(unsigned*)(yd + 256) = y1.x; *(unsigned*)(yd + 320) = y1.y;
    };
    if (w < 4) {
      int nf = 0;
      v4u Vf[4];
      int known = 0;
      unsigned aold = 0;
      kv_ensure(rx, 0, known, 4);
#pragma unroll
      for (int g2 = 0; g2 < 4; ++g2) Vf[g2] = ld_sc1(rx, (int)OFF_VP + ((4 * w + g2) * 28 + ct) * 1024 + lane * 16);
      const int g = n >> 2, i4 = n & 3, hP = 4 * w + g;
      unsigned char* pdst = smem + L_P1 + (kg * 4 + i4) * 80 + (w * 4 + g) * 4;
      const unsigned char* ys = smem + L_YB + ((4 * w) * 4 + 2 * (kg & 1) + (kg >> 1)) * 64 + n * 4;
#pragma nounroll
      for (int s = 0; s < NSEG; ++s) {
        const int eb = ex_off(s, 1 + 2 * rg);
        int oe = eb + (hP * 4 + i4) * 64 + kg * 16;
        int ol = eb + 4096 + (hP * 4 + i4) * 4;
        int om = ex_off(s, 2 + 2 * rg) + ((hP * 4 + 2 * (kg >> 1)) * 4 + i4) * 32 + (kg & 1) * 16;
        KEEP4(oe); KEEP4(ol); KEEP4(om);
        BAR();
        STAMP(2);
        compute_y(s);
#pragma unroll
        for (int g2 = 0; g2 < 4; ++g2) KEEP4(Vf[g2]);
        if (w == 3 && lane == 0 && rg == 0 && s > 0) arrive_finish(rx, aold, CTR_M + (s - 1) * 128, 84u);
        if (s == NSEG - 1) break;
        unsigned tiss;
        for (;;) {
          const v2u tv = *(volatile const v2u*)(smem + L_SCH);
          tiss = __builtin_amdgcn_readfirstlane(tv.x);
          if (__builtin_amdgcn_readfirstlane(tv.y) == (unsigned)(s + 1)) break;
        }
        STAMP(3);
        if (s > 0) tiss = wait_until(tiss);
        else { sleep_units(T_Q1); tiss = rt32(); }
        STAMP(6);
        int spin = 0;
        f32x4 dres;
        v4u Yf = v4u{0, 0, 0, 0};
#pragma nounroll
        for (;;) {
          asm volatile("" ::: "memory");
          const v4u q0 = ld_sc1(rx, om), q1 = ld_sc1(rx, om + 128);
          const unsigned ls = __builtin_amdgcn_raw_buffer_load_b32(rx, ol, 0, AUX_SC1);
          const v4u ex = ld_sc1(rx, oe);
          __builtin_amdgcn_sched_barrier(0);
          if (spin == 0) {
            BAR();
            Yf = v4u{*(const unsigned*)(ys), *(const unsigned*)(ys + 256), *(const unsigned*)(ys + 512), *(const unsigned*)(ys + 768)};
            KEEP4(Yf);
          }
          __builtin_amdgcn_sched_barrier(0);
          const f32x4 sq = __builtin_bit_cast(f32x4, q0) + __builtin_bit_cast(f32x4, q1);
          auto ra = __builtin_amdgcn_permlane32_swap(__float_as_uint(sq.x), __float_as_uint(sq.z), false, false);
          auto rb = __builtin_amdgcn_permlane32_swap(__float_as_uint(sq.y), __float_as_uint(sq.w), false, false);
          const unsigned ra0 = ra[0], ra1 = ra[1], rb0 = rb[0], rb1 = rb[1];
          const float sm0 = __uint_as_float(ra0) + __uint_as_float(ra1), sm1 = __uint_as_float(rb0) + __uint_as_float(rb1);
          const float lse = __uint_as_float(ls);
          const float mx = xmax32(xmax16(fmaxf(sm0, sm1)));
          const float nb = -fmaxf(mx * sc, lse);
          const float e0 = __builtin_amdgcn_exp2f(__builtin_fmaf(sm0, sc, nb)), e1 = __builtin_amdgcn_exp2f(__builtin_fmaf(sm1, sc, nb));
          const float wx = __builtin_amdgcn_exp2f(lse + nb);
          const unsigned pk = pk2(e0, e1);
          const v4u pb = v4u{g == 0 ? pk : 0u, g == 1 ? pk : 0u, g == 2 ? pk : 0u, g == 3 ? pk : 0u};
          const f32x4 dx0 = mfma32(Vf[0], ex, f32x4{0, 0, 0, 0});
          const f32x4 dx1 = mfma32(Vf[1], ex, f32x4{0, 0, 0, 0});
          const f32x4 dx2 = mfma32(Vf[2], ex, f32x4{0, 0, 0, 0});
          const f32x4 dx3 = mfma32(Vf[3], ex, f32x4{0, 0, 0, 0});
          const f32x4 dm = mfma32(Yf, pb, f32x4{0, 0, 0, 0});
          const float inv = __builtin_amdgcn_rcpf(wx + xsum32(xsum16(e0 + e1)));
          const unsigned mxu = umax3(umax3(umax4(q0), umax4(q1), ls), umax4(ex), 0u);
          const f32x4 dxa = g < 2 ? (g == 0 ? dx0 : dx1) : (g == 2 ? dx2 : dx3);
          dres = (dxa * wx + dm) * inv;
          *(float*)(pdst) = dres.x; *(float*)(pdst + 1280) = dres.y; *(float*)(pdst + 2560) = dres.z; *(float*)(pdst + 3840) = dres.w;
          if (__builtin_expect(__all(mxu != SENT), 1)) break;
          if (spin == 0) { oe ^= 8192; ol ^= 8192; om ^= 8192; sleep_units(T_R1); } else __builtin_amdgcn_s_sleep(2);
          if (++spin >= SPIN_MAX) break;
          tiss = rt32();
        }
        *(unsigned*)(smem + L_SCH + 16 + w * 4) = (tiss << 2) | (spin ? 2u : 0u);
        DRAIN();
        STAMP(8);
        BAR();
        STAMPV(11, spin);
        nf += (s > 1 && spin) ? 1 : 0;
        if (w == 3 && lane == 0 && rg == 0) aold = arrive_issue(ws, CTR_M + s * 128);
        const int sn = s + 1 < NSEG - 1 ? s + 1 : s;
        kv_ensure(rx, sn >> 1, known, 4);
#pragma unroll
        for (int g2 = 0; g2 < 4; ++g2) Vf[g2] = ld_sc1(rx, (int)OFF_VP + (sn * 448 + (4 * w + g2) * 28 + ct) * 1024 + lane * 16);
      }
      BAR();
      BAR();
      FAIL_DUMP(nf);
    } else {
      Sched sd = {0u, 0u, 0, 0, 0, 0, 1};
      const unsigned char* pp = smem + L_P1 + lane * 80;
      const bool st = w == 4 && (kg & 1) == 0;
      const int eoff = ct * 256 + rg * 128 + ((((n >> 3) & 1) * 4 + (n & 3)) * 8 + ((n >> 2) & 1) * 4 + kg) * 2;
#pragma nounroll
      for (int s = 0; s < NSEG; ++s) {
        const int o0 = ex_off(s, 0) + goff;
        unsigned char* d0 = smem + L_MEM + (s & 1) * 7168 + goff;
        unsigned char* d1 = w == 7 ? smem + L_DUMMY : d0 + 1024;
        const int ot = (s > 0 && lane < NREP) ? o0 - goff + 7168 + lane * 128 : OOB;
        unsigned word;
        if (s > 1) wait_until(sd.tE2 + T_OG);
        else if (s == 1) sleep_units(8);
        STAMP(0);
        const int spin = gather_poll(rx, d0, d1, o0, w == 7 ? OOB : o0 + 1024, ot, word);
        __builtin_amdgcn_s_setprio(3);
        BAR();
        STAMP(1);
        sched_step(sd, word, lane, s);
        if (w == 4) *(volatile v2u*)(smem + L_SCH) = v2u{sd.tE1, (unsigned)(s + 1)};
        __builtin_amdgcn_s_setprio(0);

        sd.nf += (s > 1 && spin) ? 1 : 0;
        *(unsigned*)(smem + L_SCH + 32 + (w - 4) * 4) = (s > 1 && spin) ? 1u : 0u;
        compute_y(s);
        if (s == NSEG - 1) break;
        const int eon = ex_off(s + 1, 0);
        const int sto = st ? eon + eoff : ((w == 5 && lane == 0 && o_ % (56 / NREP) == 0) ? eon + 7168 + (o_ / (56 / NREP)) * 128 : OOB);
        BAR();
        publish_y(s);
        if (s == S_FAST - 2) ctr_wait(rx, CTR_FS + 256, 8u, 4);
        const v4u gf = *(const v4u*)(smem + L_SCH + 32);
        const unsigned gfl = gf.x | gf.y | gf.z | gf.w;
        const unsigned tref = sd.tE1 << 2;
        KEEP4(sto); KEEP4(gfl);
        DRAIN();
        if (w == 4) {
          BAR();
          STAMP(4);
          const f32x4 a = (*(const f32x4*)(pp) + *(const f32x4*)(pp + 16)) + (*(const f32x4*)(pp + 32) + *(const f32x4*)(pp + 48));
          const float v = (a.x + a.y) + (a.z + a.w);
          auto r = __builtin_amdgcn_permlane16_swap(__float_as_uint(v), __float_as_uint(v), false, false);
          const unsigned r0 = r[0], r1 = r[1];
          const unsigned ov = nosent(pk2(__uint_as_float(r0), __uint_as_float(r1)));
          st1_sc1(ov, rx, sto);
          st1_sc1(ov, rx, sto ^ 8192);
        } else {
          BAR();
          const v4u tw = *(const v4u*)(smem + L_SCH + 16);
          const unsigned tm = tref + (unsigned)max(max((int)(tw.x - tref), (int)(tw.y - tref)), max((int)(tw.z - tref), (int)(tw.w - tref)));
          const unsigned wd = nosent(tm | ((tw.x | tw.y | tw.z | tw.w) & 3u) | gfl);
          st1_sc1(wd, rx, sto);
          st1_sc1(wd, rx, sto ^ 8192);
        }
        __builtin_amdgcn_sched_barrier(0);
        STAMP(5);
        STAMPV(10, spin);
      }
      BAR();
      publish_y(NSEG - 1);
      DRAIN();
      BAR();
      if (w == 6 && lane == 0 && rg == 0) arrive(ws, rx, CTR_M + (NSEG - 1) * 128, 84u);
      FAIL_DUMP(w == 7 ? sd.m1 * 1000 + sd.m2 : sd.nf);
    }
  } else {
    startup();
    if (w < 2) {
      const int h = 2 * (cu - N_S - N_O) + w;
      v4u K[2][KS];
      int known = 0;
      kv_ensure(rx, 0, known, 4);
#pragma unroll
      for (int u = 0; u < 2; ++u)
#pragma unroll
        for (int ks = 0; ks < KS; ++ks) K[u][ks] = ld_sc1(rx, (int)OFF_KP + ((h * 2 + u) * 14 + ks) * 1024 + lane * 16);
#pragma nounroll
      for (int s = 0; s < NSEG - 1; ++s) {
        const int eo1 = ex_off(s, 1), eo3 = ex_off(s, 3);
        const int eb = ((n >> 2) & 1) ? eo3 : eo1;
        const int so = n < 8 ? eb + (h * 4 + (n & 3)) * 64 + kg * 16 : OOB;
        const int lo = lane < 8 ? eb + 4096 + (h * 4 + (n & 3)) * 4 : OOB;
        const unsigned char* lm = smem + L_MEM + (s & 1) * 7168 + lmoff;
#pragma unroll
        for (int ks = 0; ks < KS; ++ks) { KEEP4(K[0][ks]); KEEP4(K[1][ks]); }
        BAR();
        STAMP(2);
        v4u mf[KS];
#pragma unroll
        for (int ks = 0; ks < PFD; ++ks) mf[ks] = *(const v4u*)(lm + ks * 512);
        __builtin_amdgcn_sched_barrier(0);
        f32x4 a0 = {0, 0, 0, 0}, a1 = {0, 0, 0, 0};
#pragma unroll
        for (int ks = 0; ks < KS; ++ks) {
          if (ks + PFD < KS) mf[ks + PFD] = *(const v4u*)(lm + (ks + PFD) * 512);
          a0 = mfma32(K[0][ks], mf[ks], a0);
          a1 = mfma32(K[1][ks], mf[ks], a1);
          __builtin_amdgcn_sched_barrier(0);
        }
        float xs[8] = {rbf(a0.x), rbf(a0.y), rbf(a0.z), rbf(a0.w), rbf(a1.x), rbf(a1.y), rbf(a1.z), rbf(a1.w)};
        float mx = xs[0];
#pragma unroll
        for (int j = 1; j < 8; ++j) mx = fmaxf(mx, xs[j]);
        mx = xmax32(xmax16(mx));
        const float nb = -mx * sc;
        float sum = 0.f;
#pragma unroll
        for (int j = 0; j < 8; ++j) { xs[j] = __builtin_amdgcn_exp2f(__builtin_fmaf(xs[j], sc, nb)); sum += xs[j]; }
        sum = xsum32(xsum16(sum));
        const float inv = __builtin_amdgcn_rcpf(sum);
        v4u o = v4u{pk2(xs[0] * inv, xs[1] * inv), pk2(xs[2] * inv, xs[3] * inv), pk2(xs[4] * inv, xs[5] * inv), pk2(xs[6] * inv, xs[7] * inv)};
        o = nosent4(o);
        st4_sc1(o, rx, so);
        const float lse = __builtin_amdgcn_logf(sum) - nb;
        st1_sc1(nosent(__float_as_uint(lse)), rx, lo);
        st4_sc1(o, rx, so ^ 8192);
        st1_sc1(nosent(__float_as_uint(lse)), rx, lo ^ 8192);
        __builtin_amdgcn_sched_barrier(0);
        STAMP(5);
        const int sn = s + 1 < NSEG - 1 ? s + 1 : s;
        kv_ensure(rx, sn >> 1, known, 4);
#pragma unroll
        for (int u = 0; u < 2; ++u)
#pragma unroll
          for (int ks = 0; ks < KS; ++ks) K[u][ks] = ld_sc1(rx, (int)OFF_KP + (((sn * 16 + h) * 2 + u) * 14 + ks) * 1024 + lane * 16);
      }
    } else if (w < 4) {
#pragma nounroll
      for (int s = 0; s < NSEG - 1; ++s) BAR();
    } else {
      Sched sd = {0u, 0u, 0, 0, 0, 0, 1};
#pragma nounroll
      for (int s = 0; s < NSEG - 1; ++s) {
        const int o0 = ex_off(s, 0) + goff;
        unsigned char* d0 = smem + L_MEM + (s & 1) * 7168 + goff;
        unsigned char* d1 = w == 7 ? smem + L_DUMMY : d0 + 1024;
        const int ot = (s > 0 && lane < NREP) ? o0 - goff + 7168 + lane * 128 : OOB;
        unsigned word;
        if (s > 1) wait_until(sd.tE2);
        else if (s == 1) { int kn = 0; kv_ensure(rx, 0, kn, 8); sleep_units(T_Q2); }
        STAMP(0);
        const int spin = gather_poll(rx, d0, d1, o0, w == 7 ? OOB : o0 + 1024, ot, word);
        BAR();
        STAMP(1);
        STAMPV(10, spin);
        sched_step(sd, word, lane, s);
        sd.nf += (s > 1 && spin) ? 1 : 0;
      }
      FAIL_DUMP(sd.nf);
    }
  }
  STAMP_DUMP();
}
DI void phase_kv(const Params& p, unsigned char* lds, rsrc_t rw, int cg, int par) {
  unsigned char* ws = p.ws;
  const int tid = threadIdx.x, lane = tid & 63, w = __builtin_amdgcn_readfirstlane(tid >> 6);
  const int nt0 = cg * 16 + 2 * w;
  v4u st[7];
  {
    const unsigned char* src = ws + OFF_XN + (size_t)par * 57344 + tid * 16;
#pragma unroll
    for (int i = 0; i < 7; ++i) st[i] = *(const v4u*)(src + i * 8192);
  }
  v4u W[2][KS];
#pragma unroll
  for (int q = 0; q < 2; ++q)
#pragma unroll
    for (int ks = 0; ks < KS; ++ks) W[q][ks] = *(const v4u*)(ws + OFF_WC + ((size_t)(nt0 + q) * 14 + ks) * 1024 + lane * 16);
#pragma unroll
  for (int i = 0; i < 7; ++i) *(v4u*)(lds + i * 8192 + tid * 16) = st[i];
#pragma unroll
  for (int ks = 0; ks < KS; ++ks) { KEEP4(W[0][ks]); KEEP4(W[1][ks]); }
  BAR();
  const bool isK = cg < 28;
  const int kbase = (int)OFF_KP + (((nt0 / 28) * 2) * 14 + ((nt0 % 28) >> 1)) * 1024 + lane * 16;
  const int vbase = (int)OFF_VP + (nt0 - 448) * 1024 + lane * 16;
  int pend = -1;
  unsigned aold = 0;
#pragma nounroll
  for (int it = 0; it < 16; ++it) {
    const int hb = par + 2 * it;
    const unsigned char* cur = lds + (it & 1) * 57344 + lane * 16;
    {
      const int so = (int)OFF_XN + (it + 1 < 16 ? hb + 2 : hb) * 57344;
#pragma unroll
      for (int i = 0; i < 7; ++i) st[i] = __builtin_amdgcn_raw_buffer_load_b128(rw, tid * 16, so + i * 8192, 0);
    }
    __builtin_amdgcn_sched_barrier(0);
    f32x4 acc[4][2];
#pragma unroll
    for (int rt = 0; rt < 4; ++rt) { acc[rt][0] = f32x4{0, 0, 0, 0}; acc[rt][1] = f32x4{0, 0, 0, 0}; }
    if (isK) {
      v4u af[2][4];
#pragma unroll
      for (int rt = 0; rt < 4; ++rt) af[0][rt] = *(const v4u*)(cur + (rt * 14) * 1024);
#pragma unroll
      for (int ks = 0; ks < KS; ++ks) {
        if (ks + 1 < KS) {
#pragma unroll
          for (int rt = 0; rt < 4; ++rt) af[(ks + 1) & 1][rt] = *(const v4u*)(cur + (rt * 14 + ks + 1) * 1024);
        }
        __builtin_amdgcn_sched_barrier(0);
#pragma unroll
        for (int rt = 0; rt < 4; ++rt) {
          acc[rt][0] = mfma32(W[0][ks], af[ks & 1][rt], acc[rt][0]);
          acc[rt][1] = mfma32(W[1][ks], af[ks & 1][rt], acc[rt][1]);
        }
        __builtin_amdgcn_sched_barrier(0);
      }
    } else {
      v4u af[2][4];
#pragma unroll
      for (int rt = 0; rt < 4; ++rt) af[0][rt] = *(const v4u*)(cur + (rt * 14) * 1024);
#pragma unroll
      for (int ks = 0; ks < KS; ++ks) {
        if (ks + 1 < KS) {
#pragma unroll
          for (int rt = 0; rt < 4; ++rt) af[(ks + 1) & 1][rt] = *(const v4u*)(cur + (rt * 14 + ks + 1) * 1024);
        }
        __builtin_amdgcn_sched_barrier(0);
#pragma unroll
        for (int rt = 0; rt < 4; ++rt) {
          acc[rt][0] = mfma32(af[ks & 1][rt], W[0][ks], acc[rt][0]);
          acc[rt][1] = mfma32(af[ks & 1][rt], W[1][ks], acc[rt][1]);
        }
        __builtin_amdgcn_sched_barrier(0);
      }
    }
    __builtin_amdgcn_sched_barrier(0);
#pragma unroll
    for (int i = 0; i < 7; ++i) *(v4u*)(lds + ((it + 1) & 1) * 57344 + i * 8192 + tid * 16) = st[i];
    DRAIN();
    if (pend >= 0 && lane == 0) arrive_finish(rw, aold, pend, 56u);
    pend = -1;
    __builtin_amdgcn_sched_barrier(0);
    if (isK) {
#pragma unroll
      for (int rt = 0; rt < 4; ++rt) st4_sc1(pk8(acc[rt][0], acc[rt][1]), rw, kbase + ((hb * 2 + (rt >> 1)) * 32 + (rt & 1)) * 14336);
    } else {
#pragma unroll
      for (int q = 0; q < 2; ++q)
#pragma unroll
        for (int sg = 0; sg < 2; ++sg) st4_sc1(pk8(acc[2 * sg][q], acc[2 * sg + 1][q]), rw, vbase + ((hb * 2 + sg) * 448 + q) * 1024);
    }
    if (it == 0 || it == 15) DRAIN();
    BAR();
    if (w == 0) TL(200 + it);
    if (w == (it & 7)) {
      if (it == 0 || it == 15) { if (lane == 0) arrive(ws, rw, CTR_KV + hb * 128, 56u); }
      if (it >= 2) {
        if (it == 15) { if (lane == 0) arrive(ws, rw, CTR_KV + (hb - 2) * 128, 56u); }
        else { pend = CTR_KV + (hb - 2) * 128; if (lane == 0) aold = arrive_issue(ws, pend); }
      }
    }
  }
}

DI void gate_wait(rsrc_t rw, int rt, int nap, int lag, int own) {
  int off = (int)OFF_PM + rt * 4096;
  if (__builtin_amdgcn_readfirstlane(ld1_sc1(rw, off)) != SENT) {
    off = own; lag = 4; nap = 8;
    if (__builtin_amdgcn_readfirstlane(ld1_sc1(rw, off)) != SENT) return;
  }
#pragma nounroll
  for (int spin = 0; spin < SPIN_MAX; ++spin) {
    sleep_units(nap);
    asm volatile("" ::: "memory");
    if (__builtin_amdgcn_readfirstlane(ld1_sc1(rw, off)) != SENT) break;
  }
  sleep_units(lag);
}
#define RETRY 4
DI void regate(rsrc_t rw, int off) {
#pragma nounroll
  for (int spin = 0; spin < SPIN_MAX; ++spin) {
    sleep_units(RETRY);
    asm volatile("" ::: "memory");
    if (__all(ld1_sc1(rw, off) != SENT)) break;
  }
}
DI void tt_wait(rsrc_t rw, int s, int lag) {
  unsigned t = 0;
#pragma nounroll
  for (int spin = 0; spin < SPIN_MAX; ++spin) {
    asm volatile("" ::: "memory");
    t = __builtin_amdgcn_readfirstlane(ld1_sc1(rw, OFF_TT + s * 128));
    if (t != 0u) break;
    sleep_units(12);
  }
  wait_until(t + lag);
}
#define TT_A1 135
#define TT_A2 275
#define TT_F1 415
#define TT_F2 615
#define LAG_A2 10
#define LAG_F1 65
#define LAG_F2 140
#define NAPT 4
#define NAP 6

#define TLX(k) do { if (rt == 127 || rt == 96) TL((rt == 127 ? 130 : 140) + (k)); } while (0)
DI void stage_a1(const Params& p, rsrc_t rw, int h) {
  unsigned char* ws = p.ws;
  const int tid = threadIdx.x, lane = tid & 63, w = __builtin_amdgcn_readfirstlane(tid >> 6);
  const int kg = lane >> 4, n = lane & 15, v = w & 1;
  const float sc = 0.0625f * 1.4426950408889634f;
  const float NEG = -3.0e38f;
  int known = 0;
#pragma nounroll
  for (int rt = w; rt < 128; rt += 8) {
    const int s = rt >> 1;
    v4u xf[KS], kf[KS], k1[KS];
    kv_ensure(rw, s >> 1, known, NAP);
    const int kb = (int)OFF_KP + ((s * 16 + h) * 2) * 14336 + lane * 16;
#pragma unroll
    for (int ks = 0; ks < KS; ++ks) {
      xf[ks] = *(const v4u*)(ws + OFF_XN + ((size_t)rt * 14 + ks) * 1024 + lane * 16);
      kf[ks] = ld_sc1(rw, kb + ks * 1024);
    }
#pragma unroll
    for (int ks = 0; ks < KS; ++ks) k1[ks] = ld_sc1(rw, v ? kb + 14336 + ks * 1024 : OOB);
    __builtin_amdgcn_sched_barrier(0);
    f32x4 a0 = {0, 0, 0, 0}, a1 = {0, 0, 0, 0}, am = {0, 0, 0, 0};
#pragma unroll
    for (int ks = 0; ks < KS; ++ks) a0 = mfma32(kf[ks], xf[ks], a0);
#pragma unroll
    for (int ks = 0; ks < KS; ++ks) a1 = mfma32(k1[ks], xf[ks], a1);
    float xs[8];
#pragma unroll
    for (int r = 0; r < 4; ++r) {
      const bool diag = 4 * kg + r <= n;
      xs[r] = (v || diag) ? rbf(a0[r]) : NEG;
      xs[4 + r] = (v && diag) ? rbf(a1[r]) : NEG;
    }
    float mx = NEG;
#pragma unroll
    for (int j = 0; j < 8; ++j) mx = fmaxf(mx, xs[j]);
    const int tb = (int)OFF_KM + (s * 16 + h) * 7168 + kg * 128 + (n & 7) * 16;
    TLX(3);
    if (s < S_FAST) ctr_wait(rw, CTR_M + s * 128, 84u, NAP); else { if (s < S_FAST + 4) ctr_wait(rw, CTR_FS + 256, 8u, NAP); tt_wait(rw, s, TT_A1); }
    TLX(0);
#pragma nounroll
    for (int spin = 0; spin < SPIN_MAX; ++spin) {
      asm volatile("" ::: "memory");
      TLX(1);
      unsigned mxu = 0;
#pragma unroll
      for (int ks = 0; ks < KS; ++ks) kf[ks] = ld_sc1(rw, tb + ks * 512);
      if (s < S_FAST) break;
#pragma unroll
      for (int ks = 0; ks < KS; ++ks) mxu = max(mxu, umax4(kf[ks]));
      if (__all(mxu != SENT)) break;
      regate(rw, lane < 14 ? (int)OFF_KM + (s * 16 + h) * 7168 + lane * 512 + 508 : OOB);
    }
#pragma unroll
    for (int ks = 0; ks < KS; ++ks) am = mfma32(kf[ks], xf[ks], am);
    float mm[4];
#pragma unroll
    for (int r = 0; r < 4; ++r) { mm[r] = rbf(am[r]); mx = fmaxf(mx, mm[r]); }
    mx = xmax32(xmax16(mx));
    const float nb = -mx * sc;
    float sum = 0.f;
#pragma unroll
    for (int j = 0; j < 8; ++j) { xs[j] = xs[j] == NEG ? 0.f : __builtin_amdgcn_exp2f(__builtin_fmaf(xs[j], sc, nb)); sum += xs[j]; }
#pragma unroll
    for (int r = 0; r < 4; ++r) { mm[r] = __builtin_amdgcn_exp2f(__builtin_fmaf(mm[r], sc, nb)); if (kg < 2) sum += mm[r]; }
    sum = xsum32(xsum16(sum));
    const float inv = 1.f / sum;
    st4_sc1(nosent4(v4u{pk2(xs[0] * inv, xs[1] * inv), pk2(xs[2] * inv, xs[3] * inv), pk2(xs[4] * inv, xs[5] * inv), pk2(xs[6] * inv, xs[7] * inv)}),
            rw, (int)OFF_PX + (rt * 16 + h) * 1024 + lane * 16);
    st2_sc1(nosent2(v2u{pk2(mm[0] * inv, mm[1] * inv), pk2(mm[2] * inv, mm[3] * inv)}), rw,
            kg < 2 ? (int)OFF_PM + (rt * 4 + (h >> 2)) * 1024 + ((h & 3) * 16 + n) * 16 + kg * 8 : OOB);
    TL(rt);
  }
}

DI void stage_a2(const Params& p, rsrc_t rw, int ct) {
  const int tid = threadIdx.x, lane = tid & 63, w = __builtin_amdgcn_readfirstlane(tid >> 6);
  const int kg = lane >> 4, n = lane & 15;
  const int c = 2 * ct + (kg >> 1);
  const int col = 32 * (c >> 2) + 16 * (kg & 1) + 4 * (c & 3);
  const int hoff = (c >> 2) * 1024 + ((c & 3) * 16 + n) * 16 + (kg & 1) * 8;
  int known = 0;
#pragma nounroll
  for (int rt = w; rt < 128; rt += 8) {
    const int s = rt >> 1;
    const v2u xv = *(const v2u*)(p.x + (size_t)(rt * 16 + n) * 448 + col);
    kv_ensure(rw, s >> 1, known, NAP);
    v4u vf[16], yf[4];
#pragma unroll
    for (int h = 0; h < 16; ++h) vf[h] = ld_sc1(rw, (int)OFF_VP + (s * 448 + h * 28 + ct) * 1024 + lane * 16);
    TLX(3);
    if (s < S_FAST) ctr_wait(rw, CTR_M + s * 128, 84u, NAP); else if (s < S_FAST + 4) ctr_wait(rw, CTR_FS + 256, 8u, NAP);
    const int pb = (int)OFF_PX + rt * 16384 + lane * 16, mb = (int)OFF_PM + rt * 4096 + lane * 16;
    if (rt >= 2 * S_FAST) tt_wait(rw, rt >> 1, TT_A2); else gate_wait(rw, rt, NAPT, LAG_A2, (int)OFF_PX + (rt * 16 + (ct & 15)) * 1024);
    TLX(0);
    f32x4 acc;
#pragma nounroll
    for (int spin = 0; spin < SPIN_MAX; ++spin) {
      asm volatile("" ::: "memory");
      TLX(1);
      v4u px[16], pm[4];
#pragma unroll
      for (int q = 0; q < 4; ++q) yf[q] = ld_sc1(rw, (int)OFF_VM + ((s * 16 + 4 * q + kg) * 28 + ct) * 256 + n * 16);
#pragma unroll
      for (int h = 0; h < 16; ++h) px[h] = ld_sc1(rw, pb + h * 1024);
#pragma unroll
      for (int q = 0; q < 4; ++q) pm[q] = ld_sc1(rw, mb + q * 1024);
      unsigned mxu = 0;
      f32x4 a0 = {0, 0, 0, 0}, a1 = {0, 0, 0, 0};
#pragma unroll
      for (int h = 0; h < 16; h += 2) {
        mxu = umax3(mxu, umax4(px[h]), umax4(px[h + 1]));
        a0 = mfma32(vf[h], px[h], a0); a1 = mfma32(vf[h + 1], px[h + 1], a1);
      }
#pragma unroll
      for (int q = 0; q < 4; q += 2) {
        mxu = umax3(mxu, umax3(umax4(pm[q]), umax4(pm[q + 1]), umax4(yf[q])), umax4(yf[q + 1]));
        a0 = mfma32(yf[q], pm[q], a0); a1 = mfma32(yf[q + 1], pm[q + 1], a1);
      }
      acc = a0 + a1;
      if (__all(mxu != SENT)) break;
      regate(rw, lane < 16 ? (int)OFF_PM + rt * 4096 + lane * 256 + 252 : (lane < 32 ? (int)OFF_VM + ((s * 16 + lane - 16) * 28 + ct) * 256 + 252 : OOB));
    }
    const float h0 = bflo(xv.x) + rbf(acc.x), h1 = bfhi(xv.x) + rbf(acc.y), h2 = bflo(xv.y) + rbf(acc.z), h3 = bfhi(xv.y) + rbf(acc.w);
    st2_sc1(nosent2(v2u{pk2(h0, h1), pk2(h2, h3)}), rw, (int)OFF_HF + rt * 14336 + hoff);
    TL(rt);
  }
}

DI void stage_f1(const Params& p, unsigned char* lds, rsrc_t rw, int kf) {
  const int tid = threadIdx.x, lane = tid & 63, w = __builtin_amdgcn_readfirstlane(tid >> 6);
  const int kg = lane >> 4, n = lane & 15;
  for (int f = w; f < 56; f += 8) {
    const int t = f / 14, ks = f % 14;
    const u16* src = ((t >> 1) ? p.wu : p.wg) + 32 * kf + 16 * (t & 1) + n;
    unsigned e[8];
#pragma unroll
    for (int jj = 0; jj < 8; ++jj) e[jj] = src[(size_t)pidx(ks, kg, jj) * 1792];
    *(v4u*)(lds + f * 1024 + lane * 16) = v4u{e[0] | (e[1] << 16), e[2] | (e[3] << 16), e[4] | (e[5] << 16), e[6] | (e[7] << 16)};
  }
  BAR();
  const v4u ones = {0x3F803F80u, 0x3F803F80u, 0x3F803F80u, 0x3F803F80u};
  f32x4 cs[4];
#pragma unroll
  for (int t = 0; t < 4; ++t) {
    cs[t] = f32x4{0, 0, 0, 0};
#pragma unroll
    for (int ks = 0; ks < KS; ++ks) cs[t] = mfma32(*(const v4u*)(lds + (t * 14 + ks) * 1024 + lane * 16), ones, cs[t]);
  }
#pragma nounroll
  for (int rt = w; rt < 128; rt += 8) {
    const int hb = (int)OFF_HF + rt * 14336 + lane * 16;
    if (rt >= 2 * S_FAST) tt_wait(rw, rt >> 1, TT_F1); else gate_wait(rw, rt, NAPT, LAG_F1, (int)OFF_HF + rt * 14336 + (kf >> 1) * 512);
    TLX(0);
    v4u hf[KS];
#pragma nounroll
    for (int spin = 0; spin < SPIN_MAX; ++spin) {
      asm volatile("" ::: "memory");
      TLX(1);
      unsigned mxu = 0;
#pragma unroll
      for (int ks = 0; ks < KS; ++ks) hf[ks] = ld_sc1(rw, hb + ks * 1024);
#pragma unroll
      for (int ks = 0; ks < KS; ++ks) mxu = max(mxu, umax4(hf[ks]));
      if (__all(mxu != SENT)) break;
      regate(rw, lane < 28 ? (int)OFF_HF + rt * 14336 + lane * 512 + 508 : OOB);
    }
    TLX(2);
    f32x4 acc[4], sm = {0, 0, 0, 0}, gr = {0, 0, 0, 0};
#pragma unroll
    for (int t = 0; t < 4; ++t) acc[t] = f32x4{0, 0, 0, 0};
    v4u wf[2][4];
#pragma unroll
    for (int t = 0; t < 4; ++t) wf[0][t] = *(const v4u*)(lds + (t * 14) * 1024 + lane * 16);
#pragma unroll
    for (int ks = 0; ks < KS; ++ks) {
      if (ks + 1 < KS) {
#pragma unroll
        for (int t = 0; t < 4; ++t) wf[(ks + 1) & 1][t] = *(const v4u*)(lds + (t * 14 + ks + 1) * 1024 + lane * 16);
      }
      sm = mfma32(ones, hf[ks], sm);
      gr = mfma32(hf[ks], hf[ks], gr);
      __builtin_amdgcn_sched_barrier(0);
#pragma unroll
      for (int t = 0; t < 4; ++t) acc[t] = mfma32(wf[ks & 1][t], hf[ks], acc[t]);
      __builtin_amdgcn_sched_barrier(0);
    }
    float dg = (kg == (n >> 2)) ? ((n & 2) ? ((n & 1) ? gr.w : gr.z) : ((n & 1) ? gr.y : gr.x)) : 0.f;
    dg = xsum32(xsum16(dg));
    const float mean = sm.x * (1.f / 448.f);
    const float var = fmaxf(dg * (1.f / 448.f) - mean * mean, 0.f);
    const float mb = rbf(mean), rden = 1.f / ln_den(var);
    f32x4 o0, o1;
#pragma unroll
    for (int r = 0; r < 4; ++r) {
      o0[r] = rbf((acc[0][r] - mb * cs[0][r]) * rden) * rbf((acc[2][r] - mb * cs[2][r]) * rden);
      o1[r] = rbf((acc[1][r] - mb * cs[1][r]) * rden) * rbf((acc[3][r] - mb * cs[3][r]) * rden);
    }
    st4_sc1(nosent4(pk8(o0, o1)), rw, (int)OFF_AF + (rt * 56 + kf) * 1024 + lane * 16);
    TL(rt);
  }
}

DI void stage_f2(const Params& p, unsigned char* lds, rsrc_t rw, int ct) {
  const int tid = threadIdx.x, lane = tid & 63, w = __builtin_amdgcn_readfirstlane(tid >> 6);
  const int kg = lane >> 4, n = lane & 15;
  for (int kf = w; kf < 56; kf += 8) {
    const u16* src = p.wd + 16 * ct + n;
    unsigned e[8];
#pragma unroll
    for (int jj = 0; jj < 8; ++jj) e[jj] = src[(size_t)pidx(kf, kg, jj) * 448];
    *(v4u*)(lds + kf * 1024 + lane * 16) = v4u{e[0] | (e[1] << 16), e[2] | (e[3] << 16), e[4] | (e[5] << 16), e[6] | (e[7] << 16)};
  }
  BAR();
#pragma nounroll
  for (int rt = w; rt < 128; rt += 8) {
    const int ab = (int)OFF_AF + rt * 57344 + lane * 16;
    if (rt >= 2 * S_FAST) tt_wait(rw, rt >> 1, TT_F2); else gate_wait(rw, rt, NAPT, LAG_F2, (int)OFF_AF + rt * 57344 + (2 * ct) * 1024);
    TLX(0);
    v2u hv = v2u{0, 0};
    f32x4 a0, a1;
#pragma nounroll
    for (int spin = 0; spin < SPIN_MAX; ++spin) {
      asm volatile("" ::: "memory");
      TLX(1);
      a0 = f32x4{0, 0, 0, 0}; a1 = f32x4{0, 0, 0, 0};
      hv = __builtin_amdgcn_raw_buffer_load_b64(rw, (int)OFF_HF + rt * 14336 + (ct >> 1) * 1024 + lane * 16 + (ct & 1) * 8, 0, AUX_SC1);
      v4u af[3][14];
#pragma unroll
      for (int bb = 0; bb < 2; ++bb)
#pragma unroll
        for (int k = 0; k < 14; ++k) af[bb][k] = ld_sc1(rw, ab + (bb * 14 + k) * 1024);
      unsigned mxu = max(hv.x, hv.y);
#pragma unroll
      for (int bb = 0; bb < 4; ++bb) {
        if (bb + 2 < 4) {
#pragma unroll
          for (int k = 0; k < 14; ++k) af[(bb + 2) % 3][k] = ld_sc1(rw, ab + ((bb + 2) * 14 + k) * 1024);
        }
        __builtin_amdgcn_sched_barrier(0);
#pragma unroll
        for (int k = 0; k < 14; k += 2) {
          mxu = umax3(mxu, umax4(af[bb % 3][k]), umax4(af[bb % 3][k + 1]));
          a0 = mfma32(*(const v4u*)(lds + (bb * 14 + k) * 1024 + lane * 16), af[bb % 3][k], a0);
          a1 = mfma32(*(const v4u*)(lds + (bb * 14 + k + 1) * 1024 + lane * 16), af[bb % 3][k + 1], a1);
        }
        __builtin_amdgcn_sched_barrier(0);
      }
      if (__all(mxu != SENT)) break;
      regate(rw, lane < 56 ? (int)OFF_AF + rt * 57344 + lane * 1024 + 1020 : OOB);
    }
    const f32x4 d = a0 + a1;
    const float o0 = bflo(hv.x) + rbf(d.x), o1 = bfhi(hv.x) + rbf(d.y), o2 = bflo(hv.y) + rbf(d.z), o3 = bfhi(hv.y) + rbf(d.w);
    *(v2u*)(p.out + (size_t)(rt * 16 + n) * 448 + 16 * ct + 4 * kg) = v2u{pk2(o0, o1), pk2(o2, o3)};
    TL(rt);
  }
}

DI void arm_stage(const Params& p, rsrc_t rw, int k, int nwg, int off0, int nkb, int ctr) {
  const int tid = threadIdx.x, lane = tid & 63, w = __builtin_amdgcn_readfirstlane(tid >> 6);
  const v4u sent = {SENT, SENT, SENT, SENT};
  for (int i = k * 8 + w; i < nkb; i += nwg * 8) st4_sc1(sent, rw, off0 + i * 1024 + lane * 16);
  DRAIN();
  __syncthreads();
  if (tid == 0) arrive(p.ws, rw, ctr, (unsigned)nwg);
}

constexpr int MAIN_LDS = 114688;
__global__ void __launch_bounds__(512, 2) k_main(Params p) {
  extern __shared__ __attribute__((aligned(16))) unsigned char smem[];
  const rsrc_t rw = __builtin_amdgcn_make_buffer_rsrc((void*)p.ws, 0, (int)WS_END, 0x00020000);
  const int b = blockIdx.x;
  if (threadIdx.x < 64) TL(255);
  const int j = (b >> 3) * 4 + (b & 3);
  if ((b & 7) < 4) {
    if (j < N_CHAIN) chain_role(p, smem, rw, j);
    else { if (j == N_CHAIN) probe_place(p, rw); sleep_units(400); arm_stage(p, rw, j - N_CHAIN, 8, (int)OFF_HF, (int)((OFF_XN - OFF_HF) / 1024), CTR_FS + 128);
      arm_stage(p, rw, j - N_CHAIN, 8, (int)OFF_KM + S_FAST * 114688, (NSEG - S_FAST) * 112, CTR_FS + 384);
      arm_stage(p, rw, j - N_CHAIN, 8, (int)OFF_VM + S_FAST * 114688, (NSEG - S_FAST) * 112, CTR_FS + 256); }
    return;
  }
  if (j < 112) phase_kv(p, smem, rw, j % 56, j / 56);
  else arm_stage(p, rw, j - 112, 16, (int)OFF_PX, (int)((OFF_HF - OFF_PX) / 1024), CTR_FS);
  ctr_wait(rw, CTR_FS, 16u, 8);
  if (j < 112) ctr_wait(rw, CTR_FS + 128, 8u, 8);
  if (j < 56) stage_f1(p, smem, rw, j);
  else if (j < 84) stage_f2(p, smem, rw, j - 56);
  else if (j < 112) stage_a2(p, rw, j - 84);
  else stage_a1(p, rw, j - 112);
}

extern "C" void kernel_launch(void* const* d_in, const int* in_sizes, int n_in, void* d_out, int out_size, void* d_ws, size_t ws_size,
                              hipStream_t stream) {
  Params p;
  p.x = (const u16*)d_in[0]; p.mem0 = (const u16*)d_in[1]; p.Wq = (const u16*)d_in[2]; p.Wk = (const u16*)d_in[3];
  p.Wv = (const u16*)d_in[4]; p.Wo = (const u16*)d_in[5];
  p.wg = (const u16*)d_in[8]; p.wu = (const u16*)d_in[9]; p.wd = (const u16*)d_in[10];
  p.out = (u16*)d_out; p.ws = (unsigned char*)d_ws;
  (void)hipFuncSetAttribute((const void*)k_main, hipFuncAttributeMaxDynamicSharedMemorySize, MAIN_LDS);
  (void)hipFuncSetAttribute((const void*)k_prep, hipFuncAttributeMaxDynamicSharedMemorySize, PREP_LDS);
  k_prep<<<256, 512, PREP_LDS, stream>>>(p);
  k_main<<<256, 512, MAIN_LDS, stream>>>(p);
}
```

```cpp
#include <hip/hip_runtime.h>
#include <hip/hip_bf16.h>
#include <stdint.h>

typedef short bf16x8 __attribute__((ext_vector_type(8)));
typedef float f32x4 __attribute__((ext_vector_type(4)));
typedef float f32x2 __attribute__((ext_vector_type(2)));
typedef __bf16 bf2_t __attribute__((ext_vector_type(2)));
typedef unsigned v4u __attribute__((ext_vector_type(4)));
typedef unsigned v2u __attribute__((ext_vector_type(2)));
typedef unsigned short u16;
typedef __amdgpu_buffer_rsrc_t rsrc_t;
#define DI __device__ __forceinline__

constexpr int NSEG = 64, KS = 14;
constexpr unsigned SENT = 0xFFFFFFFFu;
constexpr int AUX_SC1 = 16;
constexpr int SPIN_MAX = 20000;
constexpr int OOB = 0x7FFFFFF0;

constexpr size_t OFF_CTR = 0;
constexpr int CTR_KV = 0, CTR_M = 4096, OFF_PTAB = 16384, CTR_FX = 16640, CTR_FS = 16896;
constexpr size_t OFF_EX = 65536;
constexpr size_t EX_STEP = 5 * 16384;
constexpr size_t OFF_PX = OFF_EX + 64 * EX_STEP;
constexpr size_t OFF_PM = OFF_PX + (size_t)128 * 16 * 1024;
constexpr size_t OFF_HF = OFF_PM + (size_t)128 * 4 * 1024;
constexpr size_t OFF_AF = OFF_HF + (size_t)128 * 14 * 1024;
constexpr size_t OFF_XN = OFF_AF + (size_t)128 * 56 * 1024;
constexpr size_t OFF_WC = OFF_XN + (size_t)128 * 14 * 1024;
constexpr size_t OFF_KP = OFF_WC + (size_t)896 * 14 * 1024;
constexpr size_t OFF_VP = OFF_KP + (size_t)64 * 16 * 2 * 14 * 1024;
constexpr size_t OFF_KM = OFF_VP + (size_t)64 * 448 * 1024;
constexpr size_t OFF_VM = OFF_KM + (size_t)64 * 16 * 14 * 512;
constexpr size_t WS_END = OFF_VM + (size_t)64 * 16 * 28 * 256;
constexpr size_t FILL_BYTES = OFF_XN - OFF_EX;
static_assert(WS_END < (size_t)200 * 1024 * 1024, "ws");
static_assert(FILL_BYTES % 16384 == 0, "fill");

DI f32x4 mfma32(v4u a, v4u b, f32x4 c) {
  return __builtin_amdgcn_mfma_f32_16x16x32_bf16(__builtin_bit_cast(bf16x8, a), __builtin_bit_cast(bf16x8, b), c, 0, 0, 0);
}
DI unsigned pk2(float a, float b) {
  f32x2 v = {a, b};
  return __builtin_bit_cast(unsigned, __builtin_convertvector(v, bf2_t));
}
DI v2u pk4(f32x4 a) { return v2u{pk2(a.x, a.y), pk2(a.z, a.w)}; }
DI v4u pk8(f32x4 a, f32x4 b) { return v4u{pk2(a.x, a.y), pk2(a.z, a.w), pk2(b.x, b.y), pk2(b.z, b.w)}; }
DI float bflo(unsigned u) { return __uint_as_float(u << 16); }
DI float bfhi(unsigned u) { return __uint_as_float(u & 0xFFFF0000u); }
DI float rbf(float a) { return bflo(pk2(a, 0.f) & 0xFFFFu); }
DI int picol(int c, int cc) { return 32 * (c >> 2) + 16 * (cc >> 2) + 4 * (c & 3) + (cc & 3); }
DI int pidx(int ks, int kg, int jj) { return 32 * ks + 16 * (jj >> 2) + 4 * kg + (jj & 3); }
DI float xsum16(float v) { auto r = __builtin_amdgcn_permlane16_swap(__float_as_uint(v), __float_as_uint(v), false, false);
  const unsigned a = r[0], b = r[1]; return __uint_as_float(a) + __uint_as_float(b); }
DI float xsum32(float v) { auto r = __builtin_amdgcn_permlane32_swap(__float_as_uint(v), __float_as_uint(v), false, false);
  const unsigned a = r[0], b = r[1]; return __uint_as_float(a) + __uint_as_float(b); }
DI float xmax16(float v) { auto r = __builtin_amdgcn_permlane16_swap(__float_as_uint(v), __float_as_uint(v), false, false);
  const unsigned a = r[0], b = r[1]; return fmaxf(__uint_as_float(a), __uint_as_float(b)); }
DI float xmax32(float v) { auto r = __builtin_amdgcn_permlane32_swap(__float_as_uint(v), __float_as_uint(v), false, false);
  const unsigned a = r[0], b = r[1]; return fmaxf(__uint_as_float(a), __uint_as_float(b)); }
DI unsigned nosent(unsigned v) { return min(v, 0xFFFFFFFEu); }
DI v2u nosent2(v2u o) { return v2u{nosent(o.x), nosent(o.y)}; }
DI v4u nosent4(v4u o) { return v4u{nosent(o.x), nosent(o.y), nosent(o.z), nosent(o.w)}; }
DI unsigned umax3(unsigned a, unsigned b, unsigned c) { return max(max(a, b), c); }
DI unsigned umax4(v4u r) { return max(umax3(r.x, r.y, r.z), r.w); }
#define KEEP4(x) asm volatile("" :: "v"(x))
#define BAR() do { asm volatile("s_waitcnt lgkmcnt(0)" ::: "memory"); __builtin_amdgcn_s_barrier(); asm volatile("" ::: "memory"); } while (0)
#define DRAIN() asm volatile("s_waitcnt vmcnt(0)" ::: "memory")
DI void sleep_units(int n) { for (int d = 0; d < n; ++d) __builtin_amdgcn_s_sleep(1); }
DI v4u ld_sc1(rsrc_t r, int off) { return __builtin_amdgcn_raw_buffer_load_b128(r, off, 0, AUX_SC1); }
DI unsigned ld1_sc1(rsrc_t r, int off) { return __builtin_amdgcn_raw_buffer_load_b32(r, off, 0, AUX_SC1); }
DI void st4_sc1(v4u v, rsrc_t r, int off) { __builtin_amdgcn_raw_buffer_store_b128(v, r, off, 0, AUX_SC1); }
DI void st2_sc1(v2u v, rsrc_t r, int off) { __builtin_amdgcn_raw_buffer_store_b64(v, r, off, 0, AUX_SC1); }
DI void st1_sc1(unsigned v, rsrc_t r, int off) { __builtin_amdgcn_raw_buffer_store_b32(v, r, off, 0, AUX_SC1); }
constexpr int FLAG_D = 32768;
DI unsigned arrive_issue(unsigned char* ws, int off) { return __hip_atomic_fetch_add((unsigned*)(ws + off), 1u, __ATOMIC_RELAXED, __HIP_MEMORY_SCOPE_AGENT); }
DI void arrive_finish(rsrc_t r, unsigned old, int off, unsigned total) { if (old == total - 1u) st1_sc1(1u, r, off + FLAG_D); }
DI void arrive(unsigned char* ws, rsrc_t r, int off, unsigned total) { arrive_finish(r, arrive_issue(ws, off), off, total); }
DI void ctr_wait(rsrc_t r, int off, unsigned want, int nap) {
  off += FLAG_D; want = 1u;
#pragma nounroll
  for (int spin = 0; spin < SPIN_MAX; ++spin) {
    asm volatile("" ::: "memory");
    const unsigned v = __builtin_amdgcn_readfirstlane(ld1_sc1(r, off));
    if (v >= want) break;
    sleep_units(nap);
  }
  asm volatile("" ::: "memory");
}

DI void kv_ensure(rsrc_t r, int hb, int& known, int nap) {
  if (hb < known) return;
  const int lane = threadIdx.x & 63;
#pragma nounroll
  for (int spin = 0; spin < SPIN_MAX; ++spin) {
    asm volatile("" ::: "memory");
    const unsigned v = ld1_sc1(r, lane < 32 ? CTR_KV + FLAG_D + lane * 128 : OOB);
    const unsigned long long m = __ballot(v != 0u);
    known = __builtin_ctzll(~m);
    if (hb < known) break;
    sleep_units(nap);
  }
  asm volatile("" ::: "memory");
}

struct Params {
  const u16 *x, *mem0, *Wq, *Wk, *Wv, *Wo, *wg, *wu, *wd;
  u16* out;
  unsigned char* ws;
};

DI float ln_den(float var) { return rbf(sqrtf(rbf(rbf(var) + 1e-5f))); }

#define TL(idx)
constexpr int PREP_LDS = 288 * 528;
constexpr int FILL_KB = (int)(FILL_BYTES / 1024);
__global__ void __launch_bounds__(512, 2) k_prep(Params p) {
  extern __shared__ __attribute__((aligned(16))) unsigned char lds[];
  const int tid = threadIdx.x, lane = tid & 63, kg = lane >> 4, n = lane & 15;
  const int w = __builtin_amdgcn_readfirstlane(tid >> 6);
  unsigned char* ws = p.ws;
#define TLP(idx)
  const int xg = blockIdx.x & 7, ix = blockIdx.x >> 3;
  const int b = ix < 28 ? (2 * xg + ix / 14) * 14 + ix % 14 : 224 + xg * 4 + (ix - 28), bb = b;
  TLP(0);
  const v4u sent = {SENT, SENT, SENT, SENT};
  if (bb < 224) {
    const int h = bb / 14, r = bb % 14, ab = r / 7, jb = r % 7;
    v4u sa[2][14], sb[2][4];
#pragma unroll
    for (int half = 0; half < 2; ++half) {
      const u16* asrc = (half ? p.Wk : p.Wv) + (size_t)(224 * ab) * 4096 + h * 256;
      if (half) {
        const u16* bsrc = p.Wq + (size_t)(64 * jb) * 4096 + h * 256;
#pragma unroll
        for (int i = 0; i < 4; ++i) { const int pc = i * 512 + tid; sb[1][i] = *(const v4u*)(bsrc + (size_t)(pc >> 5) * 4096 + (pc & 31) * 8); }
      } else {
        const u16* bsrc = p.Wo + (size_t)(h * 256) * 448 + 64 * jb;
#pragma unroll
        for (int i = 0; i < 4; ++i) { const int pc = i * 512 + tid; sb[0][i] = *(const v4u*)(bsrc + (size_t)(pc >> 3) * 448 + (pc & 7) * 8); }
      }
#pragma unroll
      for (int i = 0; i < 14; ++i) { const int pc = i * 512 + tid; sa[half][i] = *(const v4u*)(asrc + (size_t)(pc >> 5) * 4096 + (pc & 31) * 8); }
    }
    __builtin_amdgcn_sched_barrier(0);
    unsigned char* lb = lds + 224 * 528;
#pragma unroll
    for (int half = 0; half < 2; ++half) {
      if (half) {
        __syncthreads();
#pragma unroll
        for (int i = 0; i < 4; ++i) { const int pc = i * 512 + tid; *(v4u*)(lb + (pc >> 5) * 528 + (pc & 31) * 16) = sb[1][i]; }
      } else {
#pragma unroll
        for (int i = 0; i < 4; ++i) {
          const int pc = i * 512 + tid, e = pc >> 3, x0 = (pc & 7) * 8;
#pragma unroll
          for (int j = 0; j < 8; ++j) {
            const int x = x0 + j, m = x >> 5, xl = x & 31, cl = (xl >> 2) & 3;
            const int slot = (2 * m + (cl >> 1)) * 16 + (cl & 1) * 8 + (xl >> 4) * 4 + (xl & 3);
            const unsigned wv = sb[0][i][j >> 1];
            *(u16*)(lb + slot * 528 + e * 2) = (u16)((j & 1) ? (wv >> 16) : (wv & 0xFFFFu));
          }
        }
      }
#pragma unroll
      for (int i = 0; i < 14; ++i) { const int pc = i * 512 + tid; *(v4u*)(lds + (pc >> 5) * 528 + (pc & 31) * 16) = sa[half][i]; }
      __syncthreads();
      TLP(1 + 2 * half);
      if (w < 7) {
        const int ks = ab * 7 + w;
        const unsigned char* pa = lds + (32 * w + n) * 528 + kg * 16;
        const unsigned char* pb = lb + n * 528 + kg * 16;
        f32x4 acc[4][2];
#pragma unroll
        for (int t = 0; t < 4; ++t) { acc[t][0] = f32x4{0, 0, 0, 0}; acc[t][1] = f32x4{0, 0, 0, 0}; }
#pragma unroll
        for (int ke = 0; ke < 8; ++ke) {
          const v4u A0 = *(const v4u*)(pa + ke * 64), A1 = *(const v4u*)(pa + 16 * 528 + ke * 64);
#pragma unroll
          for (int t = 0; t < 4; ++t) {
            const v4u B = *(const v4u*)(pb + t * 16 * 528 + ke * 64);
            acc[t][0] = mfma32(A0, B, acc[t][0]); acc[t][1] = mfma32(A1, B, acc[t][1]);
          }
        }
#pragma unroll
        for (int t = 0; t < 4; ++t) {
          const int tile = (half ? 0 : 448) + h * 28 + jb * 4 + t;
          *(v4u*)(ws + OFF_WC + ((size_t)tile * 14 + ks) * 1024 + lane * 16) = pk8(acc[t][0], acc[t][1]);
        }
      }
      TLP(2 + 2 * half);
    }
    return;
  }
#pragma unroll
  for (int half = 0; half < 2; ++half)
  {
    const int row0 = half * 1024 + (bb - 224) * 32 + w * 4;
    const int c = lane < 56 ? lane : 0;
    const int ks = c >> 2, kc = c & 3;
    v2u a4[4], b4[4];
#pragma unroll
    for (int rr = 0; rr < 4; ++rr) {
      a4[rr] = *(const v2u*)(p.x + (size_t)(row0 + rr) * 448 + 32 * ks + 4 * kc);
      b4[rr] = *(const v2u*)(p.x + (size_t)(row0 + rr) * 448 + 32 * ks + 16 + 4 * kc);
    }
#pragma unroll
    for (int rr = 0; rr < 4; ++rr) {
      const int row = row0 + rr;
      const v2u a = a4[rr], b2 = b4[rr];
      float v[8] = {bflo(a.x), bfhi(a.x), bflo(a.y), bfhi(a.y), bflo(b2.x), bfhi(b2.x), bflo(b2.y), bfhi(b2.y)};
      float s = 0.f;
#pragma unroll
      for (int j = 0; j < 8; ++j) s += v[j];
      if (lane >= 56) s = 0.f;
#pragma unroll
      for (int o = 1; o < 64; o <<= 1) s += __shfl_xor(s, o);
      const float mean = s * (1.f / 448.f);
      float q = 0.f;
#pragma unroll
      for (int j = 0; j < 8; ++j) { float d = v[j] - mean; q += d * d; }
      if (lane >= 56) q = 0.f;
#pragma unroll
      for (int o = 1; o < 64; o <<= 1) q += __shfl_xor(q, o);
      const float den = ln_den(q * (1.f / 448.f)), mb = rbf(mean);
      float o8[8];
#pragma unroll
      for (int j = 0; j < 8; ++j) o8[j] = rbf(v[j] - mb) / den;
      if (lane < 56)
        *(v4u*)(ws + OFF_XN + ((size_t)(row >> 4) * 14 + ks) * 1024 + (kc * 16 + (row & 15)) * 16) =
            v4u{pk2(o8[0], o8[1]), pk2(o8[2], o8[3]), pk2(o8[4], o8[5]), pk2(o8[6], o8[7])};
    }
  }
  TLP(4);
  if (b == 224) {
    if (w < 7) {
      const int pc = w * 64 + lane;
      const int c = (pc >> 4) * 2 + ((pc >> 2) & 1), row = ((pc >> 3) & 1) * 4 + (pc & 3);
      const int ks = c >> 2, kc = c & 3;
      v2u a = *(const v2u*)(p.mem0 + (size_t)row * 448 + 32 * ks + 4 * kc);
      v2u b2 = *(const v2u*)(p.mem0 + (size_t)row * 448 + 32 * ks + 16 + 4 * kc);
      const v4u v = v4u{nosent(a.x), nosent(a.y), nosent(b2.x), nosent(b2.y)};
      *(v4u*)(ws + OFF_EX + pc * 16) = v;
      *(v4u*)(ws + OFF_EX + 8192 + pc * 16) = v;
    } else {
      *(v4u*)(ws + OFF_EX + 7168 + lane * 16) = sent;
      *(v4u*)(ws + OFF_EX + 8192 + 7168 + lane * 16) = sent;
    }
  }
  if (b == 225) {
#pragma unroll
    for (int i = 0; i < 8; ++i) *(v4u*)(ws + OFF_CTR + (size_t)(i * 512 + tid) * 16) = v4u{0, 0, 0, 0};
  }
}
#define STAMP(k)
#define STAMPV(k, v)
#define STAMP_INIT()
#define STAMP_DUMP()
constexpr int L_MEM = 0;
constexpr int L_P1 = 16384;
constexpr int L_YB = 24576;
constexpr int N_S = 56, N_O = 56, N_X = 8, N_CHAIN = N_S + N_O + N_X;
constexpr int L_TAB2 = 22528;
constexpr int L_VOTE = 23808;
#define L_DUMMY (14336 + lane * 16)
#define ex_off(s, c) __builtin_amdgcn_readfirstlane(*(const int*)(smem + L_TAB2 + ((s) * 5 + (c)) * 4))
#define T_A 110
#define T_B 102
#define T_SW 26
#define T_DA 0
#define T_DB 0
#define T_R2 8
#define T_R1 0
#define T_Q1 15
#define T_Q2 70
#define PFD 4
#define NREP 4
#define T_OG 0
#define T_PEN 35
#define T_UP 2
#define T_OKN 1000
constexpr int L_SCH = 23872;
DI unsigned rt32() { unsigned long long t; asm volatile("s_memrealtime %0\n\ts_waitcnt lgkmcnt(0)" : "=s"(t) :: "memory"); return (unsigned)t; }
DI unsigned wait_until(unsigned t) {
  int d = (int)(t - rt32());
  if (d < -2) return t - d;
  if (d > 3000) d = 3000;
  if (d > 24) { sleep_units(((d - 16) * 3) >> 3); d = (int)(t - rt32()); }
  if (d > 0 && d < 64) sleep_units((d * 3) >> 3);
  return t;
}
struct Sched { unsigned tE1, tE2; int m1, m2, ok, nf, late; };
DI void sched_step(Sched& y, unsigned word, int lane, int s) {
  if (s == 0) { y.tE1 = rt32(); y.tE2 = y.tE1; return; }
  int dv = lane < NREP ? (int)((word & ~3u) - (y.tE1 << 2)) >> 2 : -0x40000000;
  unsigned fl = lane < NREP ? (word & 3u) : 0u;
  if (s > 1 && __all(lane >= NREP || word == (y.tE1 << 2))) { dv = 0; fl = 0; }
  else {
#pragma unroll
    for (int o = 1; o < NREP; o <<= 1) { dv = max(dv, __shfl_xor(dv, o)); fl |= (unsigned)__shfl_xor((int)fl, o); }
    dv = __builtin_amdgcn_readfirstlane(dv); fl = __builtin_amdgcn_readfirstlane(fl);
  }
  unsigned base = y.tE2;
  if (dv > 0 || s == 1) base = y.tE1 + dv + T_B + y.m2 + T_PEN;
  if (s > 2 && !y.late) {
    if (fl & 1u) { y.m2 += T_UP; y.ok = 0; }
    else if (fl & 2u) { y.m1 += T_UP; y.ok = 0; }
    else if (++y.ok >= T_OKN) { y.ok = 0; y.m1 -= 1; y.m2 -= 1; }
  }
  y.late = dv > 0 ? 1 : 0;
  if (s == T_SW) { y.m1 -= T_DA; y.m2 -= T_DB; }
  y.tE1 = base + T_A + y.m1;
  y.tE2 = y.tE1 + T_B + y.m2;
}
#define FAIL_DUMP(v)

DI int gather_poll(__amdgpu_buffer_rsrc_t rx, unsigned char* dst0, unsigned char* dst1, int o0, int o1, int ot, unsigned& word) {
  int spin = 0;
#pragma nounroll
  for (;;) {
    asm volatile("" ::: "memory");
    const v4u v0 = ld_sc1(rx, o0), v1 = ld_sc1(rx, o1);
    word = __builtin_amdgcn_raw_buffer_load_b32(rx, ot, 0, AUX_SC1);
    *(v4u*)dst0 = v0;
    *(v4u*)dst1 = v1;
    if (__builtin_expect(__all(umax3(umax4(v0), umax4(v1), word) != SENT), 1)) break;
    if (spin == 0) { o0 ^= 8192; o1 ^= 8192; ot ^= 8192; sleep_units(T_R2); }
    if (++spin >= SPIN_MAX) break;
    __builtin_amdgcn_s_sleep(2);
  }
  return spin;
}

DI void probe_place(const Params& p, const rsrc_t rx) {
  unsigned char* ws = p.ws;
  const int lane = threadIdx.x & 63, w = __builtin_amdgcn_readfirstlane(threadIdx.x >> 6);
  const unsigned a0 = (unsigned)(size_t)(ws + OFF_EX);
    if (w >= 4) {
      const int r = w - 4;
      int pr = -1;
      for (int k = 0; k < 64; ++k) if (pr < 0 && (int)(((a0 + (unsigned)k * 81920u) >> 21) - (a0 >> 21)) == r) pr = k * 5;
      unsigned res = 2;
      if (pr >= 0) {
        const int base = (int)OFF_EX + pr * 16384 + 7168;
        const v4u sent = {SENT, SENT, SENT, SENT};
        __builtin_amdgcn_raw_buffer_store_b128(sent, rx, base + lane * 16, 0, AUX_SC1);
        __builtin_amdgcn_raw_buffer_store_b128(sent, rx, base + 8192 + lane * 16, 0, AUX_SC1);
        asm volatile("s_waitcnt vmcnt(0)" ::: "memory");
        unsigned best[2] = {0xFFFFFFFFu, 0xFFFFFFFFu};
#pragma unroll
        for (int k = 0; k < 8; ++k) {
          unsigned long long t0, t1;
          asm volatile("s_memtime %0\n\ts_waitcnt lgkmcnt(0)" : "=s"(t0) :: "memory");
          unsigned v = __builtin_amdgcn_raw_buffer_load_b32(rx, base + (k & 1) * 8192 + (k >> 1) * 128, 0, AUX_SC1);
          asm volatile("s_waitcnt vmcnt(0)" :: "v"(v) : "memory");
          asm volatile("s_memtime %0\n\ts_waitcnt lgkmcnt(0)" : "=s"(t1) :: "memory");
          best[k & 1] = min(best[k & 1], (unsigned)(t1 - t0));
        }
        const unsigned b13 = ((a0 + (unsigned)pr * 16384u) >> 13) & 1;
        res = 2 + ((best[1] < best[0]) ? (b13 ^ 1) : b13);
      }
      if (lane == 0) __builtin_amdgcn_raw_buffer_store_b32(res, rx, (int)OFF_PTAB + r * 4, 0, AUX_SC1);
    }
}

DI void chain_role(const Params& p, unsigned char* smem, const rsrc_t rx, const int cu) {
  unsigned char* ws = p.ws;
  const int tid = threadIdx.x, lane = tid & 63, w = __builtin_amdgcn_readfirstlane(tid >> 6);
  const int kg = lane >> 4, n = lane & 15;
  const float sc = 0.0625f * 1.4426950408889634f;
  STAMP_INIT();
  auto startup = [&]() {
  {
    const v4u sent = {SENT, SENT, SENT, SENT};
    constexpr int EX_KB = (int)(64 * EX_STEP / 1024);
    for (int i = 16 + cu * 8 + w; i < EX_KB; i += N_CHAIN * 8) st4_sc1(sent, rx, (int)OFF_EX + i * 1024 + lane * 16);
    if (cu * 8 + w < 448) st4_sc1(sent, rx, (cu * 8 + w < 224 ? (int)OFF_KM + 62 * 114688 + (cu * 8 + w) * 1024 : (int)OFF_VM + 62 * 114688 + (cu * 8 + w - 224) * 1024) + lane * 16);
    DRAIN();
    __syncthreads();
    if (tid == 448) arrive(ws, rx, CTR_FX, (unsigned)N_CHAIN);
  }
  {
    const unsigned a0 = (unsigned)(size_t)(ws + OFF_EX);
    if (w == 0) {
      unsigned v = 0;
      for (int spin = 0; spin < SPIN_MAX; ++spin) {
        asm volatile("" ::: "memory");
        v = __builtin_amdgcn_raw_buffer_load_b32(rx, (int)OFF_PTAB + (lane & 3) * 4, 0, AUX_SC1);
        if (__all(v != 0)) break;
        __builtin_amdgcn_s_sleep(4);
      }
      ctr_wait(rx, CTR_FX, (unsigned)N_CHAIN, 4);
      if (lane < 4) *(unsigned*)(smem + L_VOTE + lane * 4) = v;
      if (lane < 2) *(unsigned*)(smem + L_SCH + lane * 4) = 0u;
    }
    __syncthreads();
    const unsigned a = a0 + (unsigned)tid * 16384u;
    const int reg = (int)((a >> 21) - (a0 >> 21)) & 3, b13 = (a >> 13) & 1;
    if (tid < 320) { const int nearbit = *(const int*)(smem + L_VOTE + reg * 4) & 1; *(int*)(smem + L_TAB2 + tid * 4) = (int)OFF_EX + tid * 16384 + ((nearbit != b13) ? 8192 : 0); }
    __syncthreads();
  }
  };
  const int lmoff = (kg >> 1) * 256 + ((n & 7) >> 2) * 128 + (kg & 1) * 64 + (n & 3) * 16;
  const int goff = (w - 4) * 2048 + lane * 16;
  if (cu < N_S) {
    const int c = cu, m7 = c / 7, r7 = c % 7;
    if (w < 4) {
      const int tile = 8 * c + 2 * w, hS = tile / 28, ksj = (tile % 28) >> 1;
      v4u Mf[2][KS];
#pragma unroll
      for (int q = 0; q < 2; ++q)
#pragma unroll
        for (int ks = 0; ks < KS; ++ks) Mf[q][ks] = *(const v4u*)(ws + OFF_WC + ((size_t)(tile + q) * 14 + ks) * 1024 + lane * 16);
      startup();
#pragma unroll
      for (int ks = 0; ks < KS; ++ks) { KEEP4(Mf[0][ks]); KEEP4(Mf[1][ks]); }
      unsigned char* pdst = smem + ((kg < 2 && n < 8) ? L_P1 + (kg * 8 + n) * 16 + w * 4 : L_DUMMY);
      const int kmo = (int)OFF_KM + ((hS * 14 + ksj) * 4 + kg) * 128 + n * 16;
#pragma nounroll
      for (int s = 0; s < NSEG; ++s) {
        const unsigned char* lm = smem + L_MEM + (s & 1) * 7168 + lmoff;
        BAR();
        STAMP(2);
        v4u mf[KS + 1];
#pragma unroll
        for (int ks = 0; ks < PFD; ++ks) mf[ks] = *(const v4u*)(lm + ks * 512);
        __builtin_amdgcn_sched_barrier(0);
        f32x4 a0 = {0, 0, 0, 0}, a1 = {0, 0, 0, 0};
#pragma unroll
        for (int ks = 0; ks < KS; ++ks) {
          if (ks + PFD < KS) mf[ks + PFD] = *(const v4u*)(lm + (ks + PFD) * 512);
          if (ks + PFD == KS) mf[KS] = *(const v4u*)(lm + ksj * 512);
          a0 = mfma32(Mf[0][ks], mf[ks], a0); a1 = mfma32(Mf[1][ks], mf[ks], a1);
          __builtin_amdgcn_sched_barrier(0);
        }
        const v4u piece = mf[KS];
        const v4u tb = pk8(a0, a1);
        const f32x4 d2 = mfma32(tb, piece, f32x4{0, 0, 0, 0});
        *(float*)(pdst) = d2.x; *(float*)(pdst + 256) = d2.y; *(float*)(pdst + 512) = d2.z; *(float*)(pdst + 768) = d2.w;
        STAMP(3);
        BAR();
        st4_sc1(nosent4(tb), rx, n < 8 ? kmo + s * (16 * 14 * 512) : OOB);
        DRAIN();
      }
      BAR();
    } else {
      startup();
      Sched sd = {0u, 0u, 0, 0, 0, 0, 1};
      const bool strad = r7 == 3;
      const int ii = lane & 7, key = ((lane >> 3) & 1) * 4 + (lane >> 4);
      const int hh = 2 * m7 + (r7 > 3 ? 1 : 0), pp = r7 > 3 ? r7 - 3 : r7;
      const int so1 = ((hh * 4 + pp) * 4 + (ii & 3)) * 32 + key * 4;
      const int so2 = (((2 * m7 + 1) * 4 + 0) * 4 + (ii & 3)) * 32 + key * 4;
      const unsigned char* psrc = smem + L_P1 + lane * 16;
#pragma nounroll
      for (int s = 0; s < NSEG; ++s) {
        const int o0 = ex_off(s, 0) + goff;
        const int eo2 = ex_off(s, 2), eo4 = ex_off(s, 4);
        const int eb = (ii >> 2) ? eo4 : eo2;
        const int sto1 = (w == 4 && s < NSEG - 1) ? eb + so1 : OOB;
        const int sto2 = (w == 4 && s < NSEG - 1 && strad) ? eb + so2 : OOB;
        unsigned char* d0 = smem + L_MEM + (s & 1) * 7168 + goff;
        unsigned char* d1 = w == 7 ? smem + L_DUMMY : d0 + 1024;
        const int ot = (s > 0 && lane < NREP) ? o0 - goff + 7168 + lane * 128 : OOB;
        unsigned word;
        KEEP4(sto1); KEEP4(sto2);
        if (s > 1) wait_until(sd.tE2);
        else if (s == 1) { int kn = 0; kv_ensure(rx, 0, kn, 8); sleep_units(T_Q2); }
        STAMP(0);
        const int spin = gather_poll(rx, d0, d1, o0, w == 7 ? OOB : o0 + 1024, ot, word);
        BAR();
        STAMP(1);
        if (w == 7) TL(s);
        BAR();
        const f32x4 a = *(const f32x4*)psrc;
        const float u0 = a.x + a.y, u1 = a.z + a.w;
        const float v1 = strad ? u0 : u0 + u1;
        st1_sc1(nosent(__float_as_uint(v1)), rx, sto1);
        st1_sc1(nosent(__float_as_uint(u1)), rx, sto2);
        st1_sc1(nosent(__float_as_uint(v1)), rx, sto1 ^ 8192);
        st1_sc1(nosent(__float_as_uint(u1)), rx, sto2 ^ 8192);
        __builtin_amdgcn_sched_barrier(0);
        STAMP(5);
        STAMPV(10, spin);
        sched_step(sd, word, lane, s);
        sd.nf += (s > 1 && spin) ? 1 : 0;
        if (w == 6 && lane == 0 && s > 0) arrive(ws, rx, CTR_M + (s - 1) * 128, 84u);
      }
      BAR();
      if (w == 6 && lane == 0) arrive(ws, rx, CTR_M + (NSEG - 1) * 128, 84u);
      FAIL_DUMP(sd.nf);
    }
  } else if (cu < N_S + N_O) {
    const int o_ = cu - N_S, rg = o_ / 28, ct = o_ % 28;
    v4u Nf[2][KS];
#pragma unroll
    for (int q = 0; q < 2; ++q)
#pragma unroll
      for (int ks = 0; ks < KS; ++ks) Nf[q][ks] = *(const v4u*)(ws + OFF_WC + ((size_t)(448 + (2 * w + q) * 28 + ct) * 14 + ks) * 1024 + lane * 16);
    startup();
#pragma unroll
    for (int ks = 0; ks < KS; ++ks) { KEEP4(Nf[0][ks]); KEEP4(Nf[1][ks]); }
    auto publish_y = [&](int s) {
      const int h = 4 * (w - 4) + kg;
      const unsigned char* ys = smem + L_YB + (h * 4) * 64 + n * 4;
      const v4u y = v4u{*(const unsigned*)(ys), *(const unsigned*)(ys + 64), *(const unsigned*)(ys + 128), *(const unsigned*)(ys + 192)};
      st4_sc1(nosent4(y), rx, rg == 0 ? (int)OFF_VM + ((s * 16 + h) * 28 + ct) * 256 + n * 16 : OOB);
    };
    auto compute_y = [&](int s) {
      const unsigned char* lm = smem + L_MEM + (s & 1) * 7168 + lmoff;
      v4u mf[KS];
#pragma unroll
      for (int ks = 0; ks < PFD; ++ks) mf[ks] = *(const v4u*)(lm + ks * 512);
      __builtin_amdgcn_sched_barrier(0);
      f32x4 a0 = {0, 0, 0, 0}, a1 = {0, 0, 0, 0};
#pragma unroll
      for (int ks = 0; ks < KS; ++ks) {
        if (ks + PFD < KS) mf[ks + PFD] = *(const v4u*)(lm + (ks + PFD) * 512);
        a0 = mfma32(mf[ks], Nf[0][ks], a0); a1 = mfma32(mf[ks], Nf[1][ks], a1);
        __builtin_amdgcn_sched_barrier(0);
      }
      v2u y0 = pk4(a0), y1 = pk4(a1);
      unsigned char* yd = smem + (kg < 2 ? L_YB + ((2 * w) * 4 + 2 * kg) * 64 + n * 4 : L_DUMMY);
      *(unsigned*)(yd) = y0.x; *(unsigned*)(yd + 64) = y0.y;
      *(unsigned*)(yd + 256) = y1.x; *(unsigned*)(yd + 320) = y1.y;
    };
    if (w < 4) {
      int nf = 0;
      v4u Vf[4];
      int known = 0;
      unsigned aold = 0;
      kv_ensure(rx, 0, known, 4);
#pragma unroll
      for (int g2 = 0; g2 < 4; ++g2) Vf[g2] = ld_sc1(rx, (int)OFF_VP + ((4 * w + g2) * 28 + ct) * 1024 + lane * 16);
      const int g = n >> 2, i4 = n & 3, hP = 4 * w + g;
      unsigned char* pdst = smem + L_P1 + (kg * 4 + i4) * 80 + (w * 4 + g) * 4;
      const unsigned char* ys = smem + L_YB + ((4 * w) * 4 + 2 * (kg & 1) + (kg >> 1)) * 64 + n * 4;
#pragma nounroll
      for (int s = 0; s < NSEG; ++s) {
        const int eb = ex_off(s, 1 + 2 * rg);
        int oe = eb + (hP * 4 + i4) * 64 + kg * 16;
        int ol = eb + 4096 + (hP * 4 + i4) * 4;
        int om = ex_off(s, 2 + 2 * rg) + ((hP * 4 + 2 * (kg >> 1)) * 4 + i4) * 32 + (kg & 1) * 16;
        KEEP4(oe); KEEP4(ol); KEEP4(om);
        BAR();
        STAMP(2);
        compute_y(s);
#pragma unroll
        for (int g2 = 0; g2 < 4; ++g2) KEEP4(Vf[g2]);
        if (w == 3 && lane == 0 && rg == 0 && s > 0) arrive_finish(rx, aold, CTR_M + (s - 1) * 128, 84u);
        if (s == NSEG - 1) break;
        unsigned tiss;
        for (;;) {
          const v2u tv = *(volatile const v2u*)(smem + L_SCH);
          tiss = __builtin_amdgcn_readfirstlane(tv.x);
          if (__builtin_amdgcn_readfirstlane(tv.y) == (unsigned)(s + 1)) break;
        }
        STAMP(3);
        if (s > 0) tiss = wait_until(tiss);
        else { sleep_units(T_Q1); tiss = rt32(); }
        STAMP(6);
        int spin = 0;
        f32x4 dres;
        v4u Yf = v4u{0, 0, 0, 0};
#pragma nounroll
        for (;;) {
          asm volatile("" ::: "memory");
          const v4u q0 = ld_sc1(rx, om), q1 = ld_sc1(rx, om + 128);
          const unsigned ls = __builtin_amdgcn_raw_buffer_load_b32(rx, ol, 0, AUX_SC1);
          const v4u ex = ld_sc1(rx, oe);
          __builtin_amdgcn_sched_barrier(0);
          if (spin == 0) {
            BAR();
            Yf = v4u{*(const unsigned*)(ys), *(const unsigned*)(ys + 256), *(const unsigned*)(ys + 512), *(const unsigned*)(ys + 768)};
            KEEP4(Yf);
          }
          __builtin_amdgcn_sched_barrier(0);
          const f32x4 sq = __builtin_bit_cast(f32x4, q0) + __builtin_bit_cast(f32x4, q1);
          auto ra = __builtin_amdgcn_permlane32_swap(__float_as_uint(sq.x), __float_as_uint(sq.z), false, false);
          auto rb = __builtin_amdgcn_permlane32_swap(__float_as_uint(sq.y), __float_as_uint(sq.w), false, false);
          const unsigned ra0 = ra[0], ra1 = ra[1], rb0 = rb[0], rb1 = rb[1];
          const float sm0 = __uint_as_float(ra0) + __uint_as_float(ra1), sm1 = __uint_as_float(rb0) + __uint_as_float(rb1);
          const float lse = __uint_as_float(ls);
          const float mx = xmax32(xmax16(fmaxf(sm0, sm1)));
          const float nb = -fmaxf(mx * sc, lse);
          const float e0 = __builtin_amdgcn_exp2f(__builtin_fmaf(sm0, sc, nb)), e1 = __builtin_amdgcn_exp2f(__builtin_fmaf(sm1, sc, nb));
          const float wx = __builtin_amdgcn_exp2f(lse + nb);
          const unsigned pk = pk2(e0, e1);
          const v4u pb = v4u{g == 0 ? pk : 0u, g == 1 ? pk : 0u, g == 2 ? pk : 0u, g == 3 ? pk : 0u};
          const f32x4 dx0 = mfma32(Vf[0], ex, f32x4{0, 0, 0, 0});
          const f32x4 dx1 = mfma32(Vf[1], ex, f32x4{0, 0, 0, 0});
          const f32x4 dx2 = mfma32(Vf[2], ex, f32x4{0, 0, 0, 0});
          const f32x4 dx3 = mfma32(Vf[3], ex, f32x4{0, 0, 0, 0});
          const f32x4 dm = mfma32(Yf, pb, f32x4{0, 0, 0, 0});
          const float inv = __builtin_amdgcn_rcpf(wx + xsum32(xsum16(e0 + e1)));
          const unsigned mxu = umax3(umax3(umax4(q0), umax4(q1), ls), umax4(ex), 0u);
          const f32x4 dxa = g < 2 ? (g == 0 ? dx0 : dx1) : (g == 2 ? dx2 : dx3);
          dres = (dxa * wx + dm) * inv;
          *(float*)(pdst) = dres.x; *(float*)(pdst + 1280) = dres.y; *(float*)(pdst + 2560) = dres.z; *(float*)(pdst + 3840) = dres.w;
          if (__builtin_expect(__all(mxu != SENT), 1)) break;
          if (spin == 0) { oe ^= 8192; ol ^= 8192; om ^= 8192; sleep_units(T_R1); } else __builtin_amdgcn_s_sleep(2);
          if (++spin >= SPIN_MAX) break;
          tiss = rt32();
        }
        *(unsigned*)(smem + L_SCH + 16 + w * 4) = (tiss << 2) | (spin ? 2u : 0u);
        DRAIN();
        STAMP(8);
        BAR();
        STAMPV(11, spin);
        nf += (s > 1 && spin) ? 1 : 0;
        if (w == 3 && lane == 0 && rg == 0) aold = arrive_issue(ws, CTR_M + s * 128);
        const int sn = s + 1 < NSEG - 1 ? s + 1 : s;
        kv_ensure(rx, sn >> 1, known, 4);
#pragma unroll
        for (int g2 = 0; g2 < 4; ++g2) Vf[g2] = ld_sc1(rx, (int)OFF_VP + (sn * 448 + (4 * w + g2) * 28 + ct) * 1024 + lane * 16);
      }
      BAR();
      BAR();
      FAIL_DUMP(nf);
    } else {
      Sched sd = {0u, 0u, 0, 0, 0, 0, 1};
      const unsigned char* pp = smem + L_P1 + lane * 80;
      const bool st = w == 4 && (kg & 1) == 0;
      const int eoff = ct * 256 + rg * 128 + ((((n >> 3) & 1) * 4 + (n & 3)) * 8 + ((n >> 2) & 1) * 4 + kg) * 2;
#pragma nounroll
      for (int s = 0; s < NSEG; ++s) {
        const int o0 = ex_off(s, 0) + goff;
        unsigned char* d0 = smem + L_MEM + (s & 1) * 7168 + goff;
        unsigned char* d1 = w == 7 ? smem + L_DUMMY : d0 + 1024;
        const int ot = (s > 0 && lane < NREP) ? o0 - goff + 7168 + lane * 128 : OOB;
        unsigned word;
        if (s > 1) wait_until(sd.tE2 + T_OG);
        else if (s == 1) sleep_units(8);
        STAMP(0);
        const int spin = gather_poll(rx, d0, d1, o0, w == 7 ? OOB : o0 + 1024, ot, word);
        __builtin_amdgcn_s_setprio(3);
        BAR();
        STAMP(1);
        sched_step(sd, word, lane, s);
        if (w == 4) *(volatile v2u*)(smem + L_SCH) = v2u{sd.tE1, (unsigned)(s + 1)};
        __builtin_amdgcn_s_setprio(0);

        sd.nf += (s > 1 && spin) ? 1 : 0;
        *(unsigned*)(smem + L_SCH + 32 + (w - 4) * 4) = (s > 1 && spin) ? 1u : 0u;
        compute_y(s);
        if (s == NSEG - 1) break;
        const int eon = ex_off(s + 1, 0);
        const int sto = st ? eon + eoff : ((w == 5 && lane == 0 && o_ % (56 / NREP) == 0) ? eon + 7168 + (o_ / (56 / NREP)) * 128 : OOB);
        BAR();
        publish_y(s);
        const v4u gf = *(const v4u*)(smem + L_SCH + 32);
        const unsigned gfl = gf.x | gf.y | gf.z | gf.w;
        const unsigned tref = sd.tE1 << 2;
        KEEP4(sto); KEEP4(gfl);
        DRAIN();
        if (w == 4) {
          BAR();
          STAMP(4);
          const f32x4 a = (*(const f32x4*)(pp) + *(const f32x4*)(pp + 16)) + (*(const f32x4*)(pp + 32) + *(const f32x4*)(pp + 48));
          const float v = (a.x + a.y) + (a.z + a.w);
          auto r = __builtin_amdgcn_permlane16_swap(__float_as_uint(v), __float_as_uint(v), false, false);
          const unsigned r0 = r[0], r1 = r[1];
          const unsigned ov = nosent(pk2(__uint_as_float(r0), __uint_as_float(r1)));
          st1_sc1(ov, rx, sto);
          st1_sc1(ov, rx, sto ^ 8192);
        } else {
          BAR();
          const v4u tw = *(const v4u*)(smem + L_SCH + 16);
          const unsigned tm = tref + (unsigned)max(max((int)(tw.x - tref), (int)(tw.y - tref)), max((int)(tw.z - tref), (int)(tw.w - tref)));
          const unsigned wd = nosent(tm | ((tw.x | tw.y | tw.z | tw.w) & 3u) | gfl);
          st1_sc1(wd, rx, sto);
          st1_sc1(wd, rx, sto ^ 8192);
        }
        __builtin_amdgcn_sched_barrier(0);
        STAMP(5);
        STAMPV(10, spin);
      }
      BAR();
      publish_y(NSEG - 1);
      DRAIN();
      BAR();
      if (w == 6 && lane == 0 && rg == 0) arrive(ws, rx, CTR_M + (NSEG - 1) * 128, 84u);
      FAIL_DUMP(w == 7 ? sd.m1 * 1000 + sd.m2 : sd.nf);
    }
  } else {
    startup();
    if (w < 2) {
      const int h = 2 * (cu - N_S - N_O) + w;
      v4u K[2][KS];
      int known = 0;
      kv_ensure(rx, 0, known, 4);
#pragma unroll
      for (int u = 0; u < 2; ++u)
#pragma unroll
        for (int ks = 0; ks < KS; ++ks) K[u][ks] = ld_sc1(rx, (int)OFF_KP + ((h * 2 + u) * 14 + ks) * 1024 + lane * 16);
#pragma nounroll
      for (int s = 0; s < NSEG - 1; ++s) {
        const int eo1 = ex_off(s, 1), eo3 = ex_off(s, 3);
        const int eb = ((n >> 2) & 1) ? eo3 : eo1;
        const int so = n < 8 ? eb + (h * 4 + (n & 3)) * 64 + kg * 16 : OOB;
        const int lo = lane < 8 ? eb + 4096 + (h * 4 + (n & 3)) * 4 : OOB;
        const unsigned char* lm = smem + L_MEM + (s & 1) * 7168 + lmoff;
#pragma unroll
        for (int ks = 0; ks < KS; ++ks) { KEEP4(K[0][ks]); KEEP4(K[1][ks]); }
        BAR();
        STAMP(2);
        v4u mf[KS];
#pragma unroll
        for (int ks = 0; ks < PFD; ++ks) mf[ks] = *(const v4u*)(lm + ks * 512);
        __builtin_amdgcn_sched_barrier(0);
        f32x4 a0 = {0, 0, 0, 0}, a1 = {0, 0, 0, 0};
#pragma unroll
        for (int ks = 0; ks < KS; ++ks) {
          if (ks + PFD < KS) mf[ks + PFD] = *(const v4u*)(lm + (ks + PFD) * 512);
          a0 = mfma32(K[0][ks], mf[ks], a0);
          a1 = mfma32(K[1][ks], mf[ks], a1);
          __builtin_amdgcn_sched_barrier(0);
        }
        float xs[8] = {rbf(a0.x), rbf(a0.y), rbf(a0.z), rbf(a0.w), rbf(a1.x), rbf(a1.y), rbf(a1.z), rbf(a1.w)};
        float mx = xs[0];
#pragma unroll
        for (int j = 1; j < 8; ++j) mx = fmaxf(mx, xs[j]);
        mx = xmax32(xmax16(mx));
        const float nb = -mx * sc;
        float sum = 0.f;
#pragma unroll
        for (int j = 0; j < 8; ++j) { xs[j] = __builtin_amdgcn_exp2f(__builtin_fmaf(xs[j], sc, nb)); sum += xs[j]; }
        sum = xsum32(xsum16(sum));
        const float inv = __builtin_amdgcn_rcpf(sum);
        v4u o = v4u{pk2(xs[0] * inv, xs[1] * inv), pk2(xs[2] * inv, xs[3] * inv), pk2(xs[4] * inv, xs[5] * inv), pk2(xs[6] * inv, xs[7] * inv)};
        o = nosent4(o);
        st4_sc1(o, rx, so);
        const float lse = __builtin_amdgcn_logf(sum) - nb;
        st1_sc1(nosent(__float_as_uint(lse)), rx, lo);
        st4_sc1(o, rx, so ^ 8192);
        st1_sc1(nosent(__float_as_uint(lse)), rx, lo ^ 8192);
        __builtin_amdgcn_sched_barrier(0);
        STAMP(5);
        const int sn = s + 1 < NSEG - 1 ? s + 1 : s;
        kv_ensure(rx, sn >> 1, known, 4);
#pragma unroll
        for (int u = 0; u < 2; ++u)
#pragma unroll
          for (int ks = 0; ks < KS; ++ks) K[u][ks] = ld_sc1(rx, (int)OFF_KP + (((sn * 16 + h) * 2 + u) * 14 + ks) * 1024 + lane * 16);
      }
    } else if (w < 4) {
#pragma nounroll
      for (int s = 0; s < NSEG - 1; ++s) BAR();
    } else {
      Sched sd = {0u, 0u, 0, 0, 0, 0, 1};
#pragma nounroll
      for (int s = 0; s < NSEG - 1; ++s) {
        const int o0 = ex_off(s, 0) + goff;
        unsigned char* d0 = smem + L_MEM + (s & 1) * 7168 + goff;
        unsigned char* d1 = w == 7 ? smem + L_DUMMY : d0 + 1024;
        const int ot = (s > 0 && lane < NREP) ? o0 - goff + 7168 + lane * 128 : OOB;
        unsigned word;
        if (s > 1) wait_until(sd.tE2);
        else if (s == 1) { int kn = 0; kv_ensure(rx, 0, kn, 8); sleep_units(T_Q2); }
        STAMP(0);
        const int spin = gather_poll(rx, d0, d1, o0, w == 7 ? OOB : o0 + 1024, ot, word);
        BAR();
        STAMP(1);
        STAMPV(10, spin);
        sched_step(sd, word, lane, s);
        sd.nf += (s > 1 && spin) ? 1 : 0;
      }
      FAIL_DUMP(sd.nf);
    }
  }
  STAMP_DUMP();
}
DI void phase_kv(const Params& p, unsigned char* lds, rsrc_t rw, int cg, int par) {
  unsigned char* ws = p.ws;
  const int tid = threadIdx.x, lane = tid & 63, w = __builtin_amdgcn_readfirstlane(tid >> 6);
  const int nt0 = cg * 16 + 2 * w;
  v4u st[7];
  {
    const unsigned char* src = ws + OFF_XN + (size_t)par * 57344 + tid * 16;
#pragma unroll
    for (int i = 0; i < 7; ++i) st[i] = *(const v4u*)(src + i * 8192);
  }
  v4u W[2][KS];
#pragma unroll
  for (int q = 0; q < 2; ++q)
#pragma unroll
    for (int ks = 0; ks < KS; ++ks) W[q][ks] = *(const v4u*)(ws + OFF_WC + ((size_t)(nt0 + q) * 14 + ks) * 1024 + lane * 16);
#pragma unroll
  for (int i = 0; i < 7; ++i) *(v4u*)(lds + i * 8192 + tid * 16) = st[i];
#pragma unroll
  for (int ks = 0; ks < KS; ++ks) { KEEP4(W[0][ks]); KEEP4(W[1][ks]); }
  BAR();
  const bool isK = cg < 28;
  const int kbase = (int)OFF_KP + (((nt0 / 28) * 2) * 14 + ((nt0 % 28) >> 1)) * 1024 + lane * 16;
  const int vbase = (int)OFF_VP + (nt0 - 448) * 1024 + lane * 16;
  int pend = -1;
  unsigned aold = 0;
#pragma nounroll
  for (int it = 0; it < 16; ++it) {
    const int hb = par + 2 * it;
    const unsigned char* cur = lds + (it & 1) * 57344 + lane * 16;
    {
      const int so = (int)OFF_XN + (it + 1 < 16 ? hb + 2 : hb) * 57344;
#pragma unroll
      for (int i = 0; i < 7; ++i) st[i] = __builtin_amdgcn_raw_buffer_load_b128(rw, tid * 16, so + i * 8192, 0);
    }
    __builtin_amdgcn_sched_barrier(0);
    f32x4 acc[4][2];
#pragma unroll
    for (int rt = 0; rt < 4; ++rt) { acc[rt][0] = f32x4{0, 0, 0, 0}; acc[rt][1] = f32x4{0, 0, 0, 0}; }
    if (isK) {
      v4u af[2][4];
#pragma unroll
      for (int rt = 0; rt < 4; ++rt) af[0][rt] = *(const v4u*)(cur + (rt * 14) * 1024);
#pragma unroll
      for (int ks = 0; ks < KS; ++ks) {
        if (ks + 1 < KS) {
#pragma unroll
          for (int rt = 0; rt < 4; ++rt) af[(ks + 1) & 1][rt] = *(const v4u*)(cur + (rt * 14 + ks + 1) * 1024);
        }
        __builtin_amdgcn_sched_barrier(0);
#pragma unroll
        for (int rt = 0; rt < 4; ++rt) {
          acc[rt][0] = mfma32(W[0][ks], af[ks & 1][rt], acc[rt][0]);
          acc[rt][1] = mfma32(W[1][ks], af[ks & 1][rt], acc[rt][1]);
        }
        __builtin_amdgcn_sched_barrier(0);
      }
    } else {
      v4u af[2][4];
#pragma unroll
      for (int rt = 0; rt < 4; ++rt) af[0][rt] = *(const v4u*)(cur + (rt * 14) * 1024);
#pragma unroll
      for (int ks = 0; ks < KS; ++ks) {
        if (ks + 1 < KS) {
#pragma unroll
          for (int rt = 0; rt < 4; ++rt) af[(ks + 1) & 1][rt] = *(const v4u*)(cur + (rt * 14 + ks + 1) * 1024);
        }
        __builtin_amdgcn_sched_barrier(0);
#pragma unroll
        for (int rt = 0; rt < 4; ++rt) {
          acc[rt][0] = mfma32(af[ks & 1][rt], W[0][ks], acc[rt][0]);
          acc[rt][1] = mfma32(af[ks & 1][rt], W[1][ks], acc[rt][1]);
        }
        __builtin_amdgcn_sched_barrier(0);
      }
    }
    __builtin_amdgcn_sched_barrier(0);
#pragma unroll
    for (int i = 0; i < 7; ++i) *(v4u*)(lds + ((it + 1) & 1) * 57344 + i * 8192 + tid * 16) = st[i];
    DRAIN();
    if (pend >= 0 && lane == 0) arrive_finish(rw, aold, pend, 56u);
    pend = -1;
    __builtin_amdgcn_sched_barrier(0);
    if (isK) {
#pragma unroll
      for (int rt = 0; rt < 4; ++rt) st4_sc1(pk8(acc[rt][0], acc[rt][1]), rw, kbase + ((hb * 2 + (rt >> 1)) * 32 + (rt & 1)) * 14336);
    } else {
#pragma unroll
      for (int q = 0; q < 2; ++q)
#pragma unroll
        for (int sg = 0; sg < 2; ++sg) st4_sc1(pk8(acc[2 * sg][q], acc[2 * sg + 1][q]), rw, vbase + ((hb * 2 + sg) * 448 + q) * 1024);
    }
    if (it == 0 || it == 15) DRAIN();
    BAR();
    if (w == 0) TL(200 + it);
    if (w == (it & 7)) {
      if (it == 0 || it == 15) { if (lane == 0) arrive(ws, rw, CTR_KV + hb * 128, 56u); }
      if (it >= 2) {
        if (it == 15) { if (lane == 0) arrive(ws, rw, CTR_KV + (hb - 2) * 128, 56u); }
        else { pend = CTR_KV + (hb - 2) * 128; if (lane == 0) aold = arrive_issue(ws, pend); }
      }
    }
  }
}

DI void gate_wait(rsrc_t rw, int rt, int nap, int lag, int own) {
  int off = (int)OFF_PM + rt * 4096;
  if (__builtin_amdgcn_readfirstlane(ld1_sc1(rw, off)) != SENT) {
    off = own; lag = 4; nap = 8;
    if (__builtin_amdgcn_readfirstlane(ld1_sc1(rw, off)) != SENT) return;
  }
#pragma nounroll
  for (int spin = 0; spin < SPIN_MAX; ++spin) {
    sleep_units(nap);
    asm volatile("" ::: "memory");
    if (__builtin_amdgcn_readfirstlane(ld1_sc1(rw, off)) != SENT) break;
  }
  sleep_units(lag);
}
#define LAG_A2 10
#define LAG_F1 65
#define LAG_F2 140
#define RETRY 6
#define NAPT 4
#define NAP 6

#define TLX(k) do { if (rt == 127 || rt == 96) TL((rt == 127 ? 130 : 140) + (k)); } while (0)
DI void stage_a1(const Params& p, rsrc_t rw, int h) {
  unsigned char* ws = p.ws;
  const int tid = threadIdx.x, lane = tid & 63, w = __builtin_amdgcn_readfirstlane(tid >> 6);
  const int kg = lane >> 4, n = lane & 15, v = w & 1;
  const float sc = 0.0625f * 1.4426950408889634f;
  const float NEG = -3.0e38f;
  int known = 0;
  ctr_wait(rw, CTR_FX, 1u, NAP);
#pragma nounroll
  for (int rt = w; rt < 128; rt += 8) {
    const int s = rt >> 1;
    v4u xf[KS], kf[KS], k1[KS];
    kv_ensure(rw, s >> 1, known, NAP);
    const int kb = (int)OFF_KP + ((s * 16 + h) * 2) * 14336 + lane * 16;
#pragma unroll
    for (int ks = 0; ks < KS; ++ks) {
      xf[ks] = *(const v4u*)(ws + OFF_XN + ((size_t)rt * 14 + ks) * 1024 + lane * 16);
      kf[ks] = ld_sc1(rw, kb + ks * 1024);
    }
#pragma unroll
    for (int ks = 0; ks < KS; ++ks) k1[ks] = ld_sc1(rw, v ? kb + 14336 + ks * 1024 : OOB);
    __builtin_amdgcn_sched_barrier(0);
    f32x4 a0 = {0, 0, 0, 0}, a1 = {0, 0, 0, 0}, am = {0, 0, 0, 0};
#pragma unroll
    for (int ks = 0; ks < KS; ++ks) a0 = mfma32(kf[ks], xf[ks], a0);
#pragma unroll
    for (int ks = 0; ks < KS; ++ks) a1 = mfma32(k1[ks], xf[ks], a1);
    float xs[8];
#pragma unroll
    for (int r = 0; r < 4; ++r) {
      const bool diag = 4 * kg + r <= n;
      xs[r] = (v || diag) ? rbf(a0[r]) : NEG;
      xs[4 + r] = (v && diag) ? rbf(a1[r]) : NEG;
    }
    float mx = NEG;
#pragma unroll
    for (int j = 0; j < 8; ++j) mx = fmaxf(mx, xs[j]);
    const int tb = (int)OFF_KM + (s * 16 + h) * 7168 + kg * 128 + (n & 7) * 16;
    TLX(3);
    ctr_wait(rw, CTR_M + (s < NSEG - 2 ? s : s - 2) * 128, 84u, NAP);
    TLX(0);
#pragma nounroll
    for (int spin = 0; spin < SPIN_MAX; ++spin) {
      asm volatile("" ::: "memory");
      TLX(1);
      unsigned mxu = 0;
#pragma unroll
      for (int ks = 0; ks < KS; ++ks) kf[ks] = ld_sc1(rw, tb + ks * 512);
      if (s < NSEG - 2) break;
#pragma unroll
      for (int ks = 0; ks < KS; ++ks) mxu = max(mxu, umax4(kf[ks]));
      if (__all(mxu != SENT)) break;
      sleep_units(3);
    }
#pragma unroll
    for (int ks = 0; ks < KS; ++ks) am = mfma32(kf[ks], xf[ks], am);
    float mm[4];
#pragma unroll
    for (int r = 0; r < 4; ++r) { mm[r] = rbf(am[r]); mx = fmaxf(mx, mm[r]); }
    mx = xmax32(xmax16(mx));
    const float nb = -mx * sc;
    float sum = 0.f;
#pragma unroll
    for (int j = 0; j < 8; ++j) { xs[j] = xs[j] == NEG ? 0.f : __builtin_amdgcn_exp2f(__builtin_fmaf(xs[j], sc, nb)); sum += xs[j]; }
#pragma unroll
    for (int r = 0; r < 4; ++r) { mm[r] = __builtin_amdgcn_exp2f(__builtin_fmaf(mm[r], sc, nb)); if (kg < 2) sum += mm[r]; }
    sum = xsum32(xsum16(sum));
    const float inv = 1.f / sum;
    st4_sc1(nosent4(v4u{pk2(xs[0] * inv, xs[1] * inv), pk2(xs[2] * inv, xs[3] * inv), pk2(xs[4] * inv, xs[5] * inv), pk2(xs[6] * inv, xs[7] * inv)}),
            rw, (int)OFF_PX + (rt * 16 + h) * 1024 + lane * 16);
    st2_sc1(nosent2(v2u{pk2(mm[0] * inv, mm[1] * inv), pk2(mm[2] * inv, mm[3] * inv)}), rw,
            kg < 2 ? (int)OFF_PM + (rt * 4 + (h >> 2)) * 1024 + ((h & 3) * 16 + n) * 16 + kg * 8 : OOB);
    TL(rt);
  }
}

DI void stage_a2(const Params& p, rsrc_t rw, int ct) {
  const int tid = threadIdx.x, lane = tid & 63, w = __builtin_amdgcn_readfirstlane(tid >> 6);
  const int kg = lane >> 4, n = lane & 15;
  const int c = 2 * ct + (kg >> 1);
  const int col = 32 * (c >> 2) + 16 * (kg & 1) + 4 * (c & 3);
  const int hoff = (c >> 2) * 1024 + ((c & 3) * 16 + n) * 16 + (kg & 1) * 8;
  int known = 0;
  ctr_wait(rw, CTR_FX, 1u, NAP);
#pragma nounroll
  for (int rt = w; rt < 128; rt += 8) {
    const int s = rt >> 1;
    const v2u xv = *(const v2u*)(p.x + (size_t)(rt * 16 + n) * 448 + col);
    kv_ensure(rw, s >> 1, known, NAP);
    v4u vf[16], yf[4];
#pragma unroll
    for (int h = 0; h < 16; ++h) vf[h] = ld_sc1(rw, (int)OFF_VP + (s * 448 + h * 28 + ct) * 1024 + lane * 16);
    TLX(3);
    ctr_wait(rw, CTR_M + (s < NSEG - 2 ? s : s - 2) * 128, 84u, NAP);
    const int pb = (int)OFF_PX + rt * 16384 + lane * 16, mb = (int)OFF_PM + rt * 4096 + lane * 16;
    gate_wait(rw, rt, NAPT, LAG_A2, (int)OFF_PX + (rt * 16 + (ct & 15)) * 1024);
    TLX(0);
    f32x4 acc;
#pragma nounroll
    for (int spin = 0; spin < SPIN_MAX; ++spin) {
      asm volatile("" ::: "memory");
      TLX(1);
      v4u px[16], pm[4];
#pragma unroll
      for (int q = 0; q < 4; ++q) yf[q] = ld_sc1(rw, (int)OFF_VM + ((s * 16 + 4 * q + kg) * 28 + ct) * 256 + n * 16);
#pragma unroll
      for (int h = 0; h < 16; ++h) px[h] = ld_sc1(rw, pb + h * 1024);
#pragma unroll
      for (int q = 0; q < 4; ++q) pm[q] = ld_sc1(rw, mb + q * 1024);
      unsigned mxu = 0;
      f32x4 a0 = {0, 0, 0, 0}, a1 = {0, 0, 0, 0};
#pragma unroll
      for (int h = 0; h < 16; h += 2) {
        mxu = umax3(mxu, umax4(px[h]), umax4(px[h + 1]));
        a0 = mfma32(vf[h], px[h], a0); a1 = mfma32(vf[h + 1], px[h + 1], a1);
      }
#pragma unroll
      for (int q = 0; q < 4; q += 2) {
        mxu = umax3(mxu, umax3(umax4(pm[q]), umax4(pm[q + 1]), umax4(yf[q])), umax4(yf[q + 1]));
        a0 = mfma32(yf[q], pm[q], a0); a1 = mfma32(yf[q + 1], pm[q + 1], a1);
      }
      acc = a0 + a1;
      if (__all(mxu != SENT)) break;
      sleep_units(RETRY);
    }
    const float h0 = bflo(xv.x) + rbf(acc.x), h1 = bfhi(xv.x) + rbf(acc.y), h2 = bflo(xv.y) + rbf(acc.z), h3 = bfhi(xv.y) + rbf(acc.w);
    st2_sc1(nosent2(v2u{pk2(h0, h1), pk2(h2, h3)}), rw, (int)OFF_HF + rt * 14336 + hoff);
    TL(rt);
  }
}

DI void stage_f1(const Params& p, unsigned char* lds, rsrc_t rw, int kf) {
  const int tid = threadIdx.x, lane = tid & 63, w = __builtin_amdgcn_readfirstlane(tid >> 6);
  const int kg = lane >> 4, n = lane & 15;
  for (int f = w; f < 56; f += 8) {
    const int t = f / 14, ks = f % 14;
    const u16* src = ((t >> 1) ? p.wu : p.wg) + 32 * kf + 16 * (t & 1) + n;
    unsigned e[8];
#pragma unroll
    for (int jj = 0; jj < 8; ++jj) e[jj] = src[(size_t)pidx(ks, kg, jj) * 1792];
    *(v4u*)(lds + f * 1024 + lane * 16) = v4u{e[0] | (e[1] << 16), e[2] | (e[3] << 16), e[4] | (e[5] << 16), e[6] | (e[7] << 16)};
  }
  BAR();
  const v4u ones = {0x3F803F80u, 0x3F803F80u, 0x3F803F80u, 0x3F803F80u};
  f32x4 cs[4];
#pragma unroll
  for (int t = 0; t < 4; ++t) {
    cs[t] = f32x4{0, 0, 0, 0};
#pragma unroll
    for (int ks = 0; ks < KS; ++ks) cs[t] = mfma32(*(const v4u*)(lds + (t * 14 + ks) * 1024 + lane * 16), ones, cs[t]);
  }
#pragma nounroll
  for (int rt = w; rt < 128; rt += 8) {
    const int hb = (int)OFF_HF + rt * 14336 + lane * 16;
    gate_wait(rw, rt, NAPT, LAG_F1, (int)OFF_HF + rt * 14336 + (kf >> 1) * 512);
    TLX(0);
    v4u hf[KS];
#pragma nounroll
    for (int spin = 0; spin < SPIN_MAX; ++spin) {
      asm volatile("" ::: "memory");
      TLX(1);
      unsigned mxu = 0;
#pragma unroll
      for (int ks = 0; ks < KS; ++ks) hf[ks] = ld_sc1(rw, hb + ks * 1024);
#pragma unroll
      for (int ks = 0; ks < KS; ++ks) mxu = max(mxu, umax4(hf[ks]));
      if (__all(mxu != SENT)) break;
      sleep_units(RETRY);
    }
    TLX(2);
    f32x4 acc[4], sm = {0, 0, 0, 0}, gr = {0, 0, 0, 0};
#pragma unroll
    for (int t = 0; t < 4; ++t) acc[t] = f32x4{0, 0, 0, 0};
    v4u wf[2][4];
#pragma unroll
    for (int t = 0; t < 4; ++t) wf[0][t] = *(const v4u*)(lds + (t * 14) * 1024 + lane * 16);
#pragma unroll
    for (int ks = 0; ks < KS; ++ks) {
      if (ks + 1 < KS) {
#pragma unroll
        for (int t = 0; t < 4; ++t) wf[(ks + 1) & 1][t] = *(const v4u*)(lds + (t * 14 + ks + 1) * 1024 + lane * 16);
      }
      sm = mfma32(ones, hf[ks], sm);
      gr = mfma32(hf[ks], hf[ks], gr);
      __builtin_amdgcn_sched_barrier(0);
#pragma unroll
      for (int t = 0; t < 4; ++t) acc[t] = mfma32(wf[ks & 1][t], hf[ks], acc[t]);
      __builtin_amdgcn_sched_barrier(0);
    }
    float dg = (kg == (n >> 2)) ? ((n & 2) ? ((n & 1) ? gr.w : gr.z) : ((n & 1) ? gr.y : gr.x)) : 0.f;
    dg = xsum32(xsum16(dg));
    const float mean = sm.x * (1.f / 448.f);
    const float var = fmaxf(dg * (1.f / 448.f) - mean * mean, 0.f);
    const float mb = rbf(mean), rden = 1.f / ln_den(var);
    f32x4 o0, o1;
#pragma unroll
    for (int r = 0; r < 4; ++r) {
      o0[r] = rbf((acc[0][r] - mb * cs[0][r]) * rden) * rbf((acc[2][r] - mb * cs[2][r]) * rden);
      o1[r] = rbf((acc[1][r] - mb * cs[1][r]) * rden) * rbf((acc[3][r] - mb * cs[3][r]) * rden);
    }
    st4_sc1(nosent4(pk8(o0, o1)), rw, (int)OFF_AF + (rt * 56 + kf) * 1024 + lane * 16);
    TL(rt);
  }
}

DI void stage_f2(const Params& p, unsigned char* lds, rsrc_t rw, int ct) {
  const int tid = threadIdx.x, lane = tid & 63, w = __builtin_amdgcn_readfirstlane(tid >> 6);
  const int kg = lane >> 4, n = lane & 15;
  for (int kf = w; kf < 56; kf += 8) {
    const u16* src = p.wd + 16 * ct + n;
    unsigned e[8];
#pragma unroll
    for (int jj = 0; jj < 8; ++jj) e[jj] = src[(size_t)pidx(kf, kg, jj) * 448];
    *(v4u*)(lds + kf * 1024 + lane * 16) = v4u{e[0] | (e[1] << 16), e[2] | (e[3] << 16), e[4] | (e[5] << 16), e[6] | (e[7] << 16)};
  }
  BAR();
#pragma nounroll
  for (int rt = w; rt < 128; rt += 8) {
    const int ab = (int)OFF_AF + rt * 57344 + lane * 16;
    gate_wait(rw, rt, NAPT, LAG_F2, (int)OFF_AF + rt * 57344 + (2 * ct) * 1024);
    TLX(0);
    v2u hv = v2u{0, 0};
    f32x4 a0, a1;
#pragma nounroll
    for (int spin = 0; spin < SPIN_MAX; ++spin) {
      asm volatile("" ::: "memory");
      TLX(1);
      a0 = f32x4{0, 0, 0, 0}; a1 = f32x4{0, 0, 0, 0};
      hv = __builtin_amdgcn_raw_buffer_load_b64(rw, (int)OFF_HF + rt * 14336 + (ct >> 1) * 1024 + lane * 16 + (ct & 1) * 8, 0, AUX_SC1);
      v4u af[3][14];
#pragma unroll
      for (int bb = 0; bb < 2; ++bb)
#pragma unroll
        for (int k = 0; k < 14; ++k) af[bb][k] = ld_sc1(rw, ab + (bb * 14 + k) * 1024);
      unsigned mxu = max(hv.x, hv.y);
#pragma unroll
      for (int bb = 0; bb < 4; ++bb) {
        if (bb + 2 < 4) {
#pragma unroll
          for (int k = 0; k < 14; ++k) af[(bb + 2) % 3][k] = ld_sc1(rw, ab + ((bb + 2) * 14 + k) * 1024);
        }
        __builtin_amdgcn_sched_barrier(0);
#pragma unroll
        for (int k = 0; k < 14; k += 2) {
          mxu = umax3(mxu, umax4(af[bb % 3][k]), umax4(af[bb % 3][k + 1]));
          a0 = mfma32(*(const v4u*)(lds + (bb * 14 + k) * 1024 + lane * 16), af[bb % 3][k], a0);
          a1 = mfma32(*(const v4u*)(lds + (bb * 14 + k + 1) * 1024 + lane * 16), af[bb % 3][k + 1], a1);
        }
        __builtin_amdgcn_sched_barrier(0);
      }
      if (__all(mxu != SENT)) break;
      sleep_units(RETRY);
    }
    const f32x4 d = a0 + a1;
    const float o0 = bflo(hv.x) + rbf(d.x), o1 = bfhi(hv.x) + rbf(d.y), o2 = bflo(hv.y) + rbf(d.z), o3 = bfhi(hv.y) + rbf(d.w);
    *(v2u*)(p.out + (size_t)(rt * 16 + n) * 448 + 16 * ct + 4 * kg) = v2u{pk2(o0, o1), pk2(o2, o3)};
    TL(rt);
  }
}

DI void arm_stage(const Params& p, rsrc_t rw, int k, int nwg, int off0, int nkb, int ctr) {
  const int tid = threadIdx.x, lane = tid & 63, w = __builtin_amdgcn_readfirstlane(tid >> 6);
  const v4u sent = {SENT, SENT, SENT, SENT};
  for (int i = k * 8 + w; i < nkb; i += nwg * 8) st4_sc1(sent, rw, off0 + i * 1024 + lane * 16);
  DRAIN();
  __syncthreads();
  if (tid == 0) arrive(p.ws, rw, ctr, (unsigned)nwg);
}

constexpr int MAIN_LDS = 114688;
__global__ void __launch_bounds__(512, 2) k_main(Params p) {
  extern __shared__ __attribute__((aligned(16))) unsigned char smem[];
  const rsrc_t rw = __builtin_amdgcn_make_buffer_rsrc((void*)p.ws, 0, (int)WS_END, 0x00020000);
  const int b = blockIdx.x;
  if (threadIdx.x < 64) TL(255);
  const int j = (b >> 3) * 4 + (b & 3);
  if ((b & 7) < 4) {
    if (j < N_CHAIN) chain_role(p, smem, rw, j);
    else { if (j == N_CHAIN) probe_place(p, rw); sleep_units(400); arm_stage(p, rw, j - N_CHAIN, 8, (int)OFF_HF, (int)((OFF_XN - OFF_HF) / 1024), CTR_FS + 128); }
    return;
  }
  if (j < 112) phase_kv(p, smem, rw, j % 56, j / 56);
  else arm_stage(p, rw, j - 112, 16, (int)OFF_PX, (int)((OFF_HF - OFF_PX) / 1024), CTR_FS);
  ctr_wait(rw, CTR_FS, 16u, 8);
  if (j < 112) ctr_wait(rw, CTR_FS + 128, 8u, 8);
  if (j < 56) stage_f1(p, smem, rw, j);
  else if (j < 84) stage_f2(p, smem, rw, j - 56);
  else if (j < 112) stage_a2(p, rw, j - 84);
  else stage_a1(p, rw, j - 112);
}

extern "C" void kernel_launch(void* const* d_in, const int* in_sizes, int n_in, void* d_out, int out_size, void* d_ws, size_t ws_size,
                              hipStream_t stream) {
  Params p;
  p.x = (const u16*)d_in[0]; p.mem0 = (const u16*)d_in[1]; p.Wq = (const u16*)d_in[2]; p.Wk = (const u16*)d_in[3];
  p.Wv = (const u16*)d_in[4]; p.Wo = (const u16*)d_in[5];
  p.wg = (const u16*)d_in[8]; p.wu = (const u16*)d_in[9]; p.wd = (const u16*)d_in[10];
  p.out = (u16*)d_out; p.ws = (unsigned char*)d_ws;
  (void)hipFuncSetAttribute((const void*)k_main, hipFuncAttributeMaxDynamicSharedMemorySize, MAIN_LDS);
  (void)hipFuncSetAttribute((const void*)k_prep, hipFuncAttributeMaxDynamicSharedMemorySize, PREP_LDS);
  k_prep<<<256, 512, PREP_LDS, stream>>>(p);
  k_main<<<256, 512, MAIN_LDS, stream>>>(p);
}
```

```cpp
#include <hip/hip_runtime.h>
#include <hip/hip_bf16.h>
#include <stdint.h>

typedef short bf16x8 __attribute__((ext_vector_type(8)));
typedef float f32x4 __attribute__((ext_vector_type(4)));
typedef float f32x2 __attribute__((ext_vector_type(2)));
typedef __bf16 bf2_t __attribute__((ext_vector_type(2)));
typedef unsigned v4u __attribute__((ext_vector_type(4)));
typedef unsigned v2u __attribute__((ext_vector_type(2)));
typedef unsigned short u16;
typedef __amdgpu_buffer_rsrc_t rsrc_t;
#define DI __device__ __forceinline__

constexpr int NSEG = 64, KS = 14;
constexpr unsigned SENT = 0xFFFFFFFFu;
constexpr int AUX_SC1 = 16;
constexpr int SPIN_MAX = 20000;
constexpr int OOB = 0x7FFFFFF0;

constexpr size_t OFF_CTR = 0;
constexpr int CTR_KV = 0, CTR_M = 4096, OFF_PTAB = 16384, CTR_FX = 16640, CTR_FS = 16896;
constexpr size_t OFF_EX = 65536;
constexpr size_t EX_STEP = 5 * 16384;
constexpr size_t OFF_PX = OFF_EX + 64 * EX_STEP;
constexpr size_t OFF_PM = OFF_PX + (size_t)128 * 16 * 1024;
constexpr size_t OFF_HF = OFF_PM + (size_t)128 * 4 * 1024;
constexpr size_t OFF_AF = OFF_HF + (size_t)128 * 14 * 1024;
constexpr size_t OFF_XN = OFF_AF + (size_t)128 * 56 * 1024;
constexpr size_t OFF_WC = OFF_XN + (size_t)128 * 14 * 1024;
constexpr size_t OFF_KP = OFF_WC + (size_t)896 * 14 * 1024;
constexpr size_t OFF_VP = OFF_KP + (size_t)64 * 16 * 2 * 14 * 1024;
constexpr size_t OFF_KM = OFF_VP + (size_t)64 * 448 * 1024;
constexpr size_t OFF_VM = OFF_KM + (size_t)64 * 16 * 14 * 512;
constexpr size_t WS_END = OFF_VM + (size_t)64 * 16 * 28 * 256;
constexpr size_t FILL_BYTES = OFF_XN - OFF_EX;
static_assert(WS_END < (size_t)200 * 1024 * 1024, "ws");
static_assert(FILL_BYTES % 16384 == 0, "fill");

DI f32x4 mfma32(v4u a, v4u b, f32x4 c) {
  return __builtin_amdgcn_mfma_f32_16x16x32_bf16(__builtin_bit_cast(bf16x8, a), __builtin_bit_cast(bf16x8, b), c, 0, 0, 0);
}
DI unsigned pk2(float a, float b) {
  f32x2 v = {a, b};
  return __builtin_bit_cast(unsigned, __builtin_convertvector(v, bf2_t));
}
DI v2u pk4(f32x4 a) { return v2u{pk2(a.x, a.y), pk2(a.z, a.w)}; }
DI v4u pk8(f32x4 a, f32x4 b) { return v4u{pk2(a.x, a.y), pk2(a.z, a.w), pk2(b.x, b.y), pk2(b.z, b.w)}; }
DI float bflo(unsigned u) { return __uint_as_float(u << 16); }
DI float bfhi(unsigned u) { return __uint_as_float(u & 0xFFFF0000u); }
DI float rbf(float a) { return bflo(pk2(a, 0.f) & 0xFFFFu); }
DI int picol(int c, int cc) { return 32 * (c >> 2) + 16 * (cc >> 2) + 4 * (c & 3) + (cc & 3); }
DI int pidx(int ks, int kg, int jj) { return 32 * ks + 16 * (jj >> 2) + 4 * kg + (jj & 3); }
DI float xsum16(float v) { auto r = __builtin_amdgcn_permlane16_swap(__float_as_uint(v), __float_as_uint(v), false, false);
  const unsigned a = r[0], b = r[1]; return __uint_as_float(a) + __uint_as_float(b); }
DI float xsum32(float v) { auto r = __builtin_amdgcn_permlane32_swap(__float_as_uint(v), __float_as_uint(v), false, false);
  const unsigned a = r[0], b = r[1]; return __uint_as_float(a) + __uint_as_float(b); }
DI float xmax16(float v) { auto r = __builtin_amdgcn_permlane16_swap(__float_as_uint(v), __float_as_uint(v), false, false);
  const unsigned a = r[0], b = r[1]; return fmaxf(__uint_as_float(a), __uint_as_float(b)); }
DI float xmax32(float v) { auto r = __builtin_amdgcn_permlane32_swap(__float_as_uint(v), __float_as_uint(v), false, false);
  const unsigned a = r[0], b = r[1]; return fmaxf(__uint_as_float(a), __uint_as_float(b)); }
DI unsigned nosent(unsigned v) { return min(v, 0xFFFFFFFEu); }
DI v2u nosent2(v2u o) { return v2u{nosent(o.x), nosent(o.y)}; }
DI v4u nosent4(v4u o) { return v4u{nosent(o.x), nosent(o.y), nosent(o.z), nosent(o.w)}; }
DI unsigned umax3(unsigned a, unsigned b, unsigned c) { return max(max(a, b), c); }
DI unsigned umax4(v4u r) { return max(umax3(r.x, r.y, r.z), r.w); }
#define KEEP4(x) asm volatile("" :: "v"(x))
#define BAR() do { asm volatile("s_waitcnt lgkmcnt(0)" ::: "memory"); __builtin_amdgcn_s_barrier(); asm volatile("" ::: "memory"); } while (0)
#define DRAIN() asm volatile("s_waitcnt vmcnt(0)" ::: "memory")
DI void sleep_units(int n) { for (int d = 0; d < n; ++d) __builtin_amdgcn_s_sleep(1); }
DI v4u ld_sc1(rsrc_t r, int off) { return __builtin_amdgcn_raw_buffer_load_b128(r, off, 0, AUX_SC1); }
DI unsigned ld1_sc1(rsrc_t r, int off) { return __builtin_amdgcn_raw_buffer_load_b32(r, off, 0, AUX_SC1); }
DI void st4_sc1(v4u v, rsrc_t r, int off) { __builtin_amdgcn_raw_buffer_store_b128(v, r, off, 0, AUX_SC1); }
DI void st2_sc1(v2u v, rsrc_t r, int off) { __builtin_amdgcn_raw_buffer_store_b64(v, r, off, 0, AUX_SC1); }
DI void st1_sc1(unsigned v, rsrc_t r, int off) { __builtin_amdgcn_raw_buffer_store_b32(v, r, off, 0, AUX_SC1); }
constexpr int FLAG_D = 32768;
DI unsigned arrive_issue(unsigned char* ws, int off) { return __hip_atomic_fetch_add((unsigned*)(ws + off), 1u, __ATOMIC_RELAXED, __HIP_MEMORY_SCOPE_AGENT); }
DI void arrive_finish(rsrc_t r, unsigned old, int off, unsigned total) { if (old == total - 1u) st1_sc1(1u, r, off + FLAG_D); }
DI void arrive(unsigned char* ws, rsrc_t r, int off, unsigned total) { arrive_finish(r, arrive_issue(ws, off), off, total); }
DI void ctr_wait(rsrc_t r, int off, unsigned want, int nap) {
  off += FLAG_D; want = 1u;
#pragma nounroll
  for (int spin = 0; spin < SPIN_MAX; ++spin) {
    asm volatile("" ::: "memory");
    const unsigned v = __builtin_amdgcn_readfirstlane(ld1_sc1(r, off));
    if (v >= want) break;
    sleep_units(nap);
  }
  asm volatile("" ::: "memory");
}

DI void kv_ensure(rsrc_t r, int hb, int& known, int nap) {
  if (hb < known) return;
  const int lane = threadIdx.x & 63;
#pragma nounroll
  for (int spin = 0; spin < SPIN_MAX; ++spin) {
    asm volatile("" ::: "memory");
    const unsigned v = ld1_sc1(r, lane < 32 ? CTR_KV + FLAG_D + lane * 128 : OOB);
    const unsigned long long m = __ballot(v != 0u);
    known = __builtin_ctzll(~m);
    if (hb < known) break;
    sleep_units(nap);
  }
  asm volatile("" ::: "memory");
}

struct Params {
  const u16 *x, *mem0, *Wq, *Wk, *Wv, *Wo, *wg, *wu, *wd;
  u16* out;
  unsigned char* ws;
};

DI float ln_den(float var) { return rbf(sqrtf(rbf(rbf(var) + 1e-5f))); }

#define TL(idx)
constexpr int PREP_LDS = 288 * 528;
constexpr int FILL_KB = (int)(FILL_BYTES / 1024);
__global__ void __launch_bounds__(512, 2) k_prep(Params p) {
  extern __shared__ __attribute__((aligned(16))) unsigned char lds[];
  const int tid = threadIdx.x, lane = tid & 63, kg = lane >> 4, n = lane & 15;
  const int w = __builtin_amdgcn_readfirstlane(tid >> 6);
  unsigned char* ws = p.ws;
#define TLP(idx)
  const int xg = blockIdx.x & 7, ix = blockIdx.x >> 3;
  const int b = ix < 28 ? (2 * xg + ix / 14) * 14 + ix % 14 : 224 + xg * 4 + (ix - 28), bb = b;
  TLP(0);
  const v4u sent = {SENT, SENT, SENT, SENT};
  if (bb < 224) {
    const int h = bb / 14, r = bb % 14, ab = r / 7, jb = r % 7;
    v4u sa[2][14], sb[2][4];
#pragma unroll
    for (int half = 0; half < 2; ++half) {
      const u16* asrc = (half ? p.Wk : p.Wv) + (size_t)(224 * ab) * 4096 + h * 256;
      if (half) {
        const u16* bsrc = p.Wq + (size_t)(64 * jb) * 4096 + h * 256;
#pragma unroll
        for (int i = 0; i < 4; ++i) { const int pc = i * 512 + tid; sb[1][i] = *(const v4u*)(bsrc + (size_t)(pc >> 5) * 4096 + (pc & 31) * 8); }
      } else {
        const u16* bsrc = p.Wo + (size_t)(h * 256) * 448 + 64 * jb;
#pragma unroll
        for (int i = 0; i < 4; ++i) { const int pc = i * 512 + tid; sb[0][i] = *(const v4u*)(bsrc + (size_t)(pc >> 3) * 448 + (pc & 7) * 8); }
      }
#pragma unroll
      for (int i = 0; i < 14; ++i) { const int pc = i * 512 + tid; sa[half][i] = *(const v4u*)(asrc + (size_t)(pc >> 5) * 4096 + (pc & 31) * 8); }
    }
    __builtin_amdgcn_sched_barrier(0);
    unsigned char* lb = lds + 224 * 528;
#pragma unroll
    for (int half = 0; half < 2; ++half) {
      if (half) {
        __syncthreads();
#pragma unroll
        for (int i = 0; i < 4; ++i) { const int pc = i * 512 + tid; *(v4u*)(lb + (pc >> 5) * 528 + (pc & 31) * 16) = sb[1][i]; }
      } else {
#pragma unroll
        for (int i = 0; i < 4; ++i) {
          const int pc = i * 512 + tid, e = pc >> 3, x0 = (pc & 7) * 8;
#pragma unroll
          for (int j = 0; j < 8; ++j) {
            const int x = x0 + j, m = x >> 5, xl = x & 31, cl = (xl >> 2) & 3;
            const int slot = (2 * m + (cl >> 1)) * 16 + (cl & 1) * 8 + (xl >> 4) * 4 + (xl & 3);
            const unsigned wv = sb[0][i][j >> 1];
            *(u16*)(lb + slot * 528 + e * 2) = (u16)((j & 1) ? (wv >> 16) : (wv & 0xFFFFu));
          }
        }
      }
#pragma unroll
      for (int i = 0; i < 14; ++i) { const int pc = i * 512 + tid; *(v4u*)(lds + (pc >> 5) * 528 + (pc & 31) * 16) = sa[half][i]; }
      __syncthreads();
      TLP(1 + 2 * half);
      if (w < 7) {
        const int ks = ab * 7 + w;
        const unsigned char* pa = lds + (32 * w + n) * 528 + kg * 16;
        const unsigned char* pb = lb + n * 528 + kg * 16;
        f32x4 acc[4][2];
#pragma unroll
        for (int t = 0; t < 4; ++t) { acc[t][0] = f32x4{0, 0, 0, 0}; acc[t][1] = f32x4{0, 0, 0, 0}; }
#pragma unroll
        for (int ke = 0; ke < 8; ++ke) {
          const v4u A0 = *(const v4u*)(pa + ke * 64), A1 = *(const v4u*)(pa + 16 * 528 + ke * 64);
#pragma unroll
          for (int t = 0; t < 4; ++t) {
            const v4u B = *(const v4u*)(pb + t * 16 * 528 + ke * 64);
            acc[t][0] = mfma32(A0, B, acc[t][0]); acc[t][1] = mfma32(A1, B, acc[t][1]);
          }
        }
#pragma unroll
        for (int t = 0; t < 4; ++t) {
          const int tile = (half ? 0 : 448) + h * 28 + jb * 4 + t;
          *(v4u*)(ws + OFF_WC + ((size_t)tile * 14 + ks) * 1024 + lane * 16) = pk8(acc[t][0], acc[t][1]);
        }
      }
      TLP(2 + 2 * half);
    }
    return;
  }
#pragma unroll
  for (int half = 0; half < 2; ++half)
  {
    const int row0 = half * 1024 + (bb - 224) * 32 + w * 4;
    const int c = lane < 56 ? lane : 0;
    const int ks = c >> 2, kc = c & 3;
    v2u a4[4], b4[4];
#pragma unroll
    for (int rr = 0; rr < 4; ++rr) {
      a4[rr] = *(const v2u*)(p.x + (size_t)(row0 + rr) * 448 + 32 * ks + 4 * kc);
      b4[rr] = *(const v2u*)(p.x + (size_t)(row0 + rr) * 448 + 32 * ks + 16 + 4 * kc);
    }
#pragma unroll
    for (int rr = 0; rr < 4; ++rr) {
      const int row = row0 + rr;
      const v2u a = a4[rr], b2 = b4[rr];
      float v[8] = {bflo(a.x), bfhi(a.x), bflo(a.y), bfhi(a.y), bflo(b2.x), bfhi(b2.x), bflo(b2.y), bfhi(b2.y)};
      float s = 0.f;
#pragma unroll
      for (int j = 0; j < 8; ++j) s += v[j];
      if (lane >= 56) s = 0.f;
#pragma unroll
      for (int o = 1; o < 64; o <<= 1) s += __shfl_xor(s, o);
      const float mean = s * (1.f / 448.f);
      float q = 0.f;
#pragma unroll
      for (int j = 0; j < 8; ++j) { float d = v[j] - mean; q += d * d; }
      if (lane >= 56) q = 0.f;
#pragma unroll
      for (int o = 1; o < 64; o <<= 1) q += __shfl_xor(q, o);
      const float den = ln_den(q * (1.f / 448.f)), mb = rbf(mean);
      float o8[8];
#pragma unroll
      for (int j = 0; j < 8; ++j) o8[j] = rbf(v[j] - mb) / den;
      if (lane < 56)
        *(v4u*)(ws + OFF_XN + ((size_t)(row >> 4) * 14 + ks) * 1024 + (kc * 16 + (row & 15)) * 16) =
            v4u{pk2(o8[0], o8[1]), pk2(o8[2], o8[3]), pk2(o8[4], o8[5]), pk2(o8[6], o8[7])};
    }
  }
  TLP(4);
  if (b == 224) {
    if (w < 7) {
      const int pc = w * 64 + lane;
      const int c = (pc >> 4) * 2 + ((pc >> 2) & 1), row = ((pc >> 3) & 1) * 4 + (pc & 3);
      const int ks = c >> 2, kc = c & 3;
      v2u a = *(const v2u*)(p.mem0 + (size_t)row * 448 + 32 * ks + 4 * kc);
      v2u b2 = *(const v2u*)(p.mem0 + (size_t)row * 448 + 32 * ks + 16 + 4 * kc);
      const v4u v = v4u{nosent(a.x), nosent(a.y), nosent(b2.x), nosent(b2.y)};
      *(v4u*)(ws + OFF_EX + pc * 16) = v;
      *(v4u*)(ws + OFF_EX + 8192 + pc * 16) = v;
    } else {
      *(v4u*)(ws + OFF_EX + 7168 + lane * 16) = sent;
      *(v4u*)(ws + OFF_EX + 8192 + 7168 + lane * 16) = sent;
    }
  }
  if (b == 225) {
#pragma unroll
    for (int i = 0; i < 8; ++i) *(v4u*)(ws + OFF_CTR + (size_t)(i * 512 + tid) * 16) = v4u{0, 0, 0, 0};
  }
}
#define STAMP(k)
#define STAMPV(k, v)
#define STAMP_INIT()
#define STAMP_DUMP()
constexpr int L_MEM = 0;
constexpr int L_P1 = 16384;
constexpr int L_YB = 24576;
constexpr int N_S = 56, N_O = 56, N_X = 8, N_CHAIN = N_S + N_O + N_X;
constexpr int L_TAB2 = 22528;
constexpr int L_VOTE = 23808;
#define L_DUMMY (14336 + lane * 16)
#define ex_off(s, c) __builtin_amdgcn_readfirstlane(*(const int*)(smem + L_TAB2 + ((s) * 5 + (c)) * 4))
#define T_A 106
#define T_B 100
#define T_SW 26
#define T_DA 0
#define T_DB 0
#define T_R2 8
#define T_R1 0
#define T_Q1 15
#define T_Q2 70
#define PFD 4
#define T_EX 30
#define T_EXN 4
#define NREP 4
#define T_OG 0
#define T_PEN 35
#define T_UP 2
#define T_OKN 1000
constexpr int L_SCH = 23872;
DI unsigned rt32() { unsigned long long t; asm volatile("s_memrealtime %0\n\ts_waitcnt lgkmcnt(0)" : "=s"(t) :: "memory"); return (unsigned)t; }
DI unsigned wait_until(unsigned t) {
  int d = (int)(t - rt32());
  if (d < -2) return t - d;
  if (d > 3000) d = 3000;
  if (d > 24) { sleep_units(((d - 16) * 3) >> 3); d = (int)(t - rt32()); }
  if (d > 0 && d < 64) sleep_units((d * 3) >> 3);
  return t;
}
struct Sched { unsigned tE1, tE2; int m1, m2, ok, nf, late; };
DI void sched_step(Sched& y, unsigned word, int lane, int s) {
  if (s == 0) { y.tE1 = rt32(); y.tE2 = y.tE1; return; }
  int dv = lane < NREP ? (int)((word & ~3u) - (y.tE1 << 2)) >> 2 : -0x40000000;
  unsigned fl = lane < NREP ? (word & 3u) : 0u;
  if (s > 1 && __all(lane >= NREP || word == (y.tE1 << 2))) { dv = 0; fl = 0; }
  else {
#pragma unroll
    for (int o = 1; o < NREP; o <<= 1) { dv = max(dv, __shfl_xor(dv, o)); fl |= (unsigned)__shfl_xor((int)fl, o); }
    dv = __builtin_amdgcn_readfirstlane(dv); fl = __builtin_amdgcn_readfirstlane(fl);
  }
  unsigned base = y.tE2;
  if (dv > 0 || s == 1) base = y.tE1 + dv + T_B + y.m2 + T_PEN;
  if (s > 2 && !y.late) {
    if (fl & 1u) { y.m2 += T_UP; y.ok = 0; }
    else if (fl & 2u) { y.m1 += T_UP; y.ok = 0; }
    else if (++y.ok >= T_OKN) { y.ok = 0; y.m1 -= 1; y.m2 -= 1; }
  }
  y.late = dv > 0 ? 1 : 0;
  if (s == T_SW) { y.m1 -= T_DA; y.m2 -= T_DB; }
  const int ex = s < T_EXN ? T_EX : 0;
  y.tE1 = base + T_A + y.m1 + ex;
  y.tE2 = y.tE1 + T_B + y.m2 + (ex >> 1);
}
#define FAIL_DUMP(v)

DI int gather_poll(__amdgpu_buffer_rsrc_t rx, unsigned char* dst0, unsigned char* dst1, int o0, int o1, int ot, unsigned& word) {
  int spin = 0;
#pragma nounroll
  for (;;) {
    asm volatile("" ::: "memory");
    const v4u v0 = ld_sc1(rx, o0), v1 = ld_sc1(rx, o1);
    word = __builtin_amdgcn_raw_buffer_load_b32(rx, ot, 0, AUX_SC1);
    *(v4u*)dst0 = v0;
    *(v4u*)dst1 = v1;
    if (__builtin_expect(__all(umax3(umax4(v0), umax4(v1), word) != SENT), 1)) break;
    if (spin == 0) { o0 ^= 8192; o1 ^= 8192; ot ^= 8192; sleep_units(T_R2); }
    if (++spin >= SPIN_MAX) break;
    __builtin_amdgcn_s_sleep(2);
  }
  return spin;
}

DI void probe_place(const Params& p, const rsrc_t rx) {
  unsigned char* ws = p.ws;
  const int lane = threadIdx.x & 63, w = __builtin_amdgcn_readfirstlane(threadIdx.x >> 6);
  const unsigned a0 = (unsigned)(size_t)(ws + OFF_EX);
    if (w >= 4) {
      const int r = w - 4;
      int pr = -1;
      for (int k = 0; k < 64; ++k) if (pr < 0 && (int)(((a0 + (unsigned)k * 81920u) >> 21) - (a0 >> 21)) == r) pr = k * 5;
      unsigned res = 2;
      if (pr >= 0) {
        const int base = (int)OFF_EX + pr * 16384 + 7168;
        const v4u sent = {SENT, SENT, SENT, SENT};
        __builtin_amdgcn_raw_buffer_store_b128(sent, rx, base + lane * 16, 0, AUX_SC1);
        __builtin_amdgcn_raw_buffer_store_b128(sent, rx, base + 8192 + lane * 16, 0, AUX_SC1);
        asm volatile("s_waitcnt vmcnt(0)" ::: "memory");
        unsigned best[2] = {0xFFFFFFFFu, 0xFFFFFFFFu};
#pragma unroll
        for (int k = 0; k < 8; ++k) {
          unsigned long long t0, t1;
          asm volatile("s_memtime %0\n\ts_waitcnt lgkmcnt(0)" : "=s"(t0) :: "memory");
          unsigned v = __builtin_amdgcn_raw_buffer_load_b32(rx, base + (k & 1) * 8192 + (k >> 1) * 128, 0, AUX_SC1);
          asm volatile("s_waitcnt vmcnt(0)" :: "v"(v) : "memory");
          asm volatile("s_memtime %0\n\ts_waitcnt lgkmcnt(0)" : "=s"(t1) :: "memory");
          best[k & 1] = min(best[k & 1], (unsigned)(t1 - t0));
        }
        const unsigned b13 = ((a0 + (unsigned)pr * 16384u) >> 13) & 1;
        res = 2 + ((best[1] < best[0]) ? (b13 ^ 1) : b13);
      }
      if (lane == 0) __builtin_amdgcn_raw_buffer_store_b32(res, rx, (int)OFF_PTAB + r * 4, 0, AUX_SC1);
    }
}

DI void chain_role(const Params& p, unsigned char* smem, const rsrc_t rx, const int cu) {
  unsigned char* ws = p.ws;
  const int tid = threadIdx.x, lane = tid & 63, w = __builtin_amdgcn_readfirstlane(tid >> 6);
  const int kg = lane >> 4, n = lane & 15;
  const float sc = 0.0625f * 1.4426950408889634f;
  STAMP_INIT();
  auto startup = [&]() {
  {
    const v4u sent = {SENT, SENT, SENT, SENT};
    constexpr int EX_KB = (int)(64 * EX_STEP / 1024);
    for (int i = 16 + cu * 8 + w; i < EX_KB; i += N_CHAIN * 8) st4_sc1(sent, rx, (int)OFF_EX + i * 1024 + lane * 16);
    if (cu * 8 + w < 448) st4_sc1(sent, rx, (cu * 8 + w < 224 ? (int)OFF_KM + 62 * 114688 + (cu * 8 + w) * 1024 : (int)OFF_VM + 62 * 114688 + (cu * 8 + w - 224) * 1024) + lane * 16);
    DRAIN();
    __syncthreads();
    if (tid == 448) arrive(ws, rx, CTR_FX, (unsigned)N_CHAIN);
  }
  {
    const unsigned a0 = (unsigned)(size_t)(ws + OFF_EX);
    if (w == 0) {
      unsigned v = 0;
      for (int spin = 0; spin < SPIN_MAX; ++spin) {
        asm volatile("" ::: "memory");
        v = __builtin_amdgcn_raw_buffer_load_b32(rx, (int)OFF_PTAB + (lane & 3) * 4, 0, AUX_SC1);
        if (__all(v != 0)) break;
        __builtin_amdgcn_s_sleep(4);
      }
      ctr_wait(rx, CTR_FX, (unsigned)N_CHAIN, 4);
      if (lane < 4) *(unsigned*)(smem + L_VOTE + lane * 4) = v;
      if (lane < 2) *(unsigned*)(smem + L_SCH + lane * 4) = 0u;
    }
    __syncthreads();
    const unsigned a = a0 + (unsigned)tid * 16384u;
    const int reg = (int)((a >> 21) - (a0 >> 21)) & 3, b13 = (a >> 13) & 1;
    if (tid < 320) { const int nearbit = *(const int*)(smem + L_VOTE + reg * 4) & 1; *(int*)(smem + L_TAB2 + tid * 4) = (int)OFF_EX + tid * 16384 + ((nearbit != b13) ? 8192 : 0); }
    __syncthreads();
  }
  };
  const int lmoff = (kg >> 1) * 256 + ((n & 7) >> 2) * 128 + (kg & 1) * 64 + (n & 3) * 16;
  const int goff = (w - 4) * 2048 + lane * 16;
  if (cu < N_S) {
    const int c = cu, m7 = c / 7, r7 = c % 7;
    if (w < 4) {
      const int tile = 8 * c + 2 * w, hS = tile / 28, ksj = (tile % 28) >> 1;
      v4u Mf[2][KS];
#pragma unroll
      for (int q = 0; q < 2; ++q)
#pragma unroll
        for (int ks = 0; ks < KS; ++ks) Mf[q][ks] = *(const v4u*)(ws + OFF_WC + ((size_t)(tile + q) * 14 + ks) * 1024 + lane * 16);
      startup();
#pragma unroll
      for (int ks = 0; ks < KS; ++ks) { KEEP4(Mf[0][ks]); KEEP4(Mf[1][ks]); }
      unsigned char* pdst = smem + ((kg < 2 && n < 8) ? L_P1 + (kg * 8 + n) * 16 + w * 4 : L_DUMMY);
      const int kmo = (int)OFF_KM + ((hS * 14 + ksj) * 4 + kg) * 128 + n * 16;
#pragma nounroll
      for (int s = 0; s < NSEG; ++s) {
        const unsigned char* lm = smem + L_MEM + (s & 1) * 7168 + lmoff;
        BAR();
        STAMP(2);
        v4u mf[KS + 1];
#pragma unroll
        for (int ks = 0; ks < PFD; ++ks) mf[ks] = *(const v4u*)(lm + ks * 512);
        __builtin_amdgcn_sched_barrier(0);
        f32x4 a0 = {0, 0, 0, 0}, a1 = {0, 0, 0, 0};
#pragma unroll
        for (int ks = 0; ks < KS; ++ks) {
          if (ks + PFD < KS) mf[ks + PFD] = *(const v4u*)(lm + (ks + PFD) * 512);
          if (ks + PFD == KS) mf[KS] = *(const v4u*)(lm + ksj * 512);
          a0 = mfma32(Mf[0][ks], mf[ks], a0); a1 = mfma32(Mf[1][ks], mf[ks], a1);
          __builtin_amdgcn_sched_barrier(0);
        }
        const v4u piece = mf[KS];
        const v4u tb = pk8(a0, a1);
        const f32x4 d2 = mfma32(tb, piece, f32x4{0, 0, 0, 0});
        *(float*)(pdst) = d2.x; *(float*)(pdst + 256) = d2.y; *(float*)(pdst + 512) = d2.z; *(float*)(pdst + 768) = d2.w;
        STAMP(3);
        BAR();
        st4_sc1(nosent4(tb), rx, n < 8 ? kmo + s * (16 * 14 * 512) : OOB);
        DRAIN();
      }
      BAR();
    } else {
      startup();
      Sched sd = {0u, 0u, 0, 0, 0, 0, 1};
      const bool strad = r7 == 3;
      const int ii = lane & 7, key = ((lane >> 3) & 1) * 4 + (lane >> 4);
      const int hh = 2 * m7 + (r7 > 3 ? 1 : 0), pp = r7 > 3 ? r7 - 3 : r7;
      const int so1 = ((hh * 4 + pp) * 4 + (ii & 3)) * 32 + key * 4;
      const int so2 = (((2 * m7 + 1) * 4 + 0) * 4 + (ii & 3)) * 32 + key * 4;
      const unsigned char* psrc = smem + L_P1 + lane * 16;
#pragma nounroll
      for (int s = 0; s < NSEG; ++s) {
        const int o0 = ex_off(s, 0) + goff;
        const int eo2 = ex_off(s, 2), eo4 = ex_off(s, 4);
        const int eb = (ii >> 2) ? eo4 : eo2;
        const int sto1 = (w == 4 && s < NSEG - 1) ? eb + so1 : OOB;
        const int sto2 = (w == 4 && s < NSEG - 1 && strad) ? eb + so2 : OOB;
        unsigned char* d0 = smem + L_MEM + (s & 1) * 7168 + goff;
        unsigned char* d1 = w == 7 ? smem + L_DUMMY : d0 + 1024;
        const int ot = (s > 0 && lane < NREP) ? o0 - goff + 7168 + lane * 128 : OOB;
        unsigned word;
        KEEP4(sto1); KEEP4(sto2);
        if (s > 1) wait_until(sd.tE2);
        else if (s == 1) { int kn = 0; kv_ensure(rx, 0, kn, 8); sleep_units(T_Q2); }
        STAMP(0);
        const int spin = gather_poll(rx, d0, d1, o0, w == 7 ? OOB : o0 + 1024, ot, word);
        BAR();
        STAMP(1);
        if (w == 7) TL(s);
        BAR();
        const f32x4 a = *(const f32x4*)psrc;
        const float u0 = a.x + a.y, u1 = a.z + a.w;
        const float v1 = strad ? u0 : u0 + u1;
        st1_sc1(nosent(__float_as_uint(v1)), rx, sto1);
        st1_sc1(nosent(__float_as_uint(u1)), rx, sto2);
        st1_sc1(nosent(__float_as_uint(v1)), rx, sto1 ^ 8192);
        st1_sc1(nosent(__float_as_uint(u1)), rx, sto2 ^ 8192);
        __builtin_amdgcn_sched_barrier(0);
        STAMP(5);
        STAMPV(10, spin);
        sched_step(sd, word, lane, s);
        sd.nf += (s > 1 && spin) ? 1 : 0;
        if (w == 6 && lane == 0 && s > 0) arrive(ws, rx, CTR_M + (s - 1) * 128, 84u);
      }
      BAR();
      if (w == 6 && lane == 0) arrive(ws, rx, CTR_M + (NSEG - 1) * 128, 84u);
      FAIL_DUMP(sd.nf);
    }
  } else if (cu < N_S + N_O) {
    const int o_ = cu - N_S, rg = o_ / 28, ct = o_ % 28;
    v4u Nf[2][KS];
#pragma unroll
    for (int q = 0; q < 2; ++q)
#pragma unroll
      for (int ks = 0; ks < KS; ++ks) Nf[q][ks] = *(const v4u*)(ws + OFF_WC + ((size_t)(448 + (2 * w + q) * 28 + ct) * 14 + ks) * 1024 + lane * 16);
    startup();
#pragma unroll
    for (int ks = 0; ks < KS; ++ks) { KEEP4(Nf[0][ks]); KEEP4(Nf[1][ks]); }
    auto publish_y = [&](int s) {
      const int h = 4 * (w - 4) + kg;
      const unsigned char* ys = smem + L_YB + (h * 4) * 64 + n * 4;
      const v4u y = v4u{*(const unsigned*)(ys), *(const unsigned*)(ys + 64), *(const unsigned*)(ys + 128), *(const unsigned*)(ys + 192)};
      st4_sc1(nosent4(y), rx, rg == 0 ? (int)OFF_VM + ((s * 16 + h) * 28 + ct) * 256 + n * 16 : OOB);
    };
    auto compute_y = [&](int s) {
      const unsigned char* lm = smem + L_MEM + (s & 1) * 7168 + lmoff;
      v4u mf[KS];
#pragma unroll
      for (int ks = 0; ks < PFD; ++ks) mf[ks] = *(const v4u*)(lm + ks * 512);
      __builtin_amdgcn_sched_barrier(0);
      f32x4 a0 = {0, 0, 0, 0}, a1 = {0, 0, 0, 0};
#pragma unroll
      for (int ks = 0; ks < KS; ++ks) {
        if (ks + PFD < KS) mf[ks + PFD] = *(const v4u*)(lm + (ks + PFD) * 512);
        a0 = mfma32(mf[ks], Nf[0][ks], a0); a1 = mfma32(mf[ks], Nf[1][ks], a1);
        __builtin_amdgcn_sched_barrier(0);
      }
      v2u y0 = pk4(a0), y1 = pk4(a1);
      unsigned char* yd = smem + (kg < 2 ? L_YB + ((2 * w) * 4 + 2 * kg) * 64 + n * 4 : L_DUMMY);
      *(unsigned*)(yd) = y0.x; *(unsigned*)(yd + 64) = y0.y;
      *(unsigned*)(yd + 256) = y1.x; *(unsigned*)(yd + 320) = y1.y;
    };
    if (w < 4) {
      int nf = 0;
      v4u Vf[4];
      int known = 0;
      unsigned aold = 0;
      kv_ensure(rx, 0, known, 4);
#pragma unroll
      for (int g2 = 0; g2 < 4; ++g2) Vf[g2] = ld_sc1(rx, (int)OFF_VP + ((4 * w + g2) * 28 + ct) * 1024 + lane * 16);
      const int g = n >> 2, i4 = n & 3, hP = 4 * w + g;
      unsigned char* pdst = smem + L_P1 + (kg * 4 + i4) * 80 + (w * 4 + g) * 4;
      const unsigned char* ys = smem + L_YB + ((4 * w) * 4 + 2 * (kg & 1) + (kg >> 1)) * 64 + n * 4;
#pragma nounroll
      for (int s = 0; s < NSEG; ++s) {
        const int eb = ex_off(s, 1 + 2 * rg);
        int oe = eb + (hP * 4 + i4) * 64 + kg * 16;
        int ol = eb + 4096 + (hP * 4 + i4) * 4;
        int om = ex_off(s, 2 + 2 * rg) + ((hP * 4 + 2 * (kg >> 1)) * 4 + i4) * 32 + (kg & 1) * 16;
        KEEP4(oe); KEEP4(ol); KEEP4(om);
        BAR();
        STAMP(2);
        compute_y(s);
#pragma unroll
        for (int g2 = 0; g2 < 4; ++g2) KEEP4(Vf[g2]);
        if (w == 3 && lane == 0 && rg == 0 && s > 0) arrive_finish(rx, aold, CTR_M + (s - 1) * 128, 84u);
        if (s == NSEG - 1) break;
        unsigned tiss;
        for (;;) {
          const v2u tv = *(volatile const v2u*)(smem + L_SCH);
          tiss = __builtin_amdgcn_readfirstlane(tv.x);
          if (__builtin_amdgcn_readfirstlane(tv.y) == (unsigned)(s + 1)) break;
        }
        STAMP(3);
        if (s > 0) tiss = wait_until(tiss);
        else { sleep_units(T_Q1); tiss = rt32(); }
        STAMP(6);
        int spin = 0;
        f32x4 dres;
        v4u Yf = v4u{0, 0, 0, 0};
#pragma nounroll
        for (;;) {
          asm volatile("" ::: "memory");
          const v4u q0 = ld_sc1(rx, om), q1 = ld_sc1(rx, om + 128);
          const unsigned ls = __builtin_amdgcn_raw_buffer_load_b32(rx, ol, 0, AUX_SC1);
          const v4u ex = ld_sc1(rx, oe);
          __builtin_amdgcn_sched_barrier(0);
          if (spin == 0) {
            BAR();
            Yf = v4u{*(const unsigned*)(ys), *(const unsigned*)(ys + 256), *(const unsigned*)(ys + 512), *(const unsigned*)(ys + 768)};
            KEEP4(Yf);
          }
          __builtin_amdgcn_sched_barrier(0);
          const f32x4 sq = __builtin_bit_cast(f32x4, q0) + __builtin_bit_cast(f32x4, q1);
          auto ra = __builtin_amdgcn_permlane32_swap(__float_as_uint(sq.x), __float_as_uint(sq.z), false, false);
          auto rb = __builtin_amdgcn_permlane32_swap(__float_as_uint(sq.y), __float_as_uint(sq.w), false, false);
          const unsigned ra0 = ra[0], ra1 = ra[1], rb0 = rb[0], rb1 = rb[1];
          const float sm0 = __uint_as_float(ra0) + __uint_as_float(ra1), sm1 = __uint_as_float(rb0) + __uint_as_float(rb1);
          const float lse = __uint_as_float(ls);
          const float mx = xmax32(xmax16(fmaxf(sm0, sm1)));
          const float nb = -fmaxf(mx * sc, lse);
          const float e0 = __builtin_amdgcn_exp2f(__builtin_fmaf(sm0, sc, nb)), e1 = __builtin_amdgcn_exp2f(__builtin_fmaf(sm1, sc, nb));
          const float wx = __builtin_amdgcn_exp2f(lse + nb);
          const unsigned pk = pk2(e0, e1);
          const v4u pb = v4u{g == 0 ? pk : 0u, g == 1 ? pk : 0u, g == 2 ? pk : 0u, g == 3 ? pk : 0u};
          const f32x4 dx0 = mfma32(Vf[0], ex, f32x4{0, 0, 0, 0});
          const f32x4 dx1 = mfma32(Vf[1], ex, f32x4{0, 0, 0, 0});
          const f32x4 dx2 = mfma32(Vf[2], ex, f32x4{0, 0, 0, 0});
          const f32x4 dx3 = mfma32(Vf[3], ex, f32x4{0, 0, 0, 0});
          const f32x4 dm = mfma32(Yf, pb, f32x4{0, 0, 0, 0});
          const float inv = __builtin_amdgcn_rcpf(wx + xsum32(xsum16(e0 + e1)));
          const unsigned mxu = umax3(umax3(umax4(q0), umax4(q1), ls), umax4(ex), 0u);
          const f32x4 dxa = g < 2 ? (g == 0 ? dx0 : dx1) : (g == 2 ? dx2 : dx3);
          dres = (dxa * wx + dm) * inv;
          *(float*)(pdst) = dres.x; *(float*)(pdst + 1280) = dres.y; *(float*)(pdst + 2560) = dres.z; *(float*)(pdst + 3840) = dres.w;
          if (__builtin_expect(__all(mxu != SENT), 1)) break;
          if (spin == 0) { oe ^= 8192; ol ^= 8192; om ^= 8192; sleep_units(T_R1); } else __builtin_amdgcn_s_sleep(2);
          if (++spin >= SPIN_MAX) break;
          tiss = rt32();
        }
        *(unsigned*)(smem + L_SCH + 16 + w * 4) = (tiss << 2) | (spin ? 2u : 0u);
        DRAIN();
        STAMP(8);
        BAR();
        STAMPV(11, spin);
        nf += (s > 1 && spin) ? 1 : 0;
        if (w == 3 && lane == 0 && rg == 0) aold = arrive_issue(ws, CTR_M + s * 128);
        const int sn = s + 1 < NSEG - 1 ? s + 1 : s;
        kv_ensure(rx, sn >> 1, known, 4);
#pragma unroll
        for (int g2 = 0; g2 < 4; ++g2) Vf[g2] = ld_sc1(rx, (int)OFF_VP + (sn * 448 + (4 * w + g2) * 28 + ct) * 1024 + lane * 16);
      }
      BAR();
      BAR();
      FAIL_DUMP(nf);
    } else {
      Sched sd = {0u, 0u, 0, 0, 0, 0, 1};
      const unsigned char* pp = smem + L_P1 + lane * 80;
      const bool st = w == 4 && (kg & 1) == 0;
      const int eoff = ct * 256 + rg * 128 + ((((n >> 3) & 1) * 4 + (n & 3)) * 8 + ((n >> 2) & 1) * 4 + kg) * 2;
#pragma nounroll
      for (int s = 0; s < NSEG; ++s) {
        const int o0 = ex_off(s, 0) + goff;
        unsigned char* d0 = smem + L_MEM + (s & 1) * 7168 + goff;
        unsigned char* d1 = w == 7 ? smem + L_DUMMY : d0 + 1024;
        const int ot = (s > 0 && lane < NREP) ? o0 - goff + 7168 + lane * 128 : OOB;
        unsigned word;
        if (s > 1) wait_until(sd.tE2 + T_OG);
        else if (s == 1) sleep_units(8);
        STAMP(0);
        const int spin = gather_poll(rx, d0, d1, o0, w == 7 ? OOB : o0 + 1024, ot, word);
        __builtin_amdgcn_s_setprio(3);
        BAR();
        STAMP(1);
        sched_step(sd, word, lane, s);
        if (w == 4) *(volatile v2u*)(smem + L_SCH) = v2u{sd.tE1, (unsigned)(s + 1)};
        __builtin_amdgcn_s_setprio(0);

        sd.nf += (s > 1 && spin) ? 1 : 0;
        *(unsigned*)(smem + L_SCH + 32 + (w - 4) * 4) = (s > 1 && spin) ? 1u : 0u;
        compute_y(s);
        if (s == NSEG - 1) break;
        const int eon = ex_off(s + 1, 0);
        const int sto = st ? eon + eoff : ((w == 5 && lane == 0 && o_ % (56 / NREP) == 0) ? eon + 7168 + (o_ / (56 / NREP)) * 128 : OOB);
        BAR();
        publish_y(s);
        const v4u gf = *(const v4u*)(smem + L_SCH + 32);
        const unsigned gfl = gf.x | gf.y | gf.z | gf.w;
        const unsigned tref = sd.tE1 << 2;
        KEEP4(sto); KEEP4(gfl);
        DRAIN();
        if (w == 4) {
          BAR();
          STAMP(4);
          const f32x4 a = (*(const f32x4*)(pp) + *(const f32x4*)(pp + 16)) + (*(const f32x4*)(pp + 32) + *(const f32x4*)(pp + 48));
          const float v = (a.x + a.y) + (a.z + a.w);
          auto r = __builtin_amdgcn_permlane16_swap(__float_as_uint(v), __float_as_uint(v), false, false);
          const unsigned r0 = r[0], r1 = r[1];
          const unsigned ov = nosent(pk2(__uint_as_float(r0), __uint_as_float(r1)));
          st1_sc1(ov, rx, sto);
          st1_sc1(ov, rx, sto ^ 8192);
        } else {
          BAR();
          const v4u tw = *(const v4u*)(smem + L_SCH + 16);
          const unsigned tm = tref + (unsigned)max(max((int)(tw.x - tref), (int)(tw.y - tref)), max((int)(tw.z - tref), (int)(tw.w - tref)));
          const unsigned wd = nosent(tm | ((tw.x | tw.y | tw.z | tw.w) & 3u) | gfl);
          st1_sc1(wd, rx, sto);
          st1_sc1(wd, rx, sto ^ 8192);
        }
        __builtin_amdgcn_sched_barrier(0);
        STAMP(5);
        STAMPV(10, spin);
      }
      BAR();
      publish_y(NSEG - 1);
      DRAIN();
      BAR();
      if (w == 6 && lane == 0 && rg == 0) arrive(ws, rx, CTR_M + (NSEG - 1) * 128, 84u);
      FAIL_DUMP(w == 7 ? sd.m1 * 1000 + sd.m2 : sd.nf);
    }
  } else {
    startup();
    if (w < 2) {
      const int h = 2 * (cu - N_S - N_O) + w;
      v4u K[2][KS];
      int known = 0;
      kv_ensure(rx, 0, known, 4);
#pragma unroll
      for (int u = 0; u < 2; ++u)
#pragma unroll
        for (int ks = 0; ks < KS; ++ks) K[u][ks] = ld_sc1(rx, (int)OFF_KP + ((h * 2 + u) * 14 + ks) * 1024 + lane * 16);
#pragma nounroll
      for (int s = 0; s < NSEG - 1; ++s) {
        const int eo1 = ex_off(s, 1), eo3 = ex_off(s, 3);
        const int eb = ((n >> 2) & 1) ? eo3 : eo1;
        const int so = n < 8 ? eb + (h * 4 + (n & 3)) * 64 + kg * 16 : OOB;
        const int lo = lane < 8 ? eb + 4096 + (h * 4 + (n & 3)) * 4 : OOB;
        const unsigned char* lm = smem + L_MEM + (s & 1) * 7168 + lmoff;
#pragma unroll
        for (int ks = 0; ks < KS; ++ks) { KEEP4(K[0][ks]); KEEP4(K[1][ks]); }
        BAR();
        STAMP(2);
        v4u mf[KS];
#pragma unroll
        for (int ks = 0; ks < PFD; ++ks) mf[ks] = *(const v4u*)(lm + ks * 512);
        __builtin_amdgcn_sched_barrier(0);
        f32x4 a0 = {0, 0, 0, 0}, a1 = {0, 0, 0, 0};
#pragma unroll
        for (int ks = 0; ks < KS; ++ks) {
          if (ks + PFD < KS) mf[ks + PFD] = *(const v4u*)(lm + (ks + PFD) * 512);
          a0 = mfma32(K[0][ks], mf[ks], a0);
          a1 = mfma32(K[1][ks], mf[ks], a1);
          __builtin_amdgcn_sched_barrier(0);
        }
        float xs[8] = {rbf(a0.x), rbf(a0.y), rbf(a0.z), rbf(a0.w), rbf(a1.x), rbf(a1.y), rbf(a1.z), rbf(a1.w)};
        float mx = xs[0];
#pragma unroll
        for (int j = 1; j < 8; ++j) mx = fmaxf(mx, xs[j]);
        mx = xmax32(xmax16(mx));
        const float nb = -mx * sc;
        float sum = 0.f;
#pragma unroll
        for (int j = 0; j < 8; ++j) { xs[j] = __builtin_amdgcn_exp2f(__builtin_fmaf(xs[j], sc, nb)); sum += xs[j]; }
        sum = xsum32(xsum16(sum));
        const float inv = __builtin_amdgcn_rcpf(sum);
        v4u o = v4u{pk2(xs[0] * inv, xs[1] * inv), pk2(xs[2] * inv, xs[3] * inv), pk2(xs[4] * inv, xs[5] * inv), pk2(xs[6] * inv, xs[7] * inv)};
        o = nosent4(o);
        st4_sc1(o, rx, so);
        const float lse = __builtin_amdgcn_logf(sum) - nb;
        st1_sc1(nosent(__float_as_uint(lse)), rx, lo);
        st4_sc1(o, rx, so ^ 8192);
        st1_sc1(nosent(__float_as_uint(lse)), rx, lo ^ 8192);
        __builtin_amdgcn_sched_barrier(0);
        STAMP(5);
        const int sn = s + 1 < NSEG - 1 ? s + 1 : s;
        kv_ensure(rx, sn >> 1, known, 4);
#pragma unroll
        for (int u = 0; u < 2; ++u)
#pragma unroll
          for (int ks = 0; ks < KS; ++ks) K[u][ks] = ld_sc1(rx, (int)OFF_KP + (((sn * 16 + h) * 2 + u) * 14 + ks) * 1024 + lane * 16);
      }
    } else if (w < 4) {
#pragma nounroll
      for (int s = 0; s < NSEG - 1; ++s) BAR();
    } else {
      Sched sd = {0u, 0u, 0, 0, 0, 0, 1};
#pragma nounroll
      for (int s = 0; s < NSEG - 1; ++s) {
        const int o0 = ex_off(s, 0) + goff;
        unsigned char* d0 = smem + L_MEM + (s & 1) * 7168 + goff;
        unsigned char* d1 = w == 7 ? smem + L_DUMMY : d0 + 1024;
        const int ot = (s > 0 && lane < NREP) ? o0 - goff + 7168 + lane * 128 : OOB;
        unsigned word;
        if (s > 1) wait_until(sd.tE2);
        else if (s == 1) { int kn = 0; kv_ensure(rx, 0, kn, 8); sleep_units(T_Q2); }
        STAMP(0);
        const int spin = gather_poll(rx, d0, d1, o0, w == 7 ? OOB : o0 + 1024, ot, word);
        BAR();
        STAMP(1);
        STAMPV(10, spin);
        sched_step(sd, word, lane, s);
        sd.nf += (s > 1 && spin) ? 1 : 0;
      }
      FAIL_DUMP(sd.nf);
    }
  }
  STAMP_DUMP();
}
DI void phase_kv(const Params& p, unsigned char* lds, rsrc_t rw, int cg, int par) {
  unsigned char* ws = p.ws;
  const int tid = threadIdx.x, lane = tid & 63, w = __builtin_amdgcn_readfirstlane(tid >> 6);
  const int nt0 = cg * 16 + 2 * w;
  v4u st[7];
  {
    const unsigned char* src = ws + OFF_XN + (size_t)par * 57344 + tid * 16;
#pragma unroll
    for (int i = 0; i < 7; ++i) st[i] = *(const v4u*)(src + i * 8192);
  }
  v4u W[2][KS];
#pragma unroll
  for (int q = 0; q < 2; ++q)
#pragma unroll
    for (int ks = 0; ks < KS; ++ks) W[q][ks] = *(const v4u*)(ws + OFF_WC + ((size_t)(nt0 + q) * 14 + ks) * 1024 + lane * 16);
#pragma unroll
  for (int i = 0; i < 7; ++i) *(v4u*)(lds + i * 8192 + tid * 16) = st[i];
#pragma unroll
  for (int ks = 0; ks < KS; ++ks) { KEEP4(W[0][ks]); KEEP4(W[1][ks]); }
  BAR();
  const bool isK = cg < 28;
  const int kbase = (int)OFF_KP + (((nt0 / 28) * 2) * 14 + ((nt0 % 28) >> 1)) * 1024 + lane * 16;
  const int vbase = (int)OFF_VP + (nt0 - 448) * 1024 + lane * 16;
  int pend = -1;
  unsigned aold = 0;
#pragma nounroll
  for (int it = 0; it < 16; ++it) {
    const int hb = par + 2 * it;
    const unsigned char* cur = lds + (it & 1) * 57344 + lane * 16;
    {
      const int so = (int)OFF_XN + (it + 1 < 16 ? hb + 2 : hb) * 57344;
#pragma unroll
      for (int i = 0; i < 7; ++i) st[i] = __builtin_amdgcn_raw_buffer_load_b128(rw, tid * 16, so + i * 8192, 0);
    }
    __builtin_amdgcn_sched_barrier(0);
    f32x4 acc[4][2];
#pragma unroll
    for (int rt = 0; rt < 4; ++rt) { acc[rt][0] = f32x4{0, 0, 0, 0}; acc[rt][1] = f32x4{0, 0, 0, 0}; }
    if (isK) {
      v4u af[2][4];
#pragma unroll
      for (int rt = 0; rt < 4; ++rt) af[0][rt] = *(const v4u*)(cur + (rt * 14) * 1024);
#pragma unroll
      for (int ks = 0; ks < KS; ++ks) {
        if (ks + 1 < KS) {
#pragma unroll
          for (int rt = 0; rt < 4; ++rt) af[(ks + 1) & 1][rt] = *(const v4u*)(cur + (rt * 14 + ks + 1) * 1024);
        }
        __builtin_amdgcn_sched_barrier(0);
#pragma unroll
        for (int rt = 0; rt < 4; ++rt) {
          acc[rt][0] = mfma32(W[0][ks], af[ks & 1][rt], acc[rt][0]);
          acc[rt][1] = mfma32(W[1][ks], af[ks & 1][rt], acc[rt][1]);
        }
        __builtin_amdgcn_sched_barrier(0);
      }
    } else {
      v4u af[2][4];
#pragma unroll
      for (int rt = 0; rt < 4; ++rt) af[0][rt] = *(const v4u*)(cur + (rt * 14) * 1024);
#pragma unroll
      for (int ks = 0; ks < KS; ++ks) {
        if (ks + 1 < KS) {
#pragma unroll
          for (int rt = 0; rt < 4; ++rt) af[(ks + 1) & 1][rt] = *(const v4u*)(cur + (rt * 14 + ks + 1) * 1024);
        }
        __builtin_amdgcn_sched_barrier(0);
#pragma unroll
        for (int rt = 0; rt < 4; ++rt) {
          acc[rt][0] = mfma32(af[ks & 1][rt], W[0][ks], acc[rt][0]);
          acc[rt][1] = mfma32(af[ks & 1][rt], W[1][ks], acc[rt][1]);
        }
        __builtin_amdgcn_sched_barrier(0);
      }
    }
    __builtin_amdgcn_sched_barrier(0);
#pragma unroll
    for (int i = 0; i < 7; ++i) *(v4u*)(lds + ((it + 1) & 1) * 57344 + i * 8192 + tid * 16) = st[i];
    DRAIN();
    if (pend >= 0 && lane == 0) arrive_finish(rw, aold, pend, 56u);
    pend = -1;
    __builtin_amdgcn_sched_barrier(0);
    if (isK) {
#pragma unroll
      for (int rt = 0; rt < 4; ++rt) st4_sc1(pk8(acc[rt][0], acc[rt][1]), rw, kbase + ((hb * 2 + (rt >> 1)) * 32 + (rt & 1)) * 14336);
    } else {
#pragma unroll
      for (int q = 0; q < 2; ++q)
#pragma unroll
        for (int sg = 0; sg < 2; ++sg) st4_sc1(pk8(acc[2 * sg][q], acc[2 * sg + 1][q]), rw, vbase + ((hb * 2 + sg) * 448 + q) * 1024);
    }
    if (it == 0 || it == 15) DRAIN();
    BAR();
    if (w == 0) TL(200 + it);
    if (w == (it & 7)) {
      if (it == 0 || it == 15) { if (lane == 0) arrive(ws, rw, CTR_KV + hb * 128, 56u); }
      if (it >= 2) {
        if (it == 15) { if (lane == 0) arrive(ws, rw, CTR_KV + (hb - 2) * 128, 56u); }
        else { pend = CTR_KV + (hb - 2) * 128; if (lane == 0) aold = arrive_issue(ws, pend); }
      }
    }
  }
}

DI void gate_wait(rsrc_t rw, int rt, int nap, int lag, int own) {
  int off = (int)OFF_PM + rt * 4096;
  if (__builtin_amdgcn_readfirstlane(ld1_sc1(rw, off)) != SENT) {
    off = own; lag = 4; nap = 8;
    if (__builtin_amdgcn_readfirstlane(ld1_sc1(rw, off)) != SENT) return;
  }
#pragma nounroll
  for (int spin = 0; spin < SPIN_MAX; ++spin) {
    sleep_units(nap);
    asm volatile("" ::: "memory");
    if (__builtin_amdgcn_readfirstlane(ld1_sc1(rw, off)) != SENT) break;
  }
  sleep_units(lag);
}
#define LAG_A2 10
#define LAG_F1 65
#define LAG_F2 140
#define RETRY 6
#define NAPT 4
#define NAP 6

#define TLX(k) do { if (rt == 127 || rt == 96) TL((rt == 127 ? 130 : 140) + (k)); } while (0)
DI void stage_a1(const Params& p, rsrc_t rw, int h) {
  unsigned char* ws = p.ws;
  const int tid = threadIdx.x, lane = tid & 63, w = __builtin_amdgcn_readfirstlane(tid >> 6);
  const int kg = lane >> 4, n = lane & 15, v = w & 1;
  const float sc = 0.0625f * 1.4426950408889634f;
  const float NEG = -3.0e38f;
  int known = 0;
  ctr_wait(rw, CTR_FX, 1u, NAP);
#pragma nounroll
  for (int rt = w; rt < 128; rt += 8) {
    const int s = rt >> 1;
    v4u xf[KS], kf[KS], k1[KS];
    kv_ensure(rw, s >> 1, known, NAP);
    const int kb = (int)OFF_KP + ((s * 16 + h) * 2) * 14336 + lane * 16;
#pragma unroll
    for (int ks = 0; ks < KS; ++ks) {
      xf[ks] = *(const v4u*)(ws + OFF_XN + ((size_t)rt * 14 + ks) * 1024 + lane * 16);
      kf[ks] = ld_sc1(rw, kb + ks * 1024);
    }
#pragma unroll
    for (int ks = 0; ks < KS; ++ks) k1[ks] = ld_sc1(rw, v ? kb + 14336 + ks * 1024 : OOB);
    __builtin_amdgcn_sched_barrier(0);
    f32x4 a0 = {0, 0, 0, 0}, a1 = {0, 0, 0, 0}, am = {0, 0, 0, 0};
#pragma unroll
    for (int ks = 0; ks < KS; ++ks) a0 = mfma32(kf[ks], xf[ks], a0);
#pragma unroll
    for (int ks = 0; ks < KS; ++ks) a1 = mfma32(k1[ks], xf[ks], a1);
    float xs[8];
#pragma unroll
    for (int r = 0; r < 4; ++r) {
      const bool diag = 4 * kg + r <= n;
      xs[r] = (v || diag) ? rbf(a0[r]) : NEG;
      xs[4 + r] = (v && diag) ? rbf(a1[r]) : NEG;
    }
    float mx = NEG;
#pragma unroll
    for (int j = 0; j < 8; ++j) mx = fmaxf(mx, xs[j]);
    const int tb = (int)OFF_KM + (s * 16 + h) * 7168 + kg * 128 + (n & 7) * 16;
    TLX(3);
    ctr_wait(rw, CTR_M + (s < NSEG - 2 ? s : s - 2) * 128, 84u, NAP);
    TLX(0);
#pragma nounroll
    for (int spin = 0; spin < SPIN_MAX; ++spin) {
      asm volatile("" ::: "memory");
      TLX(1);
      unsigned mxu = 0;
#pragma unroll
      for (int ks = 0; ks < KS; ++ks) kf[ks] = ld_sc1(rw, tb + ks * 512);
      if (s < NSEG - 2) break;
#pragma unroll
      for (int ks = 0; ks < KS; ++ks) mxu = max(mxu, umax4(kf[ks]));
      if (__all(mxu != SENT)) break;
      sleep_units(3);
    }
#pragma unroll
    for (int ks = 0; ks < KS; ++ks) am = mfma32(kf[ks], xf[ks], am);
    float mm[4];
#pragma unroll
    for (int r = 0; r < 4; ++r) { mm[r] = rbf(am[r]); mx = fmaxf(mx, mm[r]); }
    mx = xmax32(xmax16(mx));
    const float nb = -mx * sc;
    float sum = 0.f;
#pragma unroll
    for (int j = 0; j < 8; ++j) { xs[j] = xs[j] == NEG ? 0.f : __builtin_amdgcn_exp2f(__builtin_fmaf(xs[j], sc, nb)); sum += xs[j]; }
#pragma unroll
    for (int r = 0; r < 4; ++r) { mm[r] = __builtin_amdgcn_exp2f(__builtin_fmaf(mm[r], sc, nb)); if (kg < 2) sum += mm[r]; }
    sum = xsum32(xsum16(sum));
    const float inv = 1.f / sum;
    st4_sc1(nosent4(v4u{pk2(xs[0] * inv, xs[1] * inv), pk2(xs[2] * inv, xs[3] * inv), pk2(xs[4] * inv, xs[5] * inv), pk2(xs[6] * inv, xs[7] * inv)}),
            rw, (int)OFF_PX + (rt * 16 + h) * 1024 + lane * 16);
    st2_sc1(nosent2(v2u{pk2(mm[0] * inv, mm[1] * inv), pk2(mm[2] * inv, mm[3] * inv)}), rw,
            kg < 2 ? (int)OFF_PM + (rt * 4 + (h >> 2)) * 1024 + ((h & 3) * 16 + n) * 16 + kg * 8 : OOB);
    TL(rt);
  }
}

DI void stage_a2(const Params& p, rsrc_t rw, int ct) {
  const int tid = threadIdx.x, lane = tid & 63, w = __builtin_amdgcn_readfirstlane(tid >> 6);
  const int kg = lane >> 4, n = lane & 15;
  const int c = 2 * ct + (kg >> 1);
  const int col = 32 * (c >> 2) + 16 * (kg & 1) + 4 * (c & 3);
  const int hoff = (c >> 2) * 1024 + ((c & 3) * 16 + n) * 16 + (kg & 1) * 8;
  int known = 0;
  ctr_wait(rw, CTR_FX, 1u, NAP);
#pragma nounroll
  for (int rt = w; rt < 128; rt += 8) {
    const int s = rt >> 1;
    const v2u xv = *(const v2u*)(p.x + (size_t)(rt * 16 + n) * 448 + col);
    kv_ensure(rw, s >> 1, known, NAP);
    v4u vf[16], yf[4];
#pragma unroll
    for (int h = 0; h < 16; ++h) vf[h] = ld_sc1(rw, (int)OFF_VP + (s * 448 + h * 28 + ct) * 1024 + lane * 16);
    TLX(3);
    ctr_wait(rw, CTR_M + (s < NSEG - 2 ? s : s - 2) * 128, 84u, NAP);
    const int pb = (int)OFF_PX + rt * 16384 + lane * 16, mb = (int)OFF_PM + rt * 4096 + lane * 16;
    gate_wait(rw, rt, NAPT, LAG_A2, (int)OFF_PX + (rt * 16 + (ct & 15)) * 1024);
    TLX(0);
    f32x4 acc;
#pragma nounroll
    for (int spin = 0; spin < SPIN_MAX; ++spin) {
      asm volatile("" ::: "memory");
      TLX(1);
      v4u px[16], pm[4];
#pragma unroll
      for (int q = 0; q < 4; ++q) yf[q] = ld_sc1(rw, (int)OFF_VM + ((s * 16 + 4 * q + kg) * 28 + ct) * 256 + n * 16);
#pragma unroll
      for (int h = 0; h < 16; ++h) px[h] = ld_sc1(rw, pb + h * 1024);
#pragma unroll
      for (int q = 0; q < 4; ++q) pm[q] = ld_sc1(rw, mb + q * 1024);
      unsigned mxu = 0;
      f32x4 a0 = {0, 0, 0, 0}, a1 = {0, 0, 0, 0};
#pragma unroll
      for (int h = 0; h < 16; h += 2) {
        mxu = umax3(mxu, umax4(px[h]), umax4(px[h + 1]));
        a0 = mfma32(vf[h], px[h], a0); a1 = mfma32(vf[h + 1], px[h + 1], a1);
      }
#pragma unroll
      for (int q = 0; q < 4; q += 2) {
        mxu = umax3(mxu, umax3(umax4(pm[q]), umax4(pm[q + 1]), umax4(yf[q])), umax4(yf[q + 1]));
        a0 = mfma32(yf[q], pm[q], a0); a1 = mfma32(yf[q + 1], pm[q + 1], a1);
      }
      acc = a0 + a1;
      if (__all(mxu != SENT)) break;
      sleep_units(RETRY);
    }
    const float h0 = bflo(xv.x) + rbf(acc.x), h1 = bfhi(xv.x) + rbf(acc.y), h2 = bflo(xv.y) + rbf(acc.z), h3 = bfhi(xv.y) + rbf(acc.w);
    st2_sc1(nosent2(v2u{pk2(h0, h1), pk2(h2, h3)}), rw, (int)OFF_HF + rt * 14336 + hoff);
    TL(rt);
  }
}

DI void stage_f1(const Params& p, unsigned char* lds, rsrc_t rw, int kf) {
  const int tid = threadIdx.x, lane = tid & 63, w = __builtin_amdgcn_readfirstlane(tid >> 6);
  const int kg = lane >> 4, n = lane & 15;
  for (int f = w; f < 56; f += 8) {
    const int t = f / 14, ks = f % 14;
    const u16* src = ((t >> 1) ? p.wu : p.wg) + 32 * kf + 16 * (t & 1) + n;
    unsigned e[8];
#pragma unroll
    for (int jj = 0; jj < 8; ++jj) e[jj] = src[(size_t)pidx(ks, kg, jj) * 1792];
    *(v4u*)(lds + f * 1024 + lane * 16) = v4u{e[0] | (e[1] << 16), e[2] | (e[3] << 16), e[4] | (e[5] << 16), e[6] | (e[7] << 16)};
  }
  BAR();
  const v4u ones = {0x3F803F80u, 0x3F803F80u, 0x3F803F80u, 0x3F803F80u};
  f32x4 cs[4];
#pragma unroll
  for (int t = 0; t < 4; ++t) {
    cs[t] = f32x4{0, 0, 0, 0};
#pragma unroll
    for (int ks = 0; ks < KS; ++ks) cs[t] = mfma32(*(const v4u*)(lds + (t * 14 + ks) * 1024 + lane * 16), ones, cs[t]);
  }
#pragma nounroll
  for (int rt = w; rt < 128; rt += 8) {
    const int hb = (int)OFF_HF + rt * 14336 + lane * 16;
    gate_wait(rw, rt, NAPT, LAG_F1, (int)OFF_HF + rt * 14336 + (kf >> 1) * 512);
    TLX(0);
    v4u hf[KS];
#pragma nounroll
    for (int spin = 0; spin < SPIN_MAX; ++spin) {
      asm volatile("" ::: "memory");
      TLX(1);
      unsigned mxu = 0;
#pragma unroll
      for (int ks = 0; ks < KS; ++ks) hf[ks] = ld_sc1(rw, hb + ks * 1024);
#pragma unroll
      for (int ks = 0; ks < KS; ++ks) mxu = max(mxu, umax4(hf[ks]));
      if (__all(mxu != SENT)) break;
      sleep_units(RETRY);
    }
    TLX(2);
    f32x4 acc[4], sm = {0, 0, 0, 0}, gr = {0, 0, 0, 0};
#pragma unroll
    for (int t = 0; t < 4; ++t) acc[t] = f32x4{0, 0, 0, 0};
    v4u wf[2][4];
#pragma unroll
    for (int t = 0; t < 4; ++t) wf[0][t] = *(const v4u*)(lds + (t * 14) * 1024 + lane * 16);
#pragma unroll
    for (int ks = 0; ks < KS; ++ks) {
      if (ks + 1 < KS) {
#pragma unroll
        for (int t = 0; t < 4; ++t) wf[(ks + 1) & 1][t] = *(const v4u*)(lds + (t * 14 + ks + 1) * 1024 + lane * 16);
      }
      sm = mfma32(ones, hf[ks], sm);
      gr = mfma32(hf[ks], hf[ks], gr);
      __builtin_amdgcn_sched_barrier(0);
#pragma unroll
      for (int t = 0; t < 4; ++t) acc[t] = mfma32(wf[ks & 1][t], hf[ks], acc[t]);
      __builtin_amdgcn_sched_barrier(0);
    }
    float dg = (kg == (n >> 2)) ? ((n & 2) ? ((n & 1) ? gr.w : gr.z) : ((n & 1) ? gr.y : gr.x)) : 0.f;
    dg = xsum32(xsum16(dg));
    const float mean = sm.x * (1.f / 448.f);
    const float var = fmaxf(dg * (1.f / 448.f) - mean * mean, 0.f);
    const float mb = rbf(mean), rden = 1.f / ln_den(var);
    f32x4 o0, o1;
#pragma unroll
    for (int r = 0; r < 4; ++r) {
      o0[r] = rbf((acc[0][r] - mb * cs[0][r]) * rden) * rbf((acc[2][r] - mb * cs[2][r]) * rden);
      o1[r] = rbf((acc[1][r] - mb * cs[1][r]) * rden) * rbf((acc[3][r] - mb * cs[3][r]) * rden);
    }
    st4_sc1(nosent4(pk8(o0, o1)), rw, (int)OFF_AF + (rt * 56 + kf) * 1024 + lane * 16);
    TL(rt);
  }
}

DI void stage_f2(const Params& p, unsigned char* lds, rsrc_t rw, int ct) {
  const int tid = threadIdx.x, lane = tid & 63, w = __builtin_amdgcn_readfirstlane(tid >> 6);
  const int kg = lane >> 4, n = lane & 15;
  for (int kf = w; kf < 56; kf += 8) {
    const u16* src = p.wd + 16 * ct + n;
    unsigned e[8];
#pragma unroll
    for (int jj = 0; jj < 8; ++jj) e[jj] = src[(size_t)pidx(kf, kg, jj) * 448];
    *(v4u*)(lds + kf * 1024 + lane * 16) = v4u{e[0] | (e[1] << 16), e[2] | (e[3] << 16), e[4] | (e[5] << 16), e[6] | (e[7] << 16)};
  }
  BAR();
#pragma nounroll
  for (int rt = w; rt < 128; rt += 8) {
    const int ab = (int)OFF_AF + rt * 57344 + lane * 16;
    gate_wait(rw, rt, NAPT, LAG_F2, (int)OFF_AF + rt * 57344 + (2 * ct) * 1024);
    TLX(0);
    v2u hv = v2u{0, 0};
    f32x4 a0, a1;
#pragma nounroll
    for (int spin = 0; spin < SPIN_MAX; ++spin) {
      asm volatile("" ::: "memory");
      TLX(1);
      a0 = f32x4{0, 0, 0, 0}; a1 = f32x4{0, 0, 0, 0};
      hv = __builtin_amdgcn_raw_buffer_load_b64(rw, (int)OFF_HF + rt * 14336 + (ct >> 1) * 1024 + lane * 16 + (ct & 1) * 8, 0, AUX_SC1);
      v4u af[3][14];
#pragma unroll
      for (int bb = 0; bb < 2; ++bb)
#pragma unroll
        for (int k = 0; k < 14; ++k) af[bb][k] = ld_sc1(rw, ab + (bb * 14 + k) * 1024);
      unsigned mxu = max(hv.x, hv.y);
#pragma unroll
      for (int bb = 0; bb < 4; ++bb) {
        if (bb + 2 < 4) {
#pragma unroll
          for (int k = 0; k < 14; ++k) af[(bb + 2) % 3][k] = ld_sc1(rw, ab + ((bb + 2) * 14 + k) * 1024);
        }
        __builtin_amdgcn_sched_barrier(0);
#pragma unroll
        for (int k = 0; k < 14; k += 2) {
          mxu = umax3(mxu, umax4(af[bb % 3][k]), umax4(af[bb % 3][k + 1]));
          a0 = mfma32(*(const v4u*)(lds + (bb * 14 + k) * 1024 + lane * 16), af[bb % 3][k], a0);
          a1 = mfma32(*(const v4u*)(lds + (bb * 14 + k + 1) * 1024 + lane * 16), af[bb % 3][k + 1], a1);
        }
        __builtin_amdgcn_sched_barrier(0);
      }
      if (__all(mxu != SENT)) break;
      sleep_units(RETRY);
    }
    const f32x4 d = a0 + a1;
    const float o0 = bflo(hv.x) + rbf(d.x), o1 = bfhi(hv.x) + rbf(d.y), o2 = bflo(hv.y) + rbf(d.z), o3 = bfhi(hv.y) + rbf(d.w);
    *(v2u*)(p.out + (size_t)(rt * 16 + n) * 448 + 16 * ct + 4 * kg) = v2u{pk2(o0, o1), pk2(o2, o3)};
    TL(rt);
  }
}

DI void arm_stage(const Params& p, rsrc_t rw, int k, int nwg, int off0, int nkb, int ctr) {
  const int tid = threadIdx.x, lane = tid & 63, w = __builtin_amdgcn_readfirstlane(tid >> 6);
  const v4u sent = {SENT, SENT, SENT, SENT};
  for (int i = k * 8 + w; i < nkb; i += nwg * 8) st4_sc1(sent, rw, off0 + i * 1024 + lane * 16);
  DRAIN();
  __syncthreads();
  if (tid == 0) arrive(p.ws, rw, ctr, (unsigned)nwg);
}

constexpr int MAIN_LDS = 114688;
__global__ void __launch_bounds__(512, 2) k_main(Params p) {
  extern __shared__ __attribute__((aligned(16))) unsigned char smem[];
  const rsrc_t rw = __builtin_amdgcn_make_buffer_rsrc((void*)p.ws, 0, (int)WS_END, 0x00020000);
  const int b = blockIdx.x;
  if (threadIdx.x < 64) TL(255);
  const int j = (b >> 3) * 4 + (b & 3);
  if ((b & 7) < 4) {
    if (j < N_CHAIN) chain_role(p, smem, rw, j);
    else { if (j == N_CHAIN) probe_place(p, rw); sleep_units(400); arm_stage(p, rw, j - N_CHAIN, 8, (int)OFF_HF, (int)((OFF_XN - OFF_HF) / 1024), CTR_FS + 128); }
    return;
  }
  if (j < 112) phase_kv(p, smem, rw, j % 56, j / 56);
  else arm_stage(p, rw, j - 112, 16, (int)OFF_PX, (int)((OFF_HF - OFF_PX) / 1024), CTR_FS);
  ctr_wait(rw, CTR_FS, 16u, 8);
  if (j < 112) ctr_wait(rw, CTR_FS + 128, 8u, 8);
  if (j < 56) stage_f1(p, smem, rw, j);
  else if (j < 84) stage_f2(p, smem, rw, j - 56);
  else if (j < 112) stage_a2(p, rw, j - 84);
  else stage_a1(p, rw, j - 112);
}

extern "C" void kernel_launch(void* const* d_in, const int* in_sizes, int n_in, void* d_out, int out_size, void* d_ws, size_t ws_size,
                              hipStream_t stream) {
  Params p;
  p.x = (const u16*)d_in[0]; p.mem0 = (const u16*)d_in[1]; p.Wq = (const u16*)d_in[2]; p.Wk = (const u16*)d_in[3];
  p.Wv = (const u16*)d_in[4]; p.Wo = (const u16*)d_in[5];
  p.wg = (const u16*)d_in[8]; p.wu = (const u16*)d_in[9]; p.wd = (const u16*)d_in[10];
  p.out = (u16*)d_out; p.ws = (unsigned char*)d_ws;
  (void)hipFuncSetAttribute((const void*)k_main, hipFuncAttributeMaxDynamicSharedMemorySize, MAIN_LDS);
  (void)hipFuncSetAttribute((const void*)k_prep, hipFuncAttributeMaxDynamicSharedMemorySize, PREP_LDS);
  k_prep<<<256, 512, PREP_LDS, stream>>>(p);
  k_main<<<256, 512, MAIN_LDS, stream>>>(p);
}
```

```cpp
#include <hip/hip_runtime.h>
#include <hip/hip_bf16.h>
#include <stdint.h>

typedef short bf16x8 __attribute__((ext_vector_type(8)));
typedef float f32x4 __attribute__((ext_vector_type(4)));
typedef float f32x2 __attribute__((ext_vector_type(2)));
typedef __bf16 bf2_t __attribute__((ext_vector_type(2)));
typedef unsigned v4u __attribute__((ext_vector_type(4)));
typedef unsigned v2u __attribute__((ext_vector_type(2)));
typedef unsigned short u16;
typedef __amdgpu_buffer_rsrc_t rsrc_t;
#define DI __device__ __forceinline__

constexpr int NSEG = 64, KS = 14;
constexpr unsigned SENT = 0xFFFFFFFFu;
constexpr int AUX_SC1 = 16;
constexpr int SPIN_MAX = 20000;
constexpr int OOB = 0x7FFFFFF0;

constexpr size_t OFF_CTR = 0;
constexpr int CTR_KV = 0, CTR_M = 4096, OFF_PTAB = 16384, CTR_FX = 16640, CTR_FS = 16896;
constexpr size_t OFF_EX = 65536;
constexpr size_t EX_STEP = 5 * 16384;
constexpr size_t OFF_PX = OFF_EX + 64 * EX_STEP;
constexpr size_t OFF_PM = OFF_PX + (size_t)128 * 16 * 1024;
constexpr size_t OFF_HF = OFF_PM + (size_t)128 * 4 * 1024;
constexpr size_t OFF_AF = OFF_HF + (size_t)128 * 14 * 1024;
constexpr size_t OFF_XN = OFF_AF + (size_t)128 * 56 * 1024;
constexpr size_t OFF_WC = OFF_XN + (size_t)128 * 14 * 1024;
constexpr size_t OFF_KP = OFF_WC + (size_t)896 * 14 * 1024;
constexpr size_t OFF_VP = OFF_KP + (size_t)64 * 16 * 2 * 14 * 1024;
constexpr size_t OFF_KM = OFF_VP + (size_t)64 * 448 * 1024;
constexpr size_t OFF_VM = OFF_KM + (size_t)64 * 16 * 14 * 512;
constexpr size_t WS_END = OFF_VM + (size_t)64 * 16 * 28 * 256;
constexpr size_t FILL_BYTES = OFF_XN - OFF_EX;
static_assert(WS_END < (size_t)200 * 1024 * 1024, "ws");
static_assert(FILL_BYTES % 16384 == 0, "fill");

DI f32x4 mfma32(v4u a, v4u b, f32x4 c) {
  return __builtin_amdgcn_mfma_f32_16x16x32_bf16(__builtin_bit_cast(bf16x8, a), __builtin_bit_cast(bf16x8, b), c, 0, 0, 0);
}
DI unsigned pk2(float a, float b) {
  f32x2 v = {a, b};
  return __builtin_bit_cast(unsigned, __builtin_convertvector(v, bf2_t));
}
DI v2u pk4(f32x4 a) { return v2u{pk2(a.x, a.y), pk2(a.z, a.w)}; }
DI v4u pk8(f32x4 a, f32x4 b) { return v4u{pk2(a.x, a.y), pk2(a.z, a.w), pk2(b.x, b.y), pk2(b.z, b.w)}; }
DI float bflo(unsigned u) { return __uint_as_float(u << 16); }
DI float bfhi(unsigned u) { return __uint_as_float(u & 0xFFFF0000u); }
DI float rbf(float a) { return bflo(pk2(a, 0.f) & 0xFFFFu); }
DI int picol(int c, int cc) { return 32 * (c >> 2) + 16 * (cc >> 2) + 4 * (c & 3) + (cc & 3); }
DI int pidx(int ks, int kg, int jj) { return 32 * ks + 16 * (jj >> 2) + 4 * kg + (jj & 3); }
DI float xsum16(float v) { auto r = __builtin_amdgcn_permlane16_swap(__float_as_uint(v), __float_as_uint(v), false, false);
  const unsigned a = r[0], b = r[1]; return __uint_as_float(a) + __uint_as_float(b); }
DI float xsum32(float v) { auto r = __builtin_amdgcn_permlane32_swap(__float_as_uint(v), __float_as_uint(v), false, false);
  const unsigned a = r[0], b = r[1]; return __uint_as_float(a) + __uint_as_float(b); }
DI float xmax16(float v) { auto r = __builtin_amdgcn_permlane16_swap(__float_as_uint(v), __float_as_uint(v), false, false);
  const unsigned a = r[0], b = r[1]; return fmaxf(__uint_as_float(a), __uint_as_float(b)); }
DI float xmax32(float v) { auto r = __builtin_amdgcn_permlane32_swap(__float_as_uint(v), __float_as_uint(v), false, false);
  const unsigned a = r[0], b = r[1]; return fmaxf(__uint_as_float(a), __uint_as_float(b)); }
DI unsigned nosent(unsigned v) { return min(v, 0xFFFFFFFEu); }
DI v2u nosent2(v2u o) { return v2u{nosent(o.x), nosent(o.y)}; }
DI v4u nosent4(v4u o) { return v4u{nosent(o.x), nosent(o.y), nosent(o.z), nosent(o.w)}; }
DI unsigned umax3(unsigned a, unsigned b, unsigned c) { return max(max(a, b), c); }
DI unsigned umax4(v4u r) { return max(umax3(r.x, r.y, r.z), r.w); }
#define KEEP4(x) asm volatile("" :: "v"(x))
#define BAR() do { asm volatile("s_waitcnt lgkmcnt(0)" ::: "memory"); __builtin_amdgcn_s_barrier(); asm volatile("" ::: "memory"); } while (0)
#define DRAIN() asm volatile("s_waitcnt vmcnt(0)" ::: "memory")
DI void sleep_units(int n) { for (int d = 0; d < n; ++d) __builtin_amdgcn_s_sleep(1); }
DI v4u ld_sc1(rsrc_t r, int off) { return __builtin_amdgcn_raw_buffer_load_b128(r, off, 0, AUX_SC1); }
DI unsigned ld1_sc1(rsrc_t r, int off) { return __builtin_amdgcn_raw_buffer_load_b32(r, off, 0, AUX_SC1); }
DI void st4_sc1(v4u v, rsrc_t r, int off) { __builtin_amdgcn_raw_buffer_store_b128(v, r, off, 0, AUX_SC1); }
DI void st2_sc1(v2u v, rsrc_t r, int off) { __builtin_amdgcn_raw_buffer_store_b64(v, r, off, 0, AUX_SC1); }
DI void st1_sc1(unsigned v, rsrc_t r, int off) { __builtin_amdgcn_raw_buffer_store_b32(v, r, off, 0, AUX_SC1); }
constexpr int FLAG_D = 32768;
DI unsigned arrive_issue(unsigned char* ws, int off) { return __hip_atomic_fetch_add((unsigned*)(ws + off), 1u, __ATOMIC_RELAXED, __HIP_MEMORY_SCOPE_AGENT); }
DI void arrive_finish(rsrc_t r, unsigned old, int off, unsigned total) { if (old == total - 1u) st1_sc1(1u, r, off + FLAG_D); }
DI void arrive(unsigned char* ws, rsrc_t r, int off, unsigned total) { arrive_finish(r, arrive_issue(ws, off), off, total); }
DI void ctr_wait(rsrc_t r, int off, unsigned want, int nap) {
  off += FLAG_D; want = 1u;
#pragma nounroll
  for (int spin = 0; spin < SPIN_MAX; ++spin) {
    asm volatile("" ::: "memory");
    const unsigned v = __builtin_amdgcn_readfirstlane(ld1_sc1(r, off));
    if (v >= want) break;
    sleep_units(nap);
  }
  asm volatile("" ::: "memory");
}

DI void kv_ensure(rsrc_t r, int hb, int& known, int nap) {
  if (hb < known) return;
  const int lane = threadIdx.x & 63;
#pragma nounroll
  for (int spin = 0; spin < SPIN_MAX; ++spin) {
    asm volatile("" ::: "memory");
    const unsigned v = ld1_sc1(r, lane < 32 ? CTR_KV + FLAG_D + lane * 128 : OOB);
    const unsigned long long m = __ballot(v != 0u);
    known = __builtin_ctzll(~m);
    if (hb < known) break;
    sleep_units(nap);
  }
  asm volatile("" ::: "memory");
}

struct Params {
  const u16 *x, *mem0, *Wq, *Wk, *Wv, *Wo, *wg, *wu, *wd;
  u16* out;
  unsigned char* ws;
};

DI float ln_den(float var) { return rbf(sqrtf(rbf(rbf(var) + 1e-5f))); }

#define TL(idx)
#define PREP_AT 13
constexpr int PREP_LDS = 288 * 528;
constexpr int FILL_KB = (int)(FILL_BYTES / 1024);
__global__ void __launch_bounds__(512, 2) k_prep(Params p) {
  extern __shared__ __attribute__((aligned(16))) unsigned char lds[];
  const int tid = threadIdx.x, lane = tid & 63, kg = lane >> 4, n = lane & 15;
  const int w = __builtin_amdgcn_readfirstlane(tid >> 6);
  unsigned char* ws = p.ws;
#define TLP(idx)
  const int xg = blockIdx.x & 7, ix = blockIdx.x >> 3;
  const int b = ix < 28 ? (2 * xg + ix / 14) * 14 + ix % 14 : 224 + xg * 4 + (ix - 28), bb = b;
  TLP(0);
  const v4u sent = {SENT, SENT, SENT, SENT};
  if (bb < 224) {
    const int h = bb / 14, r = bb % 14, ab = r / 7, jb = r % 7;
    v4u sa[2][14], sb[2][4];
    auto issue = [&](int half) {
      const u16* asrc = (half ? p.Wk : p.Wv) + (size_t)(224 * ab) * 4096 + h * 256;
      if (half) {
        const u16* bsrc = p.Wq + (size_t)(64 * jb) * 4096 + h * 256;
#pragma unroll
        for (int i = 0; i < 4; ++i) { const int pc = i * 512 + tid; sb[1][i] = *(const v4u*)(bsrc + (size_t)(pc >> 5) * 4096 + (pc & 31) * 8); }
      } else {
        const u16* bsrc = p.Wo + (size_t)(h * 256) * 448 + 64 * jb;
#pragma unroll
        for (int i = 0; i < 4; ++i) { const int pc = i * 512 + tid; sb[0][i] = *(const v4u*)(bsrc + (size_t)(pc >> 3) * 448 + (pc & 7) * 8); }
      }
#pragma unroll
      for (int i = 0; i < 14; ++i) { const int pc = i * 512 + tid; sa[half][i] = *(const v4u*)(asrc + (size_t)(pc >> 5) * 4096 + (pc & 31) * 8); }
    };
    issue(0);
    __builtin_amdgcn_sched_barrier(0);
    unsigned char* lb = lds + 224 * 528;
#pragma unroll
    for (int half = 0; half < 2; ++half) {
      if (half) {
        BAR();
#pragma unroll
        for (int i = 0; i < 4; ++i) { const int pc = i * 512 + tid; *(v4u*)(lb + (pc >> 5) * 528 + (pc & 31) * 16) = sb[1][i]; }
      } else {
#pragma unroll
        for (int i = 0; i < 4; ++i) {
          const int pc = i * 512 + tid, e = pc >> 3, x0 = (pc & 7) * 8;
#pragma unroll
          for (int j = 0; j < 8; ++j) {
            const int x = x0 + j, m = x >> 5, xl = x & 31, cl = (xl >> 2) & 3;
            const int slot = (2 * m + (cl >> 1)) * 16 + (cl & 1) * 8 + (xl >> 4) * 4 + (xl & 3);
            const unsigned wv = sb[0][i][j >> 1];
            *(u16*)(lb + slot * 528 + e * 2) = (u16)((j & 1) ? (wv >> 16) : (wv & 0xFFFFu));
          }
        }
      }
#pragma unroll
      for (int i = 0; i < 14; ++i) {
        const int pc = i * 512 + tid; *(v4u*)(lds + (pc >> 5) * 528 + (pc & 31) * 16) = sa[half][i];
        if (half == 0 && i == PREP_AT) { __builtin_amdgcn_sched_barrier(0); issue(1); __builtin_amdgcn_sched_barrier(0); }
      }
      BAR();
      TLP(1 + 2 * half);
      if (w < 7) {
        const int ks = ab * 7 + w;
        const unsigned char* pa = lds + (32 * w + n) * 528 + kg * 16;
        const unsigned char* pb = lb + n * 528 + kg * 16;
        f32x4 acc[4][2];
#pragma unroll
        for (int t = 0; t < 4; ++t) { acc[t][0] = f32x4{0, 0, 0, 0}; acc[t][1] = f32x4{0, 0, 0, 0}; }
#pragma unroll
        for (int ke = 0; ke < 8; ++ke) {
          const v4u A0 = *(const v4u*)(pa + ke * 64), A1 = *(const v4u*)(pa + 16 * 528 + ke * 64);
#pragma unroll
          for (int t = 0; t < 4; ++t) {
            const v4u B = *(const v4u*)(pb + t * 16 * 528 + ke * 64);
            acc[t][0] = mfma32(A0, B, acc[t][0]); acc[t][1] = mfma32(A1, B, acc[t][1]);
          }
        }
#pragma unroll
        for (int t = 0; t < 4; ++t) {
          const int tile = (half ? 0 : 448) + h * 28 + jb * 4 + t;
          *(v4u*)(ws + OFF_WC + ((size_t)tile * 14 + ks) * 1024 + lane * 16) = pk8(acc[t][0], acc[t][1]);
        }
      }
      TLP(2 + 2 * half);
    }
    return;
  }
#pragma unroll
  for (int half = 0; half < 2; ++half)
  {
    const int row0 = half * 1024 + (bb - 224) * 32 + w * 4;
    const int c = lane < 56 ? lane : 0;
    const int ks = c >> 2, kc = c & 3;
    v2u a4[4], b4[4];
#pragma unroll
    for (int rr = 0; rr < 4; ++rr) {
      a4[rr] = *(const v2u*)(p.x + (size_t)(row0 + rr) * 448 + 32 * ks + 4 * kc);
      b4[rr] = *(const v2u*)(p.x + (size_t)(row0 + rr) * 448 + 32 * ks + 16 + 4 * kc);
    }
#pragma unroll
    for (int rr = 0; rr < 4; ++rr) {
      const int row = row0 + rr;
      const v2u a = a4[rr], b2 = b4[rr];
      float v[8] = {bflo(a.x), bfhi(a.x), bflo(a.y), bfhi(a.y), bflo(b2.x), bfhi(b2.x), bflo(b2.y), bfhi(b2.y)};
      float s = 0.f;
#pragma unroll
      for (int j = 0; j < 8; ++j) s += v[j];
      if (lane >= 56) s = 0.f;
#pragma unroll
      for (int o = 1; o < 64; o <<= 1) s += __shfl_xor(s, o);
      const float mean = s * (1.f / 448.f);
      float q = 0.f;
#pragma unroll
      for (int j = 0; j < 8; ++j) { float d = v[j] - mean; q += d * d; }
      if (lane >= 56) q = 0.f;
#pragma unroll
      for (int o = 1; o < 64; o <<= 1) q += __shfl_xor(q, o);
      const float den = ln_den(q * (1.f / 448.f)), mb = rbf(mean);
      float o8[8];
#pragma unroll
      for (int j = 0; j < 8; ++j) o8[j] = rbf(v[j] - mb) / den;
      if (lane < 56)
        *(v4u*)(ws + OFF_XN + ((size_t)(row >> 4) * 14 + ks) * 1024 + (kc * 16 + (row & 15)) * 16) =
            v4u{pk2(o8[0], o8[1]), pk2(o8[2], o8[3]), pk2(o8[4], o8[5]), pk2(o8[6], o8[7])};
    }
  }
  TLP(4);
  if (b == 224) {
    if (w < 7) {
      const int pc = w * 64 + lane;
      const int c = (pc >> 4) * 2 + ((pc >> 2) & 1), row = ((pc >> 3) & 1) * 4 + (pc & 3);
      const int ks = c >> 2, kc = c & 3;
      v2u a = *(const v2u*)(p.mem0 + (size_t)row * 448 + 32 * ks + 4 * kc);
      v2u b2 = *(const v2u*)(p.mem0 + (size_t)row * 448 + 32 * ks + 16 + 4 * kc);
      const v4u v = v4u{nosent(a.x), nosent(a.y), nosent(b2.x), nosent(b2.y)};
      *(v4u*)(ws + OFF_EX + pc * 16) = v;
      *(v4u*)(ws + OFF_EX + 8192 + pc * 16) = v;
    } else {
      *(v4u*)(ws + OFF_EX + 7168 + lane * 16) = sent;
      *(v4u*)(ws + OFF_EX + 8192 + 7168 + lane * 16) = sent;
    }
  }
  if (b == 225) {
#pragma unroll
    for (int i = 0; i < 8; ++i) *(v4u*)(ws + OFF_CTR + (size_t)(i * 512 + tid) * 16) = v4u{0, 0, 0, 0};
  }
}
#define STAMP(k)
#define STAMPV(k, v)
#define STAMP_INIT()
#define STAMP_DUMP()
constexpr int L_MEM = 0;
constexpr int L_P1 = 16384;
constexpr int L_YB = 24576;
constexpr int N_S = 56, N_O = 56, N_X = 8, N_CHAIN = N_S + N_O + N_X;
constexpr int L_TAB2 = 22528;
constexpr int L_VOTE = 23808;
#define L_DUMMY (14336 + lane * 16)
#define ex_off(s, c) __builtin_amdgcn_readfirstlane(*(const int*)(smem + L_TAB2 + ((s) * 5 + (c)) * 4))
#define T_A 108
#define T_B 101
#define T_SW 26
#define T_DA 0
#define T_DB 0
#define T_R2 8
#define T_R1 0
#define T_Q1 15
#define T_Q2 70
#define PFD 4
#define T_EX 30
#define T_EXN 4
#define T_UP2 2
#define D_ARM 200
#define D_O 80
#define D_S 0
#define T_MIN 0
#define NREP 4
#define T_OG 0
#define T_PEN 35
#define T_UP 3
#define T_OKN 1000
constexpr int L_SCH = 23872;
DI unsigned rt32() { unsigned long long t; asm volatile("s_memrealtime %0\n\ts_waitcnt lgkmcnt(0)" : "=s"(t) :: "memory"); return (unsigned)t; }
DI unsigned wait_until(unsigned t) {
  int d = (int)(t - rt32());
  if (d < -2) return t - d;
  if (d > 3000) d = 3000;
  if (d > 24) { sleep_units(((d - 16) * 3) >> 3); d = (int)(t - rt32()); }
  if (d > 0 && d < 64) sleep_units((d * 3) >> 3);
  return t;
}
struct Sched { unsigned tE1, tE2; int m1, m2, ok, nf, late; };
DI void sched_step(Sched& y, unsigned word, int lane, int s) {
  if (s == 0) { y.tE1 = rt32(); y.tE2 = y.tE1; return; }
  int dv = lane < NREP ? (int)((word & ~3u) - (y.tE1 << 2)) >> 2 : -0x40000000;
  unsigned fl = lane < NREP ? (word & 3u) : 0u;
  if (s > 1 && __all(lane >= NREP || word == (y.tE1 << 2))) { dv = 0; fl = 0; }
  else {
#pragma unroll
    for (int o = 1; o < NREP; o <<= 1) { dv = max(dv, __shfl_xor(dv, o)); fl |= (unsigned)__shfl_xor((int)fl, o); }
    dv = __builtin_amdgcn_readfirstlane(dv); fl = __builtin_amdgcn_readfirstlane(fl);
  }
  unsigned base = y.tE2;
  if (dv > 0 || s == 1) base = y.tE1 + dv + T_B + y.m2 + T_PEN;
  if (s > 2) {
    if (!y.late) {
      if (fl & 1u) { y.m2 += T_UP; y.ok = 0; }
      else if (fl & 2u) { y.m1 += T_UP; y.ok = 0; }
      else if (++y.ok >= T_OKN) { y.ok = 0; if (y.m1 > T_MIN) y.m1 -= 1; if (y.m2 > T_MIN) y.m2 -= 1; }
    } else if (dv > 0) { y.m1 += T_UP2; y.m2 += T_UP2; }
  }
  y.late = dv > 0 ? 1 : 0;
  if (s == T_SW) { y.m1 -= T_DA; y.m2 -= T_DB; }
  const int ex = s < T_EXN ? T_EX : 0;
  y.tE1 = base + T_A + y.m1 + ex;
  y.tE2 = y.tE1 + T_B + y.m2 + (ex >> 1);
}
#define FAIL_DUMP(v)

DI int gather_poll(__amdgpu_buffer_rsrc_t rx, unsigned char* dst0, unsigned char* dst1, int o0, int o1, int ot, unsigned& word) {
  int spin = 0;
#pragma nounroll
  for (;;) {
    asm volatile("" ::: "memory");
    const v4u v0 = ld_sc1(rx, o0), v1 = ld_sc1(rx, o1);
    word = __builtin_amdgcn_raw_buffer_load_b32(rx, ot, 0, AUX_SC1);
    *(v4u*)dst0 = v0;
    *(v4u*)dst1 = v1;
    if (__builtin_expect(__all(umax3(umax4(v0), umax4(v1), word) != SENT), 1)) break;
    if (spin == 0) { o0 ^= 8192; o1 ^= 8192; ot ^= 8192; sleep_units(T_R2); }
    if (++spin >= SPIN_MAX) break;
    __builtin_amdgcn_s_sleep(2);
  }
  return spin;
}

DI void probe_place(const Params& p, const rsrc_t rx) {
  unsigned char* ws = p.ws;
  const int lane = threadIdx.x & 63, w = __builtin_amdgcn_readfirstlane(threadIdx.x >> 6);
  const unsigned a0 = (unsigned)(size_t)(ws + OFF_EX);
    if (w >= 4) {
      const int r = w - 4;
      int pr = -1;
      for (int k = 0; k < 64; ++k) if (pr < 0 && (int)(((a0 + (unsigned)k * 81920u) >> 21) - (a0 >> 21)) == r) pr = k * 5;
      unsigned res = 2;
      if (pr >= 0) {
        const int base = (int)OFF_EX + pr * 16384 + 7168;
        const v4u sent = {SENT, SENT, SENT, SENT};
        __builtin_amdgcn_raw_buffer_store_b128(sent, rx, base + lane * 16, 0, AUX_SC1);
        __builtin_amdgcn_raw_buffer_store_b128(sent, rx, base + 8192 + lane * 16, 0, AUX_SC1);
        asm volatile("s_waitcnt vmcnt(0)" ::: "memory");
        unsigned best[2] = {0xFFFFFFFFu, 0xFFFFFFFFu};
#pragma unroll
        for (int k = 0; k < 8; ++k) {
          unsigned long long t0, t1;
          asm volatile("s_memtime %0\n\ts_waitcnt lgkmcnt(0)" : "=s"(t0) :: "memory");
          unsigned v = __builtin_amdgcn_raw_buffer_load_b32(rx, base + (k & 1) * 8192 + (k >> 1) * 128, 0, AUX_SC1);
          asm volatile("s_waitcnt vmcnt(0)" :: "v"(v) : "memory");
          asm volatile("s_memtime %0\n\ts_waitcnt lgkmcnt(0)" : "=s"(t1) :: "memory");
          best[k & 1] = min(best[k & 1], (unsigned)(t1 - t0));
        }
        const unsigned b13 = ((a0 + (unsigned)pr * 16384u) >> 13) & 1;
        res = 2 + ((best[1] < best[0]) ? (b13 ^ 1) : b13);
      }
      if (lane == 0) __builtin_amdgcn_raw_buffer_store_b32(res, rx, (int)OFF_PTAB + r * 4, 0, AUX_SC1);
    }
}

DI void chain_role(const Params& p, unsigned char* smem, const rsrc_t rx, const int cu) {
  unsigned char* ws = p.ws;
  const int tid = threadIdx.x, lane = tid & 63, w = __builtin_amdgcn_readfirstlane(tid >> 6);
  const int kg = lane >> 4, n = lane & 15;
  const float sc = 0.0625f * 1.4426950408889634f;
  STAMP_INIT();
  auto startup = [&]() {
  {
    const v4u sent = {SENT, SENT, SENT, SENT};
    constexpr int EX_KB = (int)(64 * EX_STEP / 1024);
    for (int i = 16 + cu * 8 + w; i < EX_KB; i += N_CHAIN * 8) st4_sc1(sent, rx, (int)OFF_EX + i * 1024 + lane * 16);
    if (cu * 8 + w < 448) st4_sc1(sent, rx, (cu * 8 + w < 224 ? (int)OFF_KM + 62 * 114688 + (cu * 8 + w) * 1024 : (int)OFF_VM + 62 * 114688 + (cu * 8 + w - 224) * 1024) + lane * 16);
    DRAIN();
    __syncthreads();
    if (tid == 448) arrive(ws, rx, CTR_FX, (unsigned)N_CHAIN);
  }
  {
    const unsigned a0 = (unsigned)(size_t)(ws + OFF_EX);
    if (w == 0) {
      unsigned v = 0;
      for (int spin = 0; spin < SPIN_MAX; ++spin) {
        asm volatile("" ::: "memory");
        v = __builtin_amdgcn_raw_buffer_load_b32(rx, (int)OFF_PTAB + (lane & 3) * 4, 0, AUX_SC1);
        if (__all(v != 0)) break;
        __builtin_amdgcn_s_sleep(4);
      }
      ctr_wait(rx, CTR_FX, (unsigned)N_CHAIN, 4);
      if (lane < 4) *(unsigned*)(smem + L_VOTE + lane * 4) = v;
      if (lane < 2) *(unsigned*)(smem + L_SCH + lane * 4) = 0u;
    }
    __syncthreads();
    const unsigned a = a0 + (unsigned)tid * 16384u;
    const int reg = (int)((a >> 21) - (a0 >> 21)) & 3, b13 = (a >> 13) & 1;
    if (tid < 320) { const int nearbit = *(const int*)(smem + L_VOTE + reg * 4) & 1; *(int*)(smem + L_TAB2 + tid * 4) = (int)OFF_EX + tid * 16384 + ((nearbit != b13) ? 8192 : 0); }
    __syncthreads();
  }
  };
  const int lmoff = (kg >> 1) * 256 + ((n & 7) >> 2) * 128 + (kg & 1) * 64 + (n & 3) * 16;
  const int goff = (w - 4) * 2048 + lane * 16;
  if (cu < N_S) {
    const int c = cu, m7 = c / 7, r7 = c % 7;
    sleep_units(D_S);
    if (w < 4) {
      const int tile = 8 * c + 2 * w, hS = tile / 28, ksj = (tile % 28) >> 1;
      v4u Mf[2][KS];
#pragma unroll
      for (int q = 0; q < 2; ++q)
#pragma unroll
        for (int ks = 0; ks < KS; ++ks) Mf[q][ks] = *(const v4u*)(ws + OFF_WC + ((size_t)(tile + q) * 14 + ks) * 1024 + lane * 16);
      startup();
#pragma unroll
      for (int ks = 0; ks < KS; ++ks) { KEEP4(Mf[0][ks]); KEEP4(Mf[1][ks]); }
      unsigned char* pdst = smem + ((kg < 2 && n < 8) ? L_P1 + (kg * 8 + n) * 16 + w * 4 : L_DUMMY);
      const int kmo = (int)OFF_KM + ((hS * 14 + ksj) * 4 + kg) * 128 + n * 16;
#pragma nounroll
      for (int s = 0; s < NSEG; ++s) {
        const unsigned char* lm = smem + L_MEM + (s & 1) * 7168 + lmoff;
        BAR();
        STAMP(2);
        v4u mf[KS + 1];
#pragma unroll
        for (int ks = 0; ks < PFD; ++ks) mf[ks] = *(const v4u*)(lm + ks * 512);
        __builtin_amdgcn_sched_barrier(0);
        f32x4 a0 = {0, 0, 0, 0}, a1 = {0, 0, 0, 0};
#pragma unroll
        for (int ks = 0; ks < KS; ++ks) {
          if (ks + PFD < KS) mf[ks + PFD] = *(const v4u*)(lm + (ks + PFD) * 512);
          if (ks + PFD == KS) mf[KS] = *(const v4u*)(lm + ksj * 512);
          a0 = mfma32(Mf[0][ks], mf[ks], a0); a1 = mfma32(Mf[1][ks], mf[ks], a1);
          __builtin_amdgcn_sched_barrier(0);
        }
        const v4u piece = mf[KS];
        const v4u tb = pk8(a0, a1);
        const f32x4 d2 = mfma32(tb, piece, f32x4{0, 0, 0, 0});
        *(float*)(pdst) = d2.x; *(float*)(pdst + 256) = d2.y; *(float*)(pdst + 512) = d2.z; *(float*)(pdst + 768) = d2.w;
        STAMP(3);
        BAR();
        st4_sc1(nosent4(tb), rx, n < 8 ? kmo + s * (16 * 14 * 512) : OOB);
        DRAIN();
      }
      BAR();
    } else {
      startup();
      Sched sd = {0u, 0u, 0, 0, 0, 0, 1};
      const bool strad = r7 == 3;
      const int ii = lane & 7, key = ((lane >> 3) & 1) * 4 + (lane >> 4);
      const int hh = 2 * m7 + (r7 > 3 ? 1 : 0), pp = r7 > 3 ? r7 - 3 : r7;
      const int so1 = ((hh * 4 + pp) * 4 + (ii & 3)) * 32 + key * 4;
      const int so2 = (((2 * m7 + 1) * 4 + 0) * 4 + (ii & 3)) * 32 + key * 4;
      const unsigned char* psrc = smem + L_P1 + lane * 16;
#pragma nounroll
      for (int s = 0; s < NSEG; ++s) {
        const int o0 = ex_off(s, 0) + goff;
        const int eo2 = ex_off(s, 2), eo4 = ex_off(s, 4);
        const int eb = (ii >> 2) ? eo4 : eo2;
        const int sto1 = (w == 4 && s < NSEG - 1) ? eb + so1 : OOB;
        const int sto2 = (w == 4 && s < NSEG - 1 && strad) ? eb + so2 : OOB;
        unsigned char* d0 = smem + L_MEM + (s & 1) * 7168 + goff;
        unsigned char* d1 = w == 7 ? smem + L_DUMMY : d0 + 1024;
        const int ot = (s > 0 && lane < NREP) ? o0 - goff + 7168 + lane * 128 : OOB;
        unsigned word;
        KEEP4(sto1); KEEP4(sto2);
        if (s > 1) wait_until(sd.tE2);
        else if (s == 1) { int kn = 0; kv_ensure(rx, 0, kn, 8); sleep_units(T_Q2); }
        STAMP(0);
        const int spin = gather_poll(rx, d0, d1, o0, w == 7 ? OOB : o0 + 1024, ot, word);
        BAR();
        STAMP(1);
        if (w == 7) TL(s);
        BAR();
        const f32x4 a = *(const f32x4*)psrc;
        const float u0 = a.x + a.y, u1 = a.z + a.w;
        const float v1 = strad ? u0 : u0 + u1;
        st1_sc1(nosent(__float_as_uint(v1)), rx, sto1);
        st1_sc1(nosent(__float_as_uint(u1)), rx, sto2);
        st1_sc1(nosent(__float_as_uint(v1)), rx, sto1 ^ 8192);
        st1_sc1(nosent(__float_as_uint(u1)), rx, sto2 ^ 8192);
        __builtin_amdgcn_sched_barrier(0);
        STAMP(5);
        STAMPV(10, spin);
        sched_step(sd, word, lane, s);
        sd.nf += (s > 1 && spin) ? 1 : 0;
        if (w == 6 && lane == 0 && s > 0) arrive(ws, rx, CTR_M + (s - 1) * 128, 84u);
      }
      BAR();
      if (w == 6 && lane == 0) arrive(ws, rx, CTR_M + (NSEG - 1) * 128, 84u);
      FAIL_DUMP(sd.nf);
    }
  } else if (cu < N_S + N_O) {
    const int o_ = cu - N_S, rg = o_ / 28, ct = o_ % 28;
    sleep_units(D_O);
    v4u Nf[2][KS];
#pragma unroll
    for (int q = 0; q < 2; ++q)
#pragma unroll
      for (int ks = 0; ks < KS; ++ks) Nf[q][ks] = *(const v4u*)(ws + OFF_WC + ((size_t)(448 + (2 * w + q) * 28 + ct) * 14 + ks) * 1024 + lane * 16);
    startup();
#pragma unroll
    for (int ks = 0; ks < KS; ++ks) { KEEP4(Nf[0][ks]); KEEP4(Nf[1][ks]); }
    auto publish_y = [&](int s) {
      const int h = 4 * (w - 4) + kg;
      const unsigned char* ys = smem + L_YB + (h * 4) * 64 + n * 4;
      const v4u y = v4u{*(const unsigned*)(ys), *(const unsigned*)(ys + 64), *(const unsigned*)(ys + 128), *(const unsigned*)(ys + 192)};
      st4_sc1(nosent4(y), rx, rg == 0 ? (int)OFF_VM + ((s * 16 + h) * 28 + ct) * 256 + n * 16 : OOB);
    };
    auto compute_y = [&](int s) {
      const unsigned char* lm = smem + L_MEM + (s & 1) * 7168 + lmoff;
      v4u mf[KS];
#pragma unroll
      for (int ks = 0; ks < PFD; ++ks) mf[ks] = *(const v4u*)(lm + ks * 512);
      __builtin_amdgcn_sched_barrier(0);
      f32x4 a0 = {0, 0, 0, 0}, a1 = {0, 0, 0, 0};
#pragma unroll
      for (int ks = 0; ks < KS; ++ks) {
        if (ks + PFD < KS) mf[ks + PFD] = *(const v4u*)(lm + (ks + PFD) * 512);
        a0 = mfma32(mf[ks], Nf[0][ks], a0); a1 = mfma32(mf[ks], Nf[1][ks], a1);
        __builtin_amdgcn_sched_barrier(0);
      }
      v2u y0 = pk4(a0), y1 = pk4(a1);
      unsigned char* yd = smem + (kg < 2 ? L_YB + ((2 * w) * 4 + 2 * kg) * 64 + n * 4 : L_DUMMY);
      *(unsigned*)(yd) = y0.x; *(unsigned*)(yd + 64) = y0.y;
      *(unsigned*)(yd + 256) = y1.x; *(unsigned*)(yd + 320) = y1.y;
    };
    if (w < 4) {
      int nf = 0;
      v4u Vf[4];
      int known = 0;
      unsigned aold = 0;
      kv_ensure(rx, 0, known, 4);
#pragma unroll
      for (int g2 = 0; g2 < 4; ++g2) Vf[g2] = ld_sc1(rx, (int)OFF_VP + ((4 * w + g2) * 28 + ct) * 1024 + lane * 16);
      const int g = n >> 2, i4 = n & 3, hP = 4 * w + g;
      unsigned char* pdst = smem + L_P1 + (kg * 4 + i4) * 80 + (w * 4 + g) * 4;
      const unsigned char* ys = smem + L_YB + ((4 * w) * 4 + 2 * (kg & 1) + (kg >> 1)) * 64 + n * 4;
#pragma nounroll
      for (int s = 0; s < NSEG; ++s) {
        const int eb = ex_off(s, 1 + 2 * rg);
        int oe = eb + (hP * 4 + i4) * 64 + kg * 16;
        int ol = eb + 4096 + (hP * 4 + i4) * 4;
        int om = ex_off(s, 2 + 2 * rg) + ((hP * 4 + 2 * (kg >> 1)) * 4 + i4) * 32 + (kg & 1) * 16;
        KEEP4(oe); KEEP4(ol); KEEP4(om);
        BAR();
        STAMP(2);
        compute_y(s);
#pragma unroll
        for (int g2 = 0; g2 < 4; ++g2) KEEP4(Vf[g2]);
        if (w == 3 && lane == 0 && rg == 0 && s > 0) arrive_finish(rx, aold, CTR_M + (s - 1) * 128, 84u);
        if (s == NSEG - 1) break;
        unsigned tiss;
        for (;;) {
          const v2u tv = *(volatile const v2u*)(smem + L_SCH);
          tiss = __builtin_amdgcn_readfirstlane(tv.x);
          if (__builtin_amdgcn_readfirstlane(tv.y) == (unsigned)(s + 1)) break;
        }
        STAMP(3);
        if (s > 0) tiss = wait_until(tiss);
        else { sleep_units(T_Q1); tiss = rt32(); }
        STAMP(6);
        int spin = 0;
        f32x4 dres;
        v4u Yf = v4u{0, 0, 0, 0};
#pragma nounroll
        for (;;) {
          asm volatile("" ::: "memory");
          const v4u q0 = ld_sc1(rx, om), q1 = ld_sc1(rx, om + 128);
          const unsigned ls = __builtin_amdgcn_raw_buffer_load_b32(rx, ol, 0, AUX_SC1);
          const v4u ex = ld_sc1(rx, oe);
          __builtin_amdgcn_sched_barrier(0);
          if (spin == 0) {
            BAR();
            Yf = v4u{*(const unsigned*)(ys), *(const unsigned*)(ys + 256), *(const unsigned*)(ys + 512), *(const unsigned*)(ys + 768)};
            KEEP4(Yf);
          }
          __builtin_amdgcn_sched_barrier(0);
          const f32x4 sq = __builtin_bit_cast(f32x4, q0) + __builtin_bit_cast(f32x4, q1);
          auto ra = __builtin_amdgcn_permlane32_swap(__float_as_uint(sq.x), __float_as_uint(sq.z), false, false);
          auto rb = __builtin_amdgcn_permlane32_swap(__float_as_uint(sq.y), __float_as_uint(sq.w), false, false);
          const unsigned ra0 = ra[0], ra1 = ra[1], rb0 = rb[0], rb1 = rb[1];
          const float sm0 = __uint_as_float(ra0) + __uint_as_float(ra1), sm1 = __uint_as_float(rb0) + __uint_as_float(rb1);
          const float lse = __uint_as_float(ls);
          const float mx = xmax32(xmax16(fmaxf(sm0, sm1)));
          const float nb = -fmaxf(mx * sc, lse);
          const float e0 = __builtin_amdgcn_exp2f(__builtin_fmaf(sm0, sc, nb)), e1 = __builtin_amdgcn_exp2f(__builtin_fmaf(sm1, sc, nb));
          const float wx = __builtin_amdgcn_exp2f(lse + nb);
          const unsigned pk = pk2(e0, e1);
          const v4u pb = v4u{g == 0 ? pk : 0u, g == 1 ? pk : 0u, g == 2 ? pk : 0u, g == 3 ? pk : 0u};
          const f32x4 dx0 = mfma32(Vf[0], ex, f32x4{0, 0, 0, 0});
          const f32x4 dx1 = mfma32(Vf[1], ex, f32x4{0, 0, 0, 0});
          const f32x4 dx2 = mfma32(Vf[2], ex, f32x4{0, 0, 0, 0});
          const f32x4 dx3 = mfma32(Vf[3], ex, f32x4{0, 0, 0, 0});
          const f32x4 dm = mfma32(Yf, pb, f32x4{0, 0, 0, 0});
          const float inv = __builtin_amdgcn_rcpf(wx + xsum32(xsum16(e0 + e1)));
          const unsigned mxu = umax3(umax3(umax4(q0), umax4(q1), ls), umax4(ex), 0u);
          const f32x4 dxa = g < 2 ? (g == 0 ? dx0 : dx1) : (g == 2 ? dx2 : dx3);
          dres = (dxa * wx + dm) * inv;
          *(float*)(pdst) = dres.x; *(float*)(pdst + 1280) = dres.y; *(float*)(pdst + 2560) = dres.z; *(float*)(pdst + 3840) = dres.w;
          if (__builtin_expect(__all(mxu != SENT), 1)) break;
          if (spin == 0) { oe ^= 8192; ol ^= 8192; om ^= 8192; sleep_units(T_R1); } else __builtin_amdgcn_s_sleep(2);
          if (++spin >= SPIN_MAX) break;
          tiss = rt32();
        }
        *(unsigned*)(smem + L_SCH + 16 + w * 4) = (tiss << 2) | (spin ? 2u : 0u);
        DRAIN();
        STAMP(8);
        BAR();
        STAMPV(11, spin);
        nf += (s > 1 && spin) ? 1 : 0;
        if (w == 3 && lane == 0 && rg == 0) aold = arrive_issue(ws, CTR_M + s * 128);
        const int sn = s + 1 < NSEG - 1 ? s + 1 : s;
        kv_ensure(rx, sn >> 1, known, 4);
#pragma unroll
        for (int g2 = 0; g2 < 4; ++g2) Vf[g2] = ld_sc1(rx, (int)OFF_VP + (sn * 448 + (4 * w + g2) * 28 + ct) * 1024 + lane * 16);
      }
      BAR();
      BAR();
      FAIL_DUMP(nf);
    } else {
      Sched sd = {0u, 0u, 0, 0, 0, 0, 1};
      const unsigned char* pp = smem + L_P1 + lane * 80;
      const bool st = w == 4 && (kg & 1) == 0;
      const int eoff = ct * 256 + rg * 128 + ((((n >> 3) & 1) * 4 + (n & 3)) * 8 + ((n >> 2) & 1) * 4 + kg) * 2;
#pragma nounroll
      for (int s = 0; s < NSEG; ++s) {
        const int o0 = ex_off(s, 0) + goff;
        unsigned char* d0 = smem + L_MEM + (s & 1) * 7168 + goff;
        unsigned char* d1 = w == 7 ? smem + L_DUMMY : d0 + 1024;
        const int ot = (s > 0 && lane < NREP) ? o0 - goff + 7168 + lane * 128 : OOB;
        unsigned word;
        if (s > 1) wait_until(sd.tE2 + T_OG);
        else if (s == 1) sleep_units(8);
        STAMP(0);
        const int spin = gather_poll(rx, d0, d1, o0, w == 7 ? OOB : o0 + 1024, ot, word);
        __builtin_amdgcn_s_setprio(3);
        BAR();
        STAMP(1);
        sched_step(sd, word, lane, s);
        if (w == 4) *(volatile v2u*)(smem + L_SCH) = v2u{sd.tE1, (unsigned)(s + 1)};
        __builtin_amdgcn_s_setprio(0);

        sd.nf += (s > 1 && spin) ? 1 : 0;
        *(unsigned*)(smem + L_SCH + 32 + (w - 4) * 4) = (s > 1 && spin) ? 1u : 0u;
        compute_y(s);
        if (s == NSEG - 1) break;
        const int eon = ex_off(s + 1, 0);
        const int sto = st ? eon + eoff : ((w == 5 && lane == 0 && o_ % (56 / NREP) == 0) ? eon + 7168 + (o_ / (56 / NREP)) * 128 : OOB);
        BAR();
        publish_y(s);
        const v4u gf = *(const v4u*)(smem + L_SCH + 32);
        const unsigned gfl = gf.x | gf.y | gf.z | gf.w;
        const unsigned tref = sd.tE1 << 2;
        KEEP4(sto); KEEP4(gfl);
        DRAIN();
        if (w == 4) {
          BAR();
          STAMP(4);
          const f32x4 a = (*(const f32x4*)(pp) + *(const f32x4*)(pp + 16)) + (*(const f32x4*)(pp + 32) + *(const f32x4*)(pp + 48));
          const float v = (a.x + a.y) + (a.z + a.w);
          auto r = __builtin_amdgcn_permlane16_swap(__float_as_uint(v), __float_as_uint(v), false, false);
          const unsigned r0 = r[0], r1 = r[1];
          const unsigned ov = nosent(pk2(__uint_as_float(r0), __uint_as_float(r1)));
          st1_sc1(ov, rx, sto);
          st1_sc1(ov, rx, sto ^ 8192);
        } else {
          BAR();
          const v4u tw = *(const v4u*)(smem + L_SCH + 16);
          const unsigned tm = tref + (unsigned)max(max((int)(tw.x - tref), (int)(tw.y - tref)), max((int)(tw.z - tref), (int)(tw.w - tref)));
          const unsigned wd = nosent(tm | ((tw.x | tw.y | tw.z | tw.w) & 3u) | gfl);
          st1_sc1(wd, rx, sto);
          st1_sc1(wd, rx, sto ^ 8192);
        }
        __builtin_amdgcn_sched_barrier(0);
        STAMP(5);
        STAMPV(10, spin);
      }
      BAR();
      publish_y(NSEG - 1);
      DRAIN();
      BAR();
      if (w == 6 && lane == 0 && rg == 0) arrive(ws, rx, CTR_M + (NSEG - 1) * 128, 84u);
      FAIL_DUMP(w == 7 ? sd.m1 * 1000 + sd.m2 : sd.nf);
    }
  } else {
    startup();
    if (w < 4) {
      const int h = 2 * (cu - N_S - N_O) + (w >> 1), u = w & 1;
      v4u K[KS];
      int known = 0;
      kv_ensure(rx, 0, known, 4);
#pragma unroll
      for (int ks = 0; ks < KS; ++ks) K[ks] = ld_sc1(rx, (int)OFF_KP + ((h * 2 + u) * 14 + ks) * 1024 + lane * 16);
      unsigned char* xw = smem + (kg == 0 ? L_P1 + w * 128 + n * 8 : L_DUMMY);
      const unsigned char* xr = smem + L_P1 + (w ^ 1) * 128 + n * 8;
#pragma nounroll
      for (int s = 0; s < NSEG - 1; ++s) {
        const int eo1 = ex_off(s, 1), eo3 = ex_off(s, 3);
        const int eb = ((n >> 2) & 1) ? eo3 : eo1;
        const int so = n < 8 ? eb + (h * 4 + (n & 3)) * 64 + kg * 16 + u * 8 : OOB;
        const int lo = (lane < 8 && u == 0) ? eb + 4096 + (h * 4 + (n & 3)) * 4 : OOB;
        const unsigned char* lm = smem + L_MEM + (s & 1) * 7168 + lmoff;
#pragma unroll
        for (int ks = 0; ks < KS; ++ks) KEEP4(K[ks]);
        KEEP4(so); KEEP4(lo);
        BAR();
        STAMP(2);
        v4u mf[KS];
#pragma unroll
        for (int ks = 0; ks < PFD + 2; ++ks) mf[ks] = *(const v4u*)(lm + ks * 512);
        __builtin_amdgcn_sched_barrier(0);
        f32x4 a0 = {0, 0, 0, 0}, a1 = {0, 0, 0, 0};
#pragma unroll
        for (int ks = 0; ks < KS; ks += 2) {
          if (ks + PFD + 2 < KS) mf[ks + PFD + 2] = *(const v4u*)(lm + (ks + PFD + 2) * 512);
          if (ks + PFD + 3 < KS) mf[ks + PFD + 3] = *(const v4u*)(lm + (ks + PFD + 3) * 512);
          a0 = mfma32(K[ks], mf[ks], a0);
          a1 = mfma32(K[ks + 1], mf[ks + 1], a1);
          __builtin_amdgcn_sched_barrier(0);
        }
        a0 += a1;
        float xs[4] = {rbf(a0.x), rbf(a0.y), rbf(a0.z), rbf(a0.w)};
        const float mx = xmax32(xmax16(fmaxf(fmaxf(xs[0], xs[1]), fmaxf(xs[2], xs[3])))) * sc;
#pragma unroll
        for (int j = 0; j < 4; ++j) xs[j] = __builtin_amdgcn_exp2f(__builtin_fmaf(xs[j], sc, -mx));
        const float sum = xsum32(xsum16((xs[0] + xs[1]) + (xs[2] + xs[3])));
        *(f32x2*)xw = f32x2{mx, sum};
        BAR();
        const f32x2 ot = *(const f32x2*)xr;
        const float mm = fmaxf(mx, ot.x);
        const float fu = __builtin_amdgcn_exp2f(mx - mm), fo = __builtin_amdgcn_exp2f(ot.x - mm);
        const float pu = sum * fu, po = ot.y * fo;
        const float tot = u ? po + pu : pu + po;
        const float scl = fu * __builtin_amdgcn_rcpf(tot);
        const v2u o = nosent2(v2u{pk2(xs[0] * scl, xs[1] * scl), pk2(xs[2] * scl, xs[3] * scl)});
        st2_sc1(o, rx, so);
        const unsigned lse = nosent(__float_as_uint(__builtin_amdgcn_logf(tot) + mm));
        st1_sc1(lse, rx, lo);
        st2_sc1(o, rx, so ^ 8192);
        st1_sc1(lse, rx, lo ^ 8192);
        __builtin_amdgcn_sched_barrier(0);
        STAMP(5);
        const int sn = s + 1 < NSEG - 1 ? s + 1 : s;
        kv_ensure(rx, sn >> 1, known, 4);
#pragma unroll
        for (int ks = 0; ks < KS; ++ks) K[ks] = ld_sc1(rx, (int)OFF_KP + (((sn * 16 + h) * 2 + u) * 14 + ks) * 1024 + lane * 16);
      }
    } else {
      Sched sd = {0u, 0u, 0, 0, 0, 0, 1};
#pragma nounroll
      for (int s = 0; s < NSEG - 1; ++s) {
        const int o0 = ex_off(s, 0) + goff;
        unsigned char* d0 = smem + L_MEM + (s & 1) * 7168 + goff;
        unsigned char* d1 = w == 7 ? smem + L_DUMMY : d0 + 1024;
        const int ot = (s > 0 && lane < NREP) ? o0 - goff + 7168 + lane * 128 : OOB;
        unsigned word;
        if (s > 1) wait_until(sd.tE2);
        else if (s == 1) { int kn = 0; kv_ensure(rx, 0, kn, 8); sleep_units(T_Q2); }
        STAMP(0);
        const int spin = gather_poll(rx, d0, d1, o0, w == 7 ? OOB : o0 + 1024, ot, word);
        BAR();
        STAMP(1);
        BAR();
        STAMPV(10, spin);
        sched_step(sd, word, lane, s);
        sd.nf += (s > 1 && spin) ? 1 : 0;
      }
      FAIL_DUMP(sd.nf);
    }
  }
  STAMP_DUMP();
}
DI void phase_kv(const Params& p, unsigned char* lds, rsrc_t rw, int cg, int par) {
  unsigned char* ws = p.ws;
  const int tid = threadIdx.x, lane = tid & 63, w = __builtin_amdgcn_readfirstlane(tid >> 6);
  const int nt0 = cg * 16 + 2 * w;
  v4u st[7];
  {
    const unsigned char* src = ws + OFF_XN + (size_t)par * 57344 + tid * 16;
#pragma unroll
    for (int i = 0; i < 7; ++i) st[i] = *(const v4u*)(src + i * 8192);
  }
  v4u W[2][KS];
#pragma unroll
  for (int q = 0; q < 2; ++q)
#pragma unroll
    for (int ks = 0; ks < KS; ++ks) W[q][ks] = *(const v4u*)(ws + OFF_WC + ((size_t)(nt0 + q) * 14 + ks) * 1024 + lane * 16);
#pragma unroll
  for (int i = 0; i < 7; ++i) *(v4u*)(lds + i * 8192 + tid * 16) = st[i];
#pragma unroll
  for (int ks = 0; ks < KS; ++ks) { KEEP4(W[0][ks]); KEEP4(W[1][ks]); }
  BAR();
  const bool isK = cg < 28;
  const int kbase = (int)OFF_KP + (((nt0 / 28) * 2) * 14 + ((nt0 % 28) >> 1)) * 1024 + lane * 16;
  const int vbase = (int)OFF_VP + (nt0 - 448) * 1024 + lane * 16;
  int pend = -1;
  unsigned aold = 0;
#pragma nounroll
  for (int it = 0; it < 16; ++it) {
    const int hb = par + 2 * it;
    const unsigned char* cur = lds + (it & 1) * 57344 + lane * 16;
    {
      const int so = (int)OFF_XN + (it + 1 < 16 ? hb + 2 : hb) * 57344;
#pragma unroll
      for (int i = 0; i < 7; ++i) st[i] = __builtin_amdgcn_raw_buffer_load_b128(rw, tid * 16, so + i * 8192, 0);
    }
    __builtin_amdgcn_sched_barrier(0);
    f32x4 acc[4][2];
#pragma unroll
    for (int rt = 0; rt < 4; ++rt) { acc[rt][0] = f32x4{0, 0, 0, 0}; acc[rt][1] = f32x4{0, 0, 0, 0}; }
    if (isK) {
      v4u af[2][4];
#pragma unroll
      for (int rt = 0; rt < 4; ++rt) af[0][rt] = *(const v4u*)(cur + (rt * 14) * 1024);
#pragma unroll
      for (int ks = 0; ks < KS; ++ks) {
        if (ks + 1 < KS) {
#pragma unroll
          for (int rt = 0; rt < 4; ++rt) af[(ks + 1) & 1][rt] = *(const v4u*)(cur + (rt * 14 + ks + 1) * 1024);
        }
        __builtin_amdgcn_sched_barrier(0);
#pragma unroll
        for (int rt = 0; rt < 4; ++rt) {
          acc[rt][0] = mfma32(W[0][ks], af[ks & 1][rt], acc[rt][0]);
          acc[rt][1] = mfma32(W[1][ks], af[ks & 1][rt], acc[rt][1]);
        }
        __builtin_amdgcn_sched_barrier(0);
      }
    } else {
      v4u af[2][4];
#pragma unroll
      for (int rt = 0; rt < 4; ++rt) af[0][rt] = *(const v4u*)(cur + (rt * 14) * 1024);
#pragma unroll
      for (int ks = 0; ks < KS; ++ks) {
        if (ks + 1 < KS) {
#pragma unroll
          for (int rt = 0; rt < 4; ++rt) af[(ks + 1) & 1][rt] = *(const v4u*)(cur + (rt * 14 + ks + 1) * 1024);
        }
        __builtin_amdgcn_sched_barrier(0);
#pragma unroll
        for (int rt = 0; rt < 4; ++rt) {
          acc[rt][0] = mfma32(af[ks & 1][rt], W[0][ks], acc[rt][0]);
          acc[rt][1] = mfma32(af[ks & 1][rt], W[1][ks], acc[rt][1]);
        }
        __builtin_amdgcn_sched_barrier(0);
      }
    }
    __builtin_amdgcn_sched_barrier(0);
#pragma unroll
    for (int i = 0; i < 7; ++i) *(v4u*)(lds + ((it + 1) & 1) * 57344 + i * 8192 + tid * 16) = st[i];
    DRAIN();
    if (pend >= 0 && lane == 0) arrive_finish(rw, aold, pend, 56u);
    pend = -1;
    __builtin_amdgcn_sched_barrier(0);
    if (isK) {
#pragma unroll
      for (int rt = 0; rt < 4; ++rt) st4_sc1(pk8(acc[rt][0], acc[rt][1]), rw, kbase + ((hb * 2 + (rt >> 1)) * 32 + (rt & 1)) * 14336);
    } else {
#pragma unroll
      for (int q = 0; q < 2; ++q)
#pragma unroll
        for (int sg = 0; sg < 2; ++sg) st4_sc1(pk8(acc[2 * sg][q], acc[2 * sg + 1][q]), rw, vbase + ((hb * 2 + sg) * 448 + q) * 1024);
    }
    if (it == 0 || it == 15) DRAIN();
    BAR();
    if (w == 0) TL(200 + it);
    if (w == (it & 7)) {
      if (it == 0 || it == 15) { if (lane == 0) arrive(ws, rw, CTR_KV + hb * 128, 56u); }
      if (it >= 2) {
        if (it == 15) { if (lane == 0) arrive(ws, rw, CTR_KV + (hb - 2) * 128, 56u); }
        else { pend = CTR_KV + (hb - 2) * 128; if (lane == 0) aold = arrive_issue(ws, pend); }
      }
    }
  }
}

DI void gate_wait(rsrc_t rw, int rt, int nap, int lag, int own) {
  int off = (int)OFF_PM + rt * 4096 + ((blockIdx.x >> 3) & 31) * 128;
  if (__builtin_amdgcn_readfirstlane(ld1_sc1(rw, off)) != SENT) {
    off = own; lag = 4; nap = 8;
    if (__builtin_amdgcn_readfirstlane(ld1_sc1(rw, off)) != SENT) return;
  }
#pragma nounroll
  for (int spin = 0; spin < SPIN_MAX; ++spin) {
    sleep_units(nap);
    asm volatile("" ::: "memory");
    if (__builtin_amdgcn_readfirstlane(ld1_sc1(rw, off)) != SENT) break;
  }
  sleep_units(lag);
}
#define LAG_A2 10
#define LAG_F1 65
#define LAG_F2 140
#define RETRY 6
#define NAPT 4
#define NAP 6

#define TLX(k) do { if (rt == 127 || rt == 96) TL((rt == 127 ? 130 : 140) + (k)); } while (0)
DI void stage_a1(const Params& p, rsrc_t rw, int h) {
  unsigned char* ws = p.ws;
  const int tid = threadIdx.x, lane = tid & 63, w = __builtin_amdgcn_readfirstlane(tid >> 6);
  const int kg = lane >> 4, n = lane & 15, v = w & 1;
  const float sc = 0.0625f * 1.4426950408889634f;
  const float NEG = -3.0e38f;
  int known = 0;
  ctr_wait(rw, CTR_FX, 1u, NAP);
#pragma nounroll
  for (int rt = w; rt < 128; rt += 8) {
    const int s = rt >> 1;
    v4u xf[KS], kf[KS], k1[KS];
    kv_ensure(rw, s >> 1, known, NAP);
    const int kb = (int)OFF_KP + ((s * 16 + h) * 2) * 14336 + lane * 16;
#pragma unroll
    for (int ks = 0; ks < KS; ++ks) {
      xf[ks] = *(const v4u*)(ws + OFF_XN + ((size_t)rt * 14 + ks) * 1024 + lane * 16);
      kf[ks] = ld_sc1(rw, kb + ks * 1024);
    }
#pragma unroll
    for (int ks = 0; ks < KS; ++ks) k1[ks] = ld_sc1(rw, v ? kb + 14336 + ks * 1024 : OOB);
    __builtin_amdgcn_sched_barrier(0);
    f32x4 a0 = {0, 0, 0, 0}, a1 = {0, 0, 0, 0}, am = {0, 0, 0, 0};
#pragma unroll
    for (int ks = 0; ks < KS; ++ks) a0 = mfma32(kf[ks], xf[ks], a0);
#pragma unroll
    for (int ks = 0; ks < KS; ++ks) a1 = mfma32(k1[ks], xf[ks], a1);
    float xs[8];
#pragma unroll
    for (int r = 0; r < 4; ++r) {
      const bool diag = 4 * kg + r <= n;
      xs[r] = (v || diag) ? rbf(a0[r]) : NEG;
      xs[4 + r] = (v && diag) ? rbf(a1[r]) : NEG;
    }
    float mx = NEG;
#pragma unroll
    for (int j = 0; j < 8; ++j) mx = fmaxf(mx, xs[j]);
    const int tb = (int)OFF_KM + (s * 16 + h) * 7168 + kg * 128 + (n & 7) * 16;
    TLX(3);
    ctr_wait(rw, CTR_M + (s < NSEG - 2 ? s : s - 2) * 128, 84u, NAP);
    TLX(0);
#pragma nounroll
    for (int spin = 0; spin < SPIN_MAX; ++spin) {
      asm volatile("" ::: "memory");
      TLX(1);
      unsigned mxu = 0;
#pragma unroll
      for (int ks = 0; ks < KS; ++ks) kf[ks] = ld_sc1(rw, tb + ks * 512);
      if (s < NSEG - 2) break;
#pragma unroll
      for (int ks = 0; ks < KS; ++ks) mxu = max(mxu, umax4(kf[ks]));
      if (__all(mxu != SENT)) break;
      sleep_units(3);
    }
#pragma unroll
    for (int ks = 0; ks < KS; ++ks) am = mfma32(kf[ks], xf[ks], am);
    float mm[4];
#pragma unroll
    for (int r = 0; r < 4; ++r) { mm[r] = rbf(am[r]); mx = fmaxf(mx, mm[r]); }
    mx = xmax32(xmax16(mx));
    const float nb = -mx * sc;
    float sum = 0.f;
#pragma unroll
    for (int j = 0; j < 8; ++j) { xs[j] = xs[j] == NEG ? 0.f : __builtin_amdgcn_exp2f(__builtin_fmaf(xs[j], sc, nb)); sum += xs[j]; }
#pragma unroll
    for (int r = 0; r < 4; ++r) { mm[r] = __builtin_amdgcn_exp2f(__builtin_fmaf(mm[r], sc, nb)); if (kg < 2) sum += mm[r]; }
    sum = xsum32(xsum16(sum));
    const float inv = 1.f / sum;
    st4_sc1(nosent4(v4u{pk2(xs[0] * inv, xs[1] * inv), pk2(xs[2] * inv, xs[3] * inv), pk2(xs[4] * inv, xs[5] * inv), pk2(xs[6] * inv, xs[7] * inv)}),
            rw, (int)OFF_PX + (rt * 16 + h) * 1024 + lane * 16);
    st2_sc1(nosent2(v2u{pk2(mm[0] * inv, mm[1] * inv), pk2(mm[2] * inv, mm[3] * inv)}), rw,
            kg < 2 ? (int)OFF_PM + (rt * 4 + (h >> 2)) * 1024 + ((h & 3) * 16 + n) * 16 + kg * 8 : OOB);
    TL(rt);
  }
}

DI void stage_a2(const Params& p, rsrc_t rw, int ct) {
  const int tid = threadIdx.x, lane = tid & 63, w = __builtin_amdgcn_readfirstlane(tid >> 6);
  const int kg = lane >> 4, n = lane & 15;
  const int c = 2 * ct + (kg >> 1);
  const int col = 32 * (c >> 2) + 16 * (kg & 1) + 4 * (c & 3);
  const int hoff = (c >> 2) * 1024 + ((c & 3) * 16 + n) * 16 + (kg & 1) * 8;
  int known = 0;
  ctr_wait(rw, CTR_FX, 1u, NAP);
#pragma nounroll
  for (int rt = w; rt < 128; rt += 8) {
    const int s = rt >> 1;
    const v2u xv = *(const v2u*)(p.x + (size_t)(rt * 16 + n) * 448 + col);
    kv_ensure(rw, s >> 1, known, NAP);
    v4u vf[16], yf[4];
#pragma unroll
    for (int h = 0; h < 16; ++h) vf[h] = ld_sc1(rw, (int)OFF_VP + (s * 448 + h * 28 + ct) * 1024 + lane * 16);
    TLX(3);
    ctr_wait(rw, CTR_M + (s < NSEG - 2 ? s : s - 2) * 128, 84u, NAP);
    const int pb = (int)OFF_PX + rt * 16384 + lane * 16, mb = (int)OFF_PM + rt * 4096 + lane * 16;
    gate_wait(rw, rt, NAPT, LAG_A2, (int)OFF_PX + (rt * 16 + (ct & 15)) * 1024);
    TLX(0);
    f32x4 acc;
#pragma nounroll
    for (int spin = 0; spin < SPIN_MAX; ++spin) {
      asm volatile("" ::: "memory");
      TLX(1);
      v4u px[16], pm[4];
#pragma unroll
      for (int q = 0; q < 4; ++q) yf[q] = ld_sc1(rw, (int)OFF_VM + ((s * 16 + 4 * q + kg) * 28 + ct) * 256 + n * 16);
#pragma unroll
      for (int h = 0; h < 16; ++h) px[h] = ld_sc1(rw, pb + h * 1024);
#pragma unroll
      for (int q = 0; q < 4; ++q) pm[q] = ld_sc1(rw, mb + q * 1024);
      unsigned mxu = 0;
      f32x4 a0 = {0, 0, 0, 0}, a1 = {0, 0, 0, 0};
#pragma unroll
      for (int h = 0; h < 16; h += 2) {
        mxu = umax3(mxu, umax4(px[h]), umax4(px[h + 1]));
        a0 = mfma32(vf[h], px[h], a0); a1 = mfma32(vf[h + 1], px[h + 1], a1);
      }
#pragma unroll
      for (int q = 0; q < 4; q += 2) {
        mxu = umax3(mxu, umax3(umax4(pm[q]), umax4(pm[q + 1]), umax4(yf[q])), umax4(yf[q + 1]));
        a0 = mfma32(yf[q], pm[q], a0); a1 = mfma32(yf[q + 1], pm[q + 1], a1);
      }
      acc = a0 + a1;
      if (__all(mxu != SENT)) break;
      sleep_units(RETRY);
    }
    const float h0 = bflo(xv.x) + rbf(acc.x), h1 = bfhi(xv.x) + rbf(acc.y), h2 = bflo(xv.y) + rbf(acc.z), h3 = bfhi(xv.y) + rbf(acc.w);
    st2_sc1(nosent2(v2u{pk2(h0, h1), pk2(h2, h3)}), rw, (int)OFF_HF + rt * 14336 + hoff);
    TL(rt);
  }
}

DI void stage_f1(const Params& p, unsigned char* lds, rsrc_t rw, int kf) {
  const int tid = threadIdx.x, lane = tid & 63, w = __builtin_amdgcn_readfirstlane(tid >> 6);
  const int kg = lane >> 4, n = lane & 15;
  for (int f = w; f < 56; f += 8) {
    const int t = f / 14, ks = f % 14;
    const u16* src = ((t >> 1) ? p.wu : p.wg) + 32 * kf + 16 * (t & 1) + n;
    unsigned e[8];
#pragma unroll
    for (int jj = 0; jj < 8; ++jj) e[jj] = src[(size_t)pidx(ks, kg, jj) * 1792];
    *(v4u*)(lds + f * 1024 + lane * 16) = v4u{e[0] | (e[1] << 16), e[2] | (e[3] << 16), e[4] | (e[5] << 16), e[6] | (e[7] << 16)};
  }
  BAR();
  const v4u ones = {0x3F803F80u, 0x3F803F80u, 0x3F803F80u, 0x3F803F80u};
  f32x4 cs[4];
#pragma unroll
  for (int t = 0; t < 4; ++t) {
    cs[t] = f32x4{0, 0, 0, 0};
#pragma unroll
    for (int ks = 0; ks < KS; ++ks) cs[t] = mfma32(*(const v4u*)(lds + (t * 14 + ks) * 1024 + lane * 16), ones, cs[t]);
  }
#pragma nounroll
  for (int rt = w; rt < 128; rt += 8) {
    const int hb = (int)OFF_HF + rt * 14336 + lane * 16;
    gate_wait(rw, rt, NAPT, LAG_F1, (int)OFF_HF + rt * 14336 + (kf >> 1) * 512);
    TLX(0);
    v4u hf[KS];
#pragma nounroll
    for (int spin = 0; spin < SPIN_MAX; ++spin) {
      asm volatile("" ::: "memory");
      TLX(1);
      unsigned mxu = 0;
#pragma unroll
      for (int ks = 0; ks < KS; ++ks) hf[ks] = ld_sc1(rw, hb + ks * 1024);
#pragma unroll
      for (int ks = 0; ks < KS; ++ks) mxu = max(mxu, umax4(hf[ks]));
      if (__all(mxu != SENT)) break;
      sleep_units(RETRY);
    }
    TLX(2);
    f32x4 acc[4], sm = {0, 0, 0, 0}, gr = {0, 0, 0, 0};
#pragma unroll
    for (int t = 0; t < 4; ++t) acc[t] = f32x4{0, 0, 0, 0};
    v4u wf[2][4];
#pragma unroll
    for (int t = 0; t < 4; ++t) wf[0][t] = *(const v4u*)(lds + (t * 14) * 1024 + lane * 16);
#pragma unroll
    for (int ks = 0; ks < KS; ++ks) {
      if (ks + 1 < KS) {
#pragma unroll
        for (int t = 0; t < 4; ++t) wf[(ks + 1) & 1][t] = *(const v4u*)(lds + (t * 14 + ks + 1) * 1024 + lane * 16);
      }
      sm = mfma32(ones, hf[ks], sm);
      gr = mfma32(hf[ks], hf[ks], gr);
      __builtin_amdgcn_sched_barrier(0);
#pragma unroll
      for (int t = 0; t < 4; ++t) acc[t] = mfma32(wf[ks & 1][t], hf[ks], acc[t]);
      __builtin_amdgcn_sched_barrier(0);
    }
    float dg = (kg == (n >> 2)) ? ((n & 2) ? ((n & 1) ? gr.w : gr.z) : ((n & 1) ? gr.y : gr.x)) : 0.f;
    dg = xsum32(xsum16(dg));
    const float mean = sm.x * (1.f / 448.f);
    const float var = fmaxf(dg * (1.f / 448.f) - mean * mean, 0.f);
    const float mb = rbf(mean), rden = 1.f / ln_den(var);
    f32x4 o0, o1;
#pragma unroll
    for (int r = 0; r < 4; ++r) {
      o0[r] = rbf((acc[0][r] - mb * cs[0][r]) * rden) * rbf((acc[2][r] - mb * cs[2][r]) * rden);
      o1[r] = rbf((acc[1][r] - mb * cs[1][r]) * rden) * rbf((acc[3][r] - mb * cs[3][r]) * rden);
    }
    st4_sc1(nosent4(pk8(o0, o1)), rw, (int)OFF_AF + (rt * 56 + kf) * 1024 + lane * 16);
    TL(rt);
  }
}

DI void stage_f2(const Params& p, unsigned char* lds, rsrc_t rw, int ct) {
  const int tid = threadIdx.x, lane = tid & 63, w = __builtin_amdgcn_readfirstlane(tid >> 6);
  const int kg = lane >> 4, n = lane & 15;
  for (int kf = w; kf < 56; kf += 8) {
    const u16* src = p.wd + 16 * ct + n;
    unsigned e[8];
#pragma unroll
    for (int jj = 0; jj < 8; ++jj) e[jj] = src[(size_t)pidx(kf, kg, jj) * 448];
    *(v4u*)(lds + kf * 1024 + lane * 16) = v4u{e[0] | (e[1] << 16), e[2] | (e[3] << 16), e[4] | (e[5] << 16), e[6] | (e[7] << 16)};
  }
  BAR();
#pragma nounroll
  for (int rt = w; rt < 128; rt += 8) {
    const int ab = (int)OFF_AF + rt * 57344 + lane * 16;
    gate_wait(rw, rt, NAPT, LAG_F2, (int)OFF_AF + rt * 57344 + (2 * ct) * 1024);
    TLX(0);
    v2u hv = v2u{0, 0};
    f32x4 a0, a1;
#pragma nounroll
    for (int spin = 0; spin < SPIN_MAX; ++spin) {
      asm volatile("" ::: "memory");
      TLX(1);
      a0 = f32x4{0, 0, 0, 0}; a1 = f32x4{0, 0, 0, 0};
      hv = __builtin_amdgcn_raw_buffer_load_b64(rw, (int)OFF_HF + rt * 14336 + (ct >> 1) * 1024 + lane * 16 + (ct & 1) * 8, 0, AUX_SC1);
      v4u af[3][14];
#pragma unroll
      for (int bb = 0; bb < 2; ++bb)
#pragma unroll
        for (int k = 0; k < 14; ++k) af[bb][k] = ld_sc1(rw, ab + (bb * 14 + k) * 1024);
      unsigned mxu = max(hv.x, hv.y);
#pragma unroll
      for (int bb = 0; bb < 4; ++bb) {
        if (bb + 2 < 4) {
#pragma unroll
          for (int k = 0; k < 14; ++k) af[(bb + 2) % 3][k] = ld_sc1(rw, ab + ((bb + 2) * 14 + k) * 1024);
        }
        __builtin_amdgcn_sched_barrier(0);
#pragma unroll
        for (int k = 0; k < 14; k += 2) {
          mxu = umax3(mxu, umax4(af[bb % 3][k]), umax4(af[bb % 3][k + 1]));
          a0 = mfma32(*(const v4u*)(lds + (bb * 14 + k) * 1024 + lane * 16), af[bb % 3][k], a0);
          a1 = mfma32(*(const v4u*)(lds + (bb * 14 + k + 1) * 1024 + lane * 16), af[bb % 3][k + 1], a1);
        }
        __builtin_amdgcn_sched_barrier(0);
      }
      if (__all(mxu != SENT)) break;
      sleep_units(RETRY);
    }
    const f32x4 d = a0 + a1;
    const float o0 = bflo(hv.x) + rbf(d.x), o1 = bfhi(hv.x) + rbf(d.y), o2 = bflo(hv.y) + rbf(d.z), o3 = bfhi(hv.y) + rbf(d.w);
    *(v2u*)(p.out + (size_t)(rt * 16 + n) * 448 + 16 * ct + 4 * kg) = v2u{pk2(o0, o1), pk2(o2, o3)};
    TL(rt);
  }
}

DI void arm_stage(const Params& p, rsrc_t rw, int k, int nwg, int off0, int nkb, int ctr) {
  const int tid = threadIdx.x, lane = tid & 63, w = __builtin_amdgcn_readfirstlane(tid >> 6);
  const v4u sent = {SENT, SENT, SENT, SENT};
  for (int i = k * 8 + w; i < nkb; i += nwg * 8) st4_sc1(sent, rw, off0 + i * 1024 + lane * 16);
  DRAIN();
  __syncthreads();
  if (tid == 0) arrive(p.ws, rw, ctr, (unsigned)nwg);
}

constexpr int MAIN_LDS = 114688;
__global__ void __launch_bounds__(512, 2) k_main(Params p) {
  extern __shared__ __attribute__((aligned(16))) unsigned char smem[];
  const rsrc_t rw = __builtin_amdgcn_make_buffer_rsrc((void*)p.ws, 0, (int)WS_END, 0x00020000);
  const int b = blockIdx.x;
  if (threadIdx.x < 64) TL(255);
  const int j = (b >> 3) * 4 + (b & 3);
  if ((b & 7) < 4) {
    if (j < N_CHAIN) chain_role(p, smem, rw, j);
    else { if (j == N_CHAIN) probe_place(p, rw); sleep_units(400); arm_stage(p, rw, j - N_CHAIN, 8, (int)OFF_HF, (int)((OFF_XN - OFF_HF) / 1024), CTR_FS + 128); }
    return;
  }
  if (j < 112) phase_kv(p, smem, rw, j % 56, j / 56);
  else { sleep_units(D_ARM); }
  if (j >= 112) arm_stage(p, rw, j - 112, 16, (int)OFF_PX, (int)((OFF_HF - OFF_PX) / 1024), CTR_FS);
  ctr_wait(rw, CTR_FS, 16u, 8);
  if (j < 112) ctr_wait(rw, CTR_FS + 128, 8u, 8);
  if (j < 56) stage_f1(p, smem, rw, j);
  else if (j < 84) stage_f2(p, smem, rw, j - 56);
  else if (j < 112) stage_a2(p, rw, j - 84);
  else stage_a1(p, rw, j - 112);
}

extern "C" void kernel_launch(void* const* d_in, const int* in_sizes, int n_in, void* d_out, int out_size, void* d_ws, size_t ws_size,
                              hipStream_t stream) {
  Params p;
  p.x = (const u16*)d_in[0]; p.mem0 = (const u16*)d_in[1]; p.Wq = (const u16*)d_in[2]; p.Wk = (const u16*)d_in[3];
  p.Wv = (const u16*)d_in[4]; p.Wo = (const u16*)d_in[5];
  p.wg = (const u16*)d_in[8]; p.wu = (const u16*)d_in[9]; p.wd = (const u16*)d_in[10];
  p.out = (u16*)d_out; p.ws = (unsigned char*)d_ws;
  (void)hipFuncSetAttribute((const void*)k_main, hipFuncAttributeMaxDynamicSharedMemorySize, MAIN_LDS);
  (void)hipFuncSetAttribute((const void*)k_prep, hipFuncAttributeMaxDynamicSharedMemorySize, PREP_LDS);
  k_prep<<<256, 512, PREP_LDS, stream>>>(p);
  k_main<<<256, 512, MAIN_LDS, stream>>>(p);
}
```

```cpp
#include <hip/hip_runtime.h>
#include <hip/hip_bf16.h>
#include <stdint.h>

typedef short bf16x8 __attribute__((ext_vector_type(8)));
typedef float f32x4 __attribute__((ext_vector_type(4)));
typedef float f32x2 __attribute__((ext_vector_type(2)));
typedef __bf16 bf2_t __attribute__((ext_vector_type(2)));
typedef unsigned v4u __attribute__((ext_vector_type(4)));
typedef unsigned v2u __attribute__((ext_vector_type(2)));
typedef unsigned short u16;
typedef __amdgpu_buffer_rsrc_t rsrc_t;
#define DI __device__ __forceinline__

constexpr int NSEG = 64, KS = 14;
constexpr unsigned SENT = 0xFFFFFFFFu;
constexpr int AUX_SC1 = 16;
constexpr int SPIN_MAX = 20000;
constexpr int OOB = 0x7FFFFFF0;

constexpr size_t OFF_CTR = 0;
constexpr int CTR_KV = 0, CTR_M = 4096, OFF_PTAB = 16384, CTR_FX = 16640, CTR_FS = 16896;
constexpr size_t OFF_EX = 65536;
constexpr size_t EX_STEP = 5 * 16384;
constexpr size_t OFF_PX = OFF_EX + 64 * EX_STEP;
constexpr size_t OFF_PM = OFF_PX + (size_t)128 * 16 * 1024;
constexpr size_t OFF_HF = OFF_PM + (size_t)128 * 4 * 1024;
constexpr size_t OFF_AF = OFF_HF + (size_t)128 * 14 * 1024;
constexpr size_t OFF_XN = OFF_AF + (size_t)128 * 56 * 1024;
constexpr size_t OFF_WC = OFF_XN + (size_t)128 * 14 * 1024;
constexpr size_t OFF_KP = OFF_WC + (size_t)896 * 14 * 1024;
constexpr size_t OFF_VP = OFF_KP + (size_t)64 * 16 * 2 * 14 * 1024;
constexpr size_t OFF_KM = OFF_VP + (size_t)64 * 448 * 1024;
constexpr size_t OFF_VM = OFF_KM + (size_t)64 * 16 * 14 * 512;
constexpr size_t WS_END = OFF_VM + (size_t)64 * 16 * 28 * 256;
constexpr size_t FILL_BYTES = OFF_XN - OFF_EX;
static_assert(WS_END < (size_t)200 * 1024 * 1024, "ws");
static_assert(FILL_BYTES % 16384 == 0, "fill");

DI f32x4 mfma32(v4u a, v4u b, f32x4 c) {
  return __builtin_amdgcn_mfma_f32_16x16x32_bf16(__builtin_bit_cast(bf16x8, a), __builtin_bit_cast(bf16x8, b), c, 0, 0, 0);
}
DI unsigned pk2(float a, float b) {
  f32x2 v = {a, b};
  return __builtin_bit_cast(unsigned, __builtin_convertvector(v, bf2_t));
}
DI v2u pk4(f32x4 a) { return v2u{pk2(a.x, a.y), pk2(a.z, a.w)}; }
DI v4u pk8(f32x4 a, f32x4 b) { return v4u{pk2(a.x, a.y), pk2(a.z, a.w), pk2(b.x, b.y), pk2(b.z, b.w)}; }
DI float bflo(unsigned u) { return __uint_as_float(u << 16); }
DI float bfhi(unsigned u) { return __uint_as_float(u & 0xFFFF0000u); }
DI float rbf(float a) { return bflo(pk2(a, 0.f) & 0xFFFFu); }
DI int picol(int c, int cc) { return 32 * (c >> 2) + 16 * (cc >> 2) + 4 * (c & 3) + (cc & 3); }
DI int pidx(int ks, int kg, int jj) { return 32 * ks + 16 * (jj >> 2) + 4 * kg + (jj & 3); }
DI float xsum16(float v) { auto r = __builtin_amdgcn_permlane16_swap(__float_as_uint(v), __float_as_uint(v), false, false);
  const unsigned a = r[0], b = r[1]; return __uint_as_float(a) + __uint_as_float(b); }
DI float xsum32(float v) { auto r = __builtin_amdgcn_permlane32_swap(__float_as_uint(v), __float_as_uint(v), false, false);
  const unsigned a = r[0], b = r[1]; return __uint_as_float(a) + __uint_as_float(b); }
DI float xmax16(float v) { auto r = __builtin_amdgcn_permlane16_swap(__float_as_uint(v), __float_as_uint(v), false, false);
  const unsigned a = r[0], b = r[1]; return fmaxf(__uint_as_float(a), __uint_as_float(b)); }
DI float xmax32(float v) { auto r = __builtin_amdgcn_permlane32_swap(__float_as_uint(v), __float_as_uint(v), false, false);
  const unsigned a = r[0], b = r[1]; return fmaxf(__uint_as_float(a), __uint_as_float(b)); }
DI unsigned nosent(unsigned v) { return min(v, 0xFFFFFFFEu); }
DI v2u nosent2(v2u o) { return v2u{nosent(o.x), nosent(o.y)}; }
DI v4u nosent4(v4u o) { return v4u{nosent(o.x), nosent(o.y), nosent(o.z), nosent(o.w)}; }
DI unsigned umax3(unsigned a, unsigned b, unsigned c) { return max(max(a, b), c); }
DI unsigned umax4(v4u r) { return max(umax3(r.x, r.y, r.z), r.w); }
#define KEEP4(x) asm volatile("" :: "v"(x))
#define BAR() do { asm volatile("s_waitcnt lgkmcnt(0)" ::: "memory"); __builtin_amdgcn_s_barrier(); asm volatile("" ::: "memory"); } while (0)
#define DRAIN() asm volatile("s_waitcnt vmcnt(0)" ::: "memory")
DI void sleep_units(int n) { for (int d = 0; d < n; ++d) __builtin_amdgcn_s_sleep(1); }
DI v4u ld_sc1(rsrc_t r, int off) { return __builtin_amdgcn_raw_buffer_load_b128(r, off, 0, AUX_SC1); }
DI unsigned ld1_sc1(rsrc_t r, int off) { return __builtin_amdgcn_raw_buffer_load_b32(r, off, 0, AUX_SC1); }
DI void st4_sc1(v4u v, rsrc_t r, int off) { __builtin_amdgcn_raw_buffer_store_b128(v, r, off, 0, AUX_SC1); }
DI void st2_sc1(v2u v, rsrc_t r, int off) { __builtin_amdgcn_raw_buffer_store_b64(v, r, off, 0, AUX_SC1); }
DI void st1_sc1(unsigned v, rsrc_t r, int off) { __builtin_amdgcn_raw_buffer_store_b32(v, r, off, 0, AUX_SC1); }
constexpr int FLAG_D = 32768;
DI unsigned arrive_issue(unsigned char* ws, int off) { return __hip_atomic_fetch_add((unsigned*)(ws + off), 1u, __ATOMIC_RELAXED, __HIP_MEMORY_SCOPE_AGENT); }
DI void arrive_finish(rsrc_t r, unsigned old, int off, unsigned total) { if (old == total - 1u) st1_sc1(1u, r, off + FLAG_D); }
DI void arrive(unsigned char* ws, rsrc_t r, int off, unsigned total) { arrive_finish(r, arrive_issue(ws, off), off, total); }
DI void ctr_wait(rsrc_t r, int off, unsigned want, int nap) {
  off += FLAG_D; want = 1u;
#pragma nounroll
  for (int spin = 0; spin < SPIN_MAX; ++spin) {
    asm volatile("" ::: "memory");
    const unsigned v = __builtin_amdgcn_readfirstlane(ld1_sc1(r, off));
    if (v >= want) break;
    sleep_units(nap);
  }
  asm volatile("" ::: "memory");
}

DI void kv_ensure(rsrc_t r, int hb, int& known, int nap) {
  if (hb < known) return;
  const int lane = threadIdx.x & 63;
#pragma nounroll
  for (int spin = 0; spin < SPIN_MAX; ++spin) {
    asm volatile("" ::: "memory");
    const unsigned v = ld1_sc1(r, lane < 32 ? CTR_KV + FLAG_D + lane * 128 : OOB);
    const unsigned long long m = __ballot(v != 0u);
    known = __builtin_ctzll(~m);
    if (hb < known) break;
    sleep_units(nap);
  }
  asm volatile("" ::: "memory");
}

struct Params {
  const u16 *x, *mem0, *Wq, *Wk, *Wv, *Wo, *wg, *wu, *wd;
  u16* out;
  unsigned char* ws;
};

DI float ln_den(float var) { return rbf(sqrtf(rbf(rbf(var) + 1e-5f))); }

#define TL(idx)
constexpr int PREP_LDS = 288 * 528;
constexpr int FILL_KB = (int)(FILL_BYTES / 1024);
__global__ void __launch_bounds__(512, 2) k_prep(Params p) {
  extern __shared__ __attribute__((aligned(16))) unsigned char lds[];
  const int tid = threadIdx.x, lane = tid & 63, kg = lane >> 4, n = lane & 15;
  const int w = __builtin_amdgcn_readfirstlane(tid >> 6);
  unsigned char* ws = p.ws;
#define TLP(idx)
  const int xg = blockIdx.x & 7, ix = blockIdx.x >> 3;
  const int b = ix < 28 ? (2 * xg + ix / 14) * 14 + ix % 14 : 224 + xg * 4 + (ix - 28), bb = b;
  TLP(0);
  const v4u sent = {SENT, SENT, SENT, SENT};
  if (bb < 224) {
    const int h = bb / 14, r = bb % 14, ab = r / 7, jb = r % 7;
    v4u sa[2][14], sb[2][4];
#pragma unroll
    for (int half = 0; half < 2; ++half) {
      const u16* asrc = (half ? p.Wk : p.Wv) + (size_t)(224 * ab) * 4096 + h * 256;
      if (half) {
        const u16* bsrc = p.Wq + (size_t)(64 * jb) * 4096 + h * 256;
#pragma unroll
        for (int i = 0; i < 4; ++i) { const int pc = i * 512 + tid; sb[1][i] = *(const v4u*)(bsrc + (size_t)(pc >> 5) * 4096 + (pc & 31) * 8); }
      } else {
        const u16* bsrc = p.Wo + (size_t)(h * 256) * 448 + 64 * jb;
#pragma unroll
        for (int i = 0; i < 4; ++i) { const int pc = i * 512 + tid; sb[0][i] = *(const v4u*)(bsrc + (size_t)(pc >> 3) * 448 + (pc & 7) * 8); }
      }
#pragma unroll
      for (int i = 0; i < 14; ++i) { const int pc = i * 512 + tid; sa[half][i] = *(const v4u*)(asrc + (size_t)(pc >> 5) * 4096 + (pc & 31) * 8); }
    }
    __builtin_amdgcn_sched_barrier(0);
    unsigned char* lb = lds + 224 * 528;
#pragma unroll
    for (int half = 0; half < 2; ++half) {
      if (half) {
        __syncthreads();
#pragma unroll
        for (int i = 0; i < 4; ++i) { const int pc = i * 512 + tid; *(v4u*)(lb + (pc >> 5) * 528 + (pc & 31) * 16) = sb[1][i]; }
      } else {
#pragma unroll
        for (int i = 0; i < 4; ++i) {
          const int pc = i * 512 + tid, e = pc >> 3, x0 = (pc & 7) * 8;
#pragma unroll
          for (int j = 0; j < 8; ++j) {
            const int x = x0 + j, m = x >> 5, xl = x & 31, cl = (xl >> 2) & 3;
            const int slot = (2 * m + (cl >> 1)) * 16 + (cl & 1) * 8 + (xl >> 4) * 4 + (xl & 3);
            const unsigned wv = sb[0][i][j >> 1];
            *(u16*)(lb + slot * 528 + e * 2) = (u16)((j & 1) ? (wv >> 16) : (wv & 0xFFFFu));
          }
        }
      }
#pragma unroll
      for (int i = 0; i < 14; ++i) { const int pc = i * 512 + tid; *(v4u*)(lds + (pc >> 5) * 528 + (pc & 31) * 16) = sa[half][i]; }
      __syncthreads();
      TLP(1 + 2 * half);
      if (w < 7) {
        const int ks = ab * 7 + w;
        const unsigned char* pa = lds + (32 * w + n) * 528 + kg * 16;
        const unsigned char* pb = lb + n * 528 + kg * 16;
        f32x4 acc[4][2];
#pragma unroll
        for (int t = 0; t < 4; ++t) { acc[t][0] = f32x4{0, 0, 0, 0}; acc[t][1] = f32x4{0, 0, 0, 0}; }
#pragma unroll
        for (int ke = 0; ke < 8; ++ke) {
          const v4u A0 = *(const v4u*)(pa + ke * 64), A1 = *(const v4u*)(pa + 16 * 528 + ke * 64);
#pragma unroll
          for (int t = 0; t < 4; ++t) {
            const v4u B = *(const v4u*)(pb + t * 16 * 528 + ke * 64);
            acc[t][0] = mfma32(A0, B, acc[t][0]); acc[t][1] = mfma32(A1, B, acc[t][1]);
          }
        }
#pragma unroll
        for (int t = 0; t < 4; ++t) {
          const int tile = (half ? 0 : 448) + h * 28 + jb * 4 + t;
          *(v4u*)(ws + OFF_WC + ((size_t)tile * 14 + ks) * 1024 + lane * 16) = pk8(acc[t][0], acc[t][1]);
        }
      }
      TLP(2 + 2 * half);
    }
    return;
  }
#pragma unroll
  for (int half = 0; half < 2; ++half)
  {
    const int row0 = half * 1024 + (bb - 224) * 32 + w * 4;
    const int c = lane < 56 ? lane : 0;
    const int ks = c >> 2, kc = c & 3;
    v2u a4[4], b4[4];
#pragma unroll
    for (int rr = 0; rr < 4; ++rr) {
      a4[rr] = *(const v2u*)(p.x + (size_t)(row0 + rr) * 448 + 32 * ks + 4 * kc);
      b4[rr] = *(const v2u*)(p.x + (size_t)(row0 + rr) * 448 + 32 * ks + 16 + 4 * kc);
    }
#pragma unroll
    for (int rr = 0; rr < 4; ++rr) {
      const int row = row0 + rr;
      const v2u a = a4[rr], b2 = b4[rr];
      float v[8] = {bflo(a.x), bfhi(a.x), bflo(a.y), bfhi(a.y), bflo(b2.x), bfhi(b2.x), bflo(b2.y), bfhi(b2.y)};
      float s = 0.f;
#pragma unroll
      for (int j = 0; j < 8; ++j) s += v[j];
      if (lane >= 56) s = 0.f;
#pragma unroll
      for (int o = 1; o < 64; o <<= 1) s += __shfl_xor(s, o);
      const float mean = s * (1.f / 448.f);
      float q = 0.f;
#pragma unroll
      for (int j = 0; j < 8; ++j) { float d = v[j] - mean; q += d * d; }
      if (lane >= 56) q = 0.f;
#pragma unroll
      for (int o = 1; o < 64; o <<= 1) q += __shfl_xor(q, o);
      const float den = ln_den(q * (1.f / 448.f)), mb = rbf(mean);
      float o8[8];
#pragma unroll
      for (int j = 0; j < 8; ++j) o8[j] = rbf(v[j] - mb) / den;
      if (lane < 56)
        *(v4u*)(ws + OFF_XN + ((size_t)(row >> 4) * 14 + ks) * 1024 + (kc * 16 + (row & 15)) * 16) =
            v4u{pk2(o8[0], o8[1]), pk2(o8[2], o8[3]), pk2(o8[4], o8[5]), pk2(o8[6], o8[7])};
    }
  }
  TLP(4);
  if (b == 224) {
    if (w < 7) {
      const int pc = w * 64 + lane;
      const int c = (pc >> 4) * 2 + ((pc >> 2) & 1), row = ((pc >> 3) & 1) * 4 + (pc & 3);
      const int ks = c >> 2, kc = c & 3;
      v2u a = *(const v2u*)(p.mem0 + (size_t)row * 448 + 32 * ks + 4 * kc);
      v2u b2 = *(const v2u*)(p.mem0 + (size_t)row * 448 + 32 * ks + 16 + 4 * kc);
      const v4u v = v4u{nosent(a.x), nosent(a.y), nosent(b2.x), nosent(b2.y)};
      *(v4u*)(ws + OFF_EX + pc * 16) = v;
      *(v4u*)(ws + OFF_EX + 8192 + pc * 16) = v;
    } else {
      *(v4u*)(ws + OFF_EX + 7168 + lane * 16) = sent;
      *(v4u*)(ws + OFF_EX + 8192 + 7168 + lane * 16) = sent;
    }
  }
  if (b == 225) {
#pragma unroll
    for (int i = 0; i < 8; ++i) *(v4u*)(ws + OFF_CTR + (size_t)(i * 512 + tid) * 16) = v4u{0, 0, 0, 0};
  }
}
#define STAMP(k)
#define STAMPV(k, v)
#define STAMP_INIT()
#define STAMP_DUMP()
constexpr int L_MEM = 0;
constexpr int L_P1 = 16384;
constexpr int L_YB = 24576;
constexpr int N_S = 56, N_O = 56, N_X = 8, N_CHAIN = N_S + N_O + N_X;
constexpr int L_TAB2 = 22528;
constexpr int L_VOTE = 23808;
#define L_DUMMY (14336 + lane * 16)
#define ex_off(s, c) __builtin_amdgcn_readfirstlane(*(const int*)(smem + L_TAB2 + ((s) * 5 + (c)) * 4))
#define T_A 106
#define T_B 100
#define T_SW 26
#define T_DA 0
#define T_DB 0
#define T_R2 8
#define T_R1 0
#define T_Q1 15
#define T_Q2 70
#define PFD 4
#define T_EX 30
#define T_EXN 4
#define T_UP2 2
#define D_ARM 200
#define D_O 80
#define D_S 0
#define T_MIN 0
#define NREP 4
#define T_OG 0
#define T_PEN 35
#define T_UP 2
#define T_OKN 1000
constexpr int L_SCH = 23872;
DI unsigned rt32() { unsigned long long t; asm volatile("s_memrealtime %0\n\ts_waitcnt lgkmcnt(0)" : "=s"(t) :: "memory"); return (unsigned)t; }
DI unsigned wait_until(unsigned t) {
  int d = (int)(t - rt32());
  if (d < -2) return t - d;
  if (d > 3000) d = 3000;
  if (d > 24) { sleep_units(((d - 16) * 3) >> 3); d = (int)(t - rt32()); }
  if (d > 0 && d < 64) sleep_units((d * 3) >> 3);
  return t;
}
struct Sched { unsigned tE1, tE2; int m1, m2, ok, nf, late; };
DI void sched_step(Sched& y, unsigned word, int lane, int s) {
  if (s == 0) { y.tE1 = rt32(); y.tE2 = y.tE1; return; }
  int dv = lane < NREP ? (int)((word & ~3u) - (y.tE1 << 2)) >> 2 : -0x40000000;
  unsigned fl = lane < NREP ? (word & 3u) : 0u;
  if (s > 1 && __all(lane >= NREP || word == (y.tE1 << 2))) { dv = 0; fl = 0; }
  else {
#pragma unroll
    for (int o = 1; o < NREP; o <<= 1) { dv = max(dv, __shfl_xor(dv, o)); fl |= (unsigned)__shfl_xor((int)fl, o); }
    dv = __builtin_amdgcn_readfirstlane(dv); fl = __builtin_amdgcn_readfirstlane(fl);
  }
  unsigned base = y.tE2;
  if (dv > 0 || s == 1) base = y.tE1 + dv + T_B + y.m2 + T_PEN;
  if (s > 2) {
    if (!y.late) {
      if (fl & 1u) { y.m2 += T_UP; y.ok = 0; }
      else if (fl & 2u) { y.m1 += T_UP; y.ok = 0; }
      else if (++y.ok >= T_OKN) { y.ok = 0; if (y.m1 > T_MIN) y.m1 -= 1; if (y.m2 > T_MIN) y.m2 -= 1; }
    } else if (dv > 0) { y.m1 += T_UP2; y.m2 += T_UP2; }
  }
  y.late = dv > 0 ? 1 : 0;
  if (s == T_SW) { y.m1 -= T_DA; y.m2 -= T_DB; }
  const int ex = s < T_EXN ? T_EX : 0;
  y.tE1 = base + T_A + y.m1 + ex;
  y.tE2 = y.tE1 + T_B + y.m2 + (ex >> 1);
}
#define FAIL_DUMP(v)

DI int gather_poll(__amdgpu_buffer_rsrc_t rx, unsigned char* dst0, unsigned char* dst1, int o0, int o1, int ot, unsigned& word) {
  int spin = 0;
#pragma nounroll
  for (;;) {
    asm volatile("" ::: "memory");
    const v4u v0 = ld_sc1(rx, o0), v1 = ld_sc1(rx, o1);
    word = __builtin_amdgcn_raw_buffer_load_b32(rx, ot, 0, AUX_SC1);
    *(v4u*)dst0 = v0;
    *(v4u*)dst1 = v1;
    if (__builtin_expect(__all(umax3(umax4(v0), umax4(v1), word) != SENT), 1)) break;
    if (spin == 0) { o0 ^= 8192; o1 ^= 8192; ot ^= 8192; sleep_units(T_R2); }
    if (++spin >= SPIN_MAX) break;
    __builtin_amdgcn_s_sleep(2);
  }
  return spin;
}

DI void probe_place(const Params& p, const rsrc_t rx) {
  unsigned char* ws = p.ws;
  const int lane = threadIdx.x & 63, w = __builtin_amdgcn_readfirstlane(threadIdx.x >> 6);
  const unsigned a0 = (unsigned)(size_t)(ws + OFF_EX);
    if (w >= 4) {
      const int r = w - 4;
      int pr = -1;
      for (int k = 0; k < 64; ++k) if (pr < 0 && (int)(((a0 + (unsigned)k * 81920u) >> 21) - (a0 >> 21)) == r) pr = k * 5;
      unsigned res = 2;
      if (pr >= 0) {
        const int base = (int)OFF_EX + pr * 16384 + 7168;
        const v4u sent = {SENT, SENT, SENT, SENT};
        __builtin_amdgcn_raw_buffer_store_b128(sent, rx, base + lane * 16, 0, AUX_SC1);
        __builtin_amdgcn_raw_buffer_store_b128(sent, rx, base + 8192 + lane * 16, 0, AUX_SC1);
        asm volatile("s_waitcnt vmcnt(0)" ::: "memory");
        unsigned best[2] = {0xFFFFFFFFu, 0xFFFFFFFFu};
#pragma unroll
        for (int k = 0; k < 8; ++k) {
          unsigned long long t0, t1;
          asm volatile("s_memtime %0\n\ts_waitcnt lgkmcnt(0)" : "=s"(t0) :: "memory");
          unsigned v = __builtin_amdgcn_raw_buffer_load_b32(rx, base + (k & 1) * 8192 + (k >> 1) * 128, 0, AUX_SC1);
          asm volatile("s_waitcnt vmcnt(0)" :: "v"(v) : "memory");
          asm volatile("s_memtime %0\n\ts_waitcnt lgkmcnt(0)" : "=s"(t1) :: "memory");
          best[k & 1] = min(best[k & 1], (unsigned)(t1 - t0));
        }
        const unsigned b13 = ((a0 + (unsigned)pr * 16384u) >> 13) & 1;
        res = 2 + ((best[1] < best[0]) ? (b13 ^ 1) : b13);
      }
      if (lane == 0) __builtin_amdgcn_raw_buffer_store_b32(res, rx, (int)OFF_PTAB + r * 4, 0, AUX_SC1);
    }
}

DI void chain_role(const Params& p, unsigned char* smem, const rsrc_t rx, const int cu) {
  unsigned char* ws = p.ws;
  const int tid = threadIdx.x, lane = tid & 63, w = __builtin_amdgcn_readfirstlane(tid >> 6);
  const int kg = lane >> 4, n = lane & 15;
  const float sc = 0.0625f * 1.4426950408889634f;
  STAMP_INIT();
  auto startup = [&]() {
  {
    const v4u sent = {SENT, SENT, SENT, SENT};
    constexpr int EX_KB = (int)(64 * EX_STEP / 1024);
    for (int i = 16 + cu * 8 + w; i < EX_KB; i += N_CHAIN * 8) st4_sc1(sent, rx, (int)OFF_EX + i * 1024 + lane * 16);
    if (cu * 8 + w < 448) st4_sc1(sent, rx, (cu * 8 + w < 224 ? (int)OFF_KM + 62 * 114688 + (cu * 8 + w) * 1024 : (int)OFF_VM + 62 * 114688 + (cu * 8 + w - 224) * 1024) + lane * 16);
    DRAIN();
    __syncthreads();
    if (tid == 448) arrive(ws, rx, CTR_FX, (unsigned)N_CHAIN);
  }
  {
    const unsigned a0 = (unsigned)(size_t)(ws + OFF_EX);
    if (w == 0) {
      unsigned v = 0;
      for (int spin = 0; spin < SPIN_MAX; ++spin) {
        asm volatile("" ::: "memory");
        v = __builtin_amdgcn_raw_buffer_load_b32(rx, (int)OFF_PTAB + (lane & 3) * 4, 0, AUX_SC1);
        if (__all(v != 0)) break;
        __builtin_amdgcn_s_sleep(4);
      }
      ctr_wait(rx, CTR_FX, (unsigned)N_CHAIN, 4);
      if (lane < 4) *(unsigned*)(smem + L_VOTE + lane * 4) = v;
      if (lane < 2) *(unsigned*)(smem + L_SCH + lane * 4) = 0u;
    }
    __syncthreads();
    const unsigned a = a0 + (unsigned)tid * 16384u;
    const int reg = (int)((a >> 21) - (a0 >> 21)) & 3, b13 = (a >> 13) & 1;
    if (tid < 320) { const int nearbit = *(const int*)(smem + L_VOTE + reg * 4) & 1; *(int*)(smem + L_TAB2 + tid * 4) = (int)OFF_EX + tid * 16384 + ((nearbit != b13) ? 8192 : 0); }
    __syncthreads();
  }
  };
  const int lmoff = (kg >> 1) * 256 + ((n & 7) >> 2) * 128 + (kg & 1) * 64 + (n & 3) * 16;
  const int goff = (w - 4) * 2048 + lane * 16;
  if (cu < N_S) {
    const int c = cu, m7 = c / 7, r7 = c % 7;
    sleep_units(D_S);
    if (w < 4) {
      const int tile = 8 * c + 2 * w, hS = tile / 28, ksj = (tile % 28) >> 1;
      v4u Mf[2][KS];
#pragma unroll
      for (int q = 0; q < 2; ++q)
#pragma unroll
        for (int ks = 0; ks < KS; ++ks) Mf[q][ks] = *(const v4u*)(ws + OFF_WC + ((size_t)(tile + q) * 14 + ks) * 1024 + lane * 16);
      startup();
#pragma unroll
      for (int ks = 0; ks < KS; ++ks) { KEEP4(Mf[0][ks]); KEEP4(Mf[1][ks]); }
      unsigned char* pdst = smem + ((kg < 2 && n < 8) ? L_P1 + (kg * 8 + n) * 16 + w * 4 : L_DUMMY);
      const int kmo = (int)OFF_KM + ((hS * 14 + ksj) * 4 + kg) * 128 + n * 16;
#pragma nounroll
      for (int s = 0; s < NSEG; ++s) {
        const unsigned char* lm = smem + L_MEM + (s & 1) * 7168 + lmoff;
        BAR();
        STAMP(2);
        v4u mf[KS + 1];
#pragma unroll
        for (int ks = 0; ks < PFD; ++ks) mf[ks] = *(const v4u*)(lm + ks * 512);
        __builtin_amdgcn_sched_barrier(0);
        f32x4 a0 = {0, 0, 0, 0}, a1 = {0, 0, 0, 0};
#pragma unroll
        for (int ks = 0; ks < KS; ++ks) {
          if (ks + PFD < KS) mf[ks + PFD] = *(const v4u*)(lm + (ks + PFD) * 512);
          if (ks + PFD == KS) mf[KS] = *(const v4u*)(lm + ksj * 512);
          a0 = mfma32(Mf[0][ks], mf[ks], a0); a1 = mfma32(Mf[1][ks], mf[ks], a1);
          __builtin_amdgcn_sched_barrier(0);
        }
        const v4u piece = mf[KS];
        const v4u tb = pk8(a0, a1);
        const f32x4 d2 = mfma32(tb, piece, f32x4{0, 0, 0, 0});
        *(float*)(pdst) = d2.x; *(float*)(pdst + 256) = d2.y; *(float*)(pdst + 512) = d2.z; *(float*)(pdst + 768) = d2.w;
        STAMP(3);
        BAR();
        st4_sc1(nosent4(tb), rx, n < 8 ? kmo + s * (16 * 14 * 512) : OOB);
        DRAIN();
      }
      BAR();
    } else {
      startup();
      Sched sd = {0u, 0u, 0, 0, 0, 0, 1};
      const bool strad = r7 == 3;
      const int ii = lane & 7, key = ((lane >> 3) & 1) * 4 + (lane >> 4);
      const int hh = 2 * m7 + (r7 > 3 ? 1 : 0), pp = r7 > 3 ? r7 - 3 : r7;
      const int so1 = ((hh * 4 + pp) * 4 + (ii & 3)) * 32 + key * 4;
      const int so2 = (((2 * m7 + 1) * 4 + 0) * 4 + (ii & 3)) * 32 + key * 4;
      const unsigned char* psrc = smem + L_P1 + lane * 16;
#pragma nounroll
      for (int s = 0; s < NSEG; ++s) {
        const int o0 = ex_off(s, 0) + goff;
        const int eo2 = ex_off(s, 2), eo4 = ex_off(s, 4);
        const int eb = (ii >> 2) ? eo4 : eo2;
        const int sto1 = (w == 4 && s < NSEG - 1) ? eb + so1 : OOB;
        const int sto2 = (w == 4 && s < NSEG - 1 && strad) ? eb + so2 : OOB;
        unsigned char* d0 = smem + L_MEM + (s & 1) * 7168 + goff;
        unsigned char* d1 = w == 7 ? smem + L_DUMMY : d0 + 1024;
        const int ot = (s > 0 && lane < NREP) ? o0 - goff + 7168 + lane * 128 : OOB;
        unsigned word;
        KEEP4(sto1); KEEP4(sto2);
        if (s > 1) wait_until(sd.tE2);
        else if (s == 1) { int kn = 0; kv_ensure(rx, 0, kn, 8); sleep_units(T_Q2); }
        STAMP(0);
        const int spin = gather_poll(rx, d0, d1, o0, w == 7 ? OOB : o0 + 1024, ot, word);
        BAR();
        STAMP(1);
        if (w == 7) TL(s);
        BAR();
        const f32x4 a = *(const f32x4*)psrc;
        const float u0 = a.x + a.y, u1 = a.z + a.w;
        const float v1 = strad ? u0 : u0 + u1;
        st1_sc1(nosent(__float_as_uint(v1)), rx, sto1);
        st1_sc1(nosent(__float_as_uint(u1)), rx, sto2);
        st1_sc1(nosent(__float_as_uint(v1)), rx, sto1 ^ 8192);
        st1_sc1(nosent(__float_as_uint(u1)), rx, sto2 ^ 8192);
        __builtin_amdgcn_sched_barrier(0);
        STAMP(5);
        STAMPV(10, spin);
        sched_step(sd, word, lane, s);
        sd.nf += (s > 1 && spin) ? 1 : 0;
        if (w == 6 && lane == 0 && s > 0) arrive(ws, rx, CTR_M + (s - 1) * 128, 84u);
      }
      BAR();
      if (w == 6 && lane == 0) arrive(ws, rx, CTR_M + (NSEG - 1) * 128, 84u);
      FAIL_DUMP(sd.nf);
    }
  } else if (cu < N_S + N_O) {
    const int o_ = cu - N_S, rg = o_ / 28, ct = o_ % 28;
    sleep_units(D_O);
    v4u Nf[2][KS];
#pragma unroll
    for (int q = 0; q < 2; ++q)
#pragma unroll
      for (int ks = 0; ks < KS; ++ks) Nf[q][ks] = *(const v4u*)(ws + OFF_WC + ((size_t)(448 + (2 * w + q) * 28 + ct) * 14 + ks) * 1024 + lane * 16);
    startup();
#pragma unroll
    for (int ks = 0; ks < KS; ++ks) { KEEP4(Nf[0][ks]); KEEP4(Nf[1][ks]); }
    auto publish_y = [&](int s) {
      const int h = 4 * (w - 4) + kg;
      const unsigned char* ys = smem + L_YB + (h * 4) * 64 + n * 4;
      const v4u y = v4u{*(const unsigned*)(ys), *(const unsigned*)(ys + 64), *(const unsigned*)(ys + 128), *(const unsigned*)(ys + 192)};
      st4_sc1(nosent4(y), rx, rg == 0 ? (int)OFF_VM + ((s * 16 + h) * 28 + ct) * 256 + n * 16 : OOB);
    };
    auto compute_y = [&](int s) {
      const unsigned char* lm = smem + L_MEM + (s & 1) * 7168 + lmoff;
      v4u mf[KS];
#pragma unroll
      for (int ks = 0; ks < PFD; ++ks) mf[ks] = *(const v4u*)(lm + ks * 512);
      __builtin_amdgcn_sched_barrier(0);
      f32x4 a0 = {0, 0, 0, 0}, a1 = {0, 0, 0, 0};
#pragma unroll
      for (int ks = 0; ks < KS; ++ks) {
        if (ks + PFD < KS) mf[ks + PFD] = *(const v4u*)(lm + (ks + PFD) * 512);
        a0 = mfma32(mf[ks], Nf[0][ks], a0); a1 = mfma32(mf[ks], Nf[1][ks], a1);
        __builtin_amdgcn_sched_barrier(0);
      }
      v2u y0 = pk4(a0), y1 = pk4(a1);
      unsigned char* yd = smem + (kg < 2 ? L_YB + ((2 * w) * 4 + 2 * kg) * 64 + n * 4 : L_DUMMY);
      *(unsigned*)(yd) = y0.x; *(unsigned*)(yd + 64) = y0.y;
      *(unsigned*)(yd + 256) = y1.x; *(unsigned*)(yd + 320) = y1.y;
    };
    if (w < 4) {
      int nf = 0;
      v4u Vf[4];
      int known = 0;
      unsigned aold = 0;
      kv_ensure(rx, 0, known, 4);
#pragma unroll
      for (int g2 = 0; g2 < 4; ++g2) Vf[g2] = ld_sc1(rx, (int)OFF_VP + ((4 * w + g2) * 28 + ct) * 1024 + lane * 16);
      const int g = n >> 2, i4 = n & 3, hP = 4 * w + g;
      unsigned char* pdst = smem + L_P1 + (kg * 4 + i4) * 80 + (w * 4 + g) * 4;
      const unsigned char* ys = smem + L_YB + ((4 * w) * 4 + 2 * (kg & 1) + (kg >> 1)) * 64 + n * 4;
#pragma nounroll
      for (int s = 0; s < NSEG; ++s) {
        const int eb = ex_off(s, 1 + 2 * rg);
        int oe = eb + (hP * 4 + i4) * 64 + kg * 16;
        int ol = eb + 4096 + (hP * 4 + i4) * 4;
        int om = ex_off(s, 2 + 2 * rg) + ((hP * 4 + 2 * (kg >> 1)) * 4 + i4) * 32 + (kg & 1) * 16;
        KEEP4(oe); KEEP4(ol); KEEP4(om);
        BAR();
        STAMP(2);
        compute_y(s);
#pragma unroll
        for (int g2 = 0; g2 < 4; ++g2) KEEP4(Vf[g2]);
        if (w == 3 && lane == 0 && rg == 0 && s > 0) arrive_finish(rx, aold, CTR_M + (s - 1) * 128, 84u);
        if (s == NSEG - 1) break;
        unsigned tiss;
        for (;;) {
          const v2u tv = *(volatile const v2u*)(smem + L_SCH);
          tiss = __builtin_amdgcn_readfirstlane(tv.x);
          if (__builtin_amdgcn_readfirstlane(tv.y) == (unsigned)(s + 1)) break;
        }
        STAMP(3);
        if (s > 0) tiss = wait_until(tiss);
        else { sleep_units(T_Q1); tiss = rt32(); }
        STAMP(6);
        int spin = 0;
        f32x4 dres;
        v4u Yf = v4u{0, 0, 0, 0};
#pragma nounroll
        for (;;) {
          asm volatile("" ::: "memory");
          const v4u q0 = ld_sc1(rx, om), q1 = ld_sc1(rx, om + 128);
          const unsigned ls = __builtin_amdgcn_raw_buffer_load_b32(rx, ol, 0, AUX_SC1);
          const v4u ex = ld_sc1(rx, oe);
          __builtin_amdgcn_sched_barrier(0);
          if (spin == 0) {
            BAR();
            Yf = v4u{*(const unsigned*)(ys), *(const unsigned*)(ys + 256), *(const unsigned*)(ys + 512), *(const unsigned*)(ys + 768)};
            KEEP4(Yf);
          }
          __builtin_amdgcn_sched_barrier(0);
          const f32x4 sq = __builtin_bit_cast(f32x4, q0) + __builtin_bit_cast(f32x4, q1);
          auto ra = __builtin_amdgcn_permlane32_swap(__float_as_uint(sq.x), __float_as_uint(sq.z), false, false);
          auto rb = __builtin_amdgcn_permlane32_swap(__float_as_uint(sq.y), __float_as_uint(sq.w), false, false);
          const unsigned ra0 = ra[0], ra1 = ra[1], rb0 = rb[0], rb1 = rb[1];
          const float sm0 = __uint_as_float(ra0) + __uint_as_float(ra1), sm1 = __uint_as_float(rb0) + __uint_as_float(rb1);
          const float lse = __uint_as_float(ls);
          const float mx = xmax32(xmax16(fmaxf(sm0, sm1)));
          const float nb = -fmaxf(mx * sc, lse);
          const float e0 = __builtin_amdgcn_exp2f(__builtin_fmaf(sm0, sc, nb)), e1 = __builtin_amdgcn_exp2f(__builtin_fmaf(sm1, sc, nb));
          const float wx = __builtin_amdgcn_exp2f(lse + nb);
          const unsigned pk = pk2(e0, e1);
          const v4u pb = v4u{g == 0 ? pk : 0u, g == 1 ? pk : 0u, g == 2 ? pk : 0u, g == 3 ? pk : 0u};
          const f32x4 dx0 = mfma32(Vf[0], ex, f32x4{0, 0, 0, 0});
          const f32x4 dx1 = mfma32(Vf[1], ex, f32x4{0, 0, 0, 0});
          const f32x4 dx2 = mfma32(Vf[2], ex, f32x4{0, 0, 0, 0});
          const f32x4 dx3 = mfma32(Vf[3], ex, f32x4{0, 0, 0, 0});
          const f32x4 dm = mfma32(Yf, pb, f32x4{0, 0, 0, 0});
          const float inv = __builtin_amdgcn_rcpf(wx + xsum32(xsum16(e0 + e1)));
          const unsigned mxu = umax3(umax3(umax4(q0), umax4(q1), ls), umax4(ex), 0u);
          const f32x4 dxa = g < 2 ? (g == 0 ? dx0 : dx1) : (g == 2 ? dx2 : dx3);
          dres = (dxa * wx + dm) * inv;
          *(float*)(pdst) = dres.x; *(float*)(pdst + 1280) = dres.y; *(float*)(pdst + 2560) = dres.z; *(float*)(pdst + 3840) = dres.w;
          if (__builtin_expect(__all(mxu != SENT), 1)) break;
          if (spin == 0) { oe ^= 8192; ol ^= 8192; om ^= 8192; sleep_units(T_R1); } else __builtin_amdgcn_s_sleep(2);
          if (++spin >= SPIN_MAX) break;
          tiss = rt32();
        }
        *(unsigned*)(smem + L_SCH + 16 + w * 4) = (tiss << 2) | (spin ? 2u : 0u);
        DRAIN();
        STAMP(8);
        BAR();
        STAMPV(11, spin);
        nf += (s > 1 && spin) ? 1 : 0;
        if (w == 3 && lane == 0 && rg == 0) aold = arrive_issue(ws, CTR_M + s * 128);
        const int sn = s + 1 < NSEG - 1 ? s + 1 : s;
        kv_ensure(rx, sn >> 1, known, 4);
#pragma unroll
        for (int g2 = 0; g2 < 4; ++g2) Vf[g2] = ld_sc1(rx, (int)OFF_VP + (sn * 448 + (4 * w + g2) * 28 + ct) * 1024 + lane * 16);
      }
      BAR();
      BAR();
      FAIL_DUMP(nf);
    } else {
      Sched sd = {0u, 0u, 0, 0, 0, 0, 1};
      const unsigned char* pp = smem + L_P1 + lane * 80;
      const bool st = w == 4 && (kg & 1) == 0;
      const int eoff = ct * 256 + rg * 128 + ((((n >> 3) & 1) * 4 + (n & 3)) * 8 + ((n >> 2) & 1) * 4 + kg) * 2;
#pragma nounroll
      for (int s = 0; s < NSEG; ++s) {
        const int o0 = ex_off(s, 0) + goff;
        unsigned char* d0 = smem + L_MEM + (s & 1) * 7168 + goff;
        unsigned char* d1 = w == 7 ? smem + L_DUMMY : d0 + 1024;
        const int ot = (s > 0 && lane < NREP) ? o0 - goff + 7168 + lane * 128 : OOB;
        unsigned word;
        if (s > 1) wait_until(sd.tE2 + T_OG);
        else if (s == 1) sleep_units(8);
        STAMP(0);
        const int spin = gather_poll(rx, d0, d1, o0, w == 7 ? OOB : o0 + 1024, ot, word);
        __builtin_amdgcn_s_setprio(3);
        BAR();
        STAMP(1);
        sched_step(sd, word, lane, s);
        if (w == 4) *(volatile v2u*)(smem + L_SCH) = v2u{sd.tE1, (unsigned)(s + 1)};
        __builtin_amdgcn_s_setprio(0);

        sd.nf += (s > 1 && spin) ? 1 : 0;
        *(unsigned*)(smem + L_SCH + 32 + (w - 4) * 4) = (s > 1 && spin) ? 1u : 0u;
        compute_y(s);
        if (s == NSEG - 1) break;
        const int eon = ex_off(s + 1, 0);
        const int sto = st ? eon + eoff : ((w == 5 && lane == 0 && o_ % (56 / NREP) == 0) ? eon + 7168 + (o_ / (56 / NREP)) * 128 : OOB);
        BAR();
        publish_y(s);
        const v4u gf = *(const v4u*)(smem + L_SCH + 32);
        const unsigned gfl = gf.x | gf.y | gf.z | gf.w;
        const unsigned tref = sd.tE1 << 2;
        KEEP4(sto); KEEP4(gfl);
        DRAIN();
        if (w == 4) {
          BAR();
          STAMP(4);
          const f32x4 a = (*(const f32x4*)(pp) + *(const f32x4*)(pp + 16)) + (*(const f32x4*)(pp + 32) + *(const f32x4*)(pp + 48));
          const float v = (a.x + a.y) + (a.z + a.w);
          auto r = __builtin_amdgcn_permlane16_swap(__float_as_uint(v), __float_as_uint(v), false, false);
          const unsigned r0 = r[0], r1 = r[1];
          const unsigned ov = nosent(pk2(__uint_as_float(r0), __uint_as_float(r1)));
          st1_sc1(ov, rx, sto);
          st1_sc1(ov, rx, sto ^ 8192);
        } else {
          BAR();
          const v4u tw = *(const v4u*)(smem + L_SCH + 16);
          const unsigned tm = tref + (unsigned)max(max((int)(tw.x - tref), (int)(tw.y - tref)), max((int)(tw.z - tref), (int)(tw.w - tref)));
          const unsigned wd = nosent(tm | ((tw.x | tw.y | tw.z | tw.w) & 3u) | gfl);
          st1_sc1(wd, rx, sto);
          st1_sc1(wd, rx, sto ^ 8192);
        }
        __builtin_amdgcn_sched_barrier(0);
        STAMP(5);
        STAMPV(10, spin);
      }
      BAR();
      publish_y(NSEG - 1);
      DRAIN();
      BAR();
      if (w == 6 && lane == 0 && rg == 0) arrive(ws, rx, CTR_M + (NSEG - 1) * 128, 84u);
      FAIL_DUMP(w == 7 ? sd.m1 * 1000 + sd.m2 : sd.nf);
    }
  } else {
    startup();
    if (w < 4) {
      const int h = 2 * (cu - N_S - N_O) + (w >> 1), u = w & 1;
      v4u K[KS];
      int known = 0;
      kv_ensure(rx, 0, known, 4);
#pragma unroll
      for (int ks = 0; ks < KS; ++ks) K[ks] = ld_sc1(rx, (int)OFF_KP + ((h * 2 + u) * 14 + ks) * 1024 + lane * 16);
      unsigned char* xw = smem + (kg == 0 ? L_P1 + w * 128 + n * 8 : L_DUMMY);
      const unsigned char* xr = smem + L_P1 + (w ^ 1) * 128 + n * 8;
#pragma nounroll
      for (int s = 0; s < NSEG - 1; ++s) {
        const int eo1 = ex_off(s, 1), eo3 = ex_off(s, 3);
        const int eb = ((n >> 2) & 1) ? eo3 : eo1;
        const int so = n < 8 ? eb + (h * 4 + (n & 3)) * 64 + kg * 16 + u * 8 : OOB;
        const int lo = (lane < 8 && u == 0) ? eb + 4096 + (h * 4 + (n & 3)) * 4 : OOB;
        const unsigned char* lm = smem + L_MEM + (s & 1) * 7168 + lmoff;
#pragma unroll
        for (int ks = 0; ks < KS; ++ks) KEEP4(K[ks]);
        KEEP4(so); KEEP4(lo);
        BAR();
        STAMP(2);
        v4u mf[KS];
#pragma unroll
        for (int ks = 0; ks < PFD + 2; ++ks) mf[ks] = *(const v4u*)(lm + ks * 512);
        __builtin_amdgcn_sched_barrier(0);
        f32x4 a0 = {0, 0, 0, 0}, a1 = {0, 0, 0, 0};
#pragma unroll
        for (int ks = 0; ks < KS; ks += 2) {
          if (ks + PFD + 2 < KS) mf[ks + PFD + 2] = *(const v4u*)(lm + (ks + PFD + 2) * 512);
          if (ks + PFD + 3 < KS) mf[ks + PFD + 3] = *(const v4u*)(lm + (ks + PFD + 3) * 512);
          a0 = mfma32(K[ks], mf[ks], a0);
          a1 = mfma32(K[ks + 1], mf[ks + 1], a1);
          __builtin_amdgcn_sched_barrier(0);
        }
        a0 += a1;
        float xs[4] = {rbf(a0.x), rbf(a0.y), rbf(a0.z), rbf(a0.w)};
        const float mx = xmax32(xmax16(fmaxf(fmaxf(xs[0], xs[1]), fmaxf(xs[2], xs[3])))) * sc;
#pragma unroll
        for (int j = 0; j < 4; ++j) xs[j] = __builtin_amdgcn_exp2f(__builtin_fmaf(xs[j], sc, -mx));
        const float sum = xsum32(xsum16((xs[0] + xs[1]) + (xs[2] + xs[3])));
        *(f32x2*)xw = f32x2{mx, sum};
        BAR();
        const f32x2 ot = *(const f32x2*)xr;
        const float mm = fmaxf(mx, ot.x);
        const float fu = __builtin_amdgcn_exp2f(mx - mm), fo = __builtin_amdgcn_exp2f(ot.x - mm);
        const float pu = sum * fu, po = ot.y * fo;
        const float tot = u ? po + pu : pu + po;
        const float scl = fu * __builtin_amdgcn_rcpf(tot);
        const v2u o = nosent2(v2u{pk2(xs[0] * scl, xs[1] * scl), pk2(xs[2] * scl, xs[3] * scl)});
        st2_sc1(o, rx, so);
        const unsigned lse = nosent(__float_as_uint(__builtin_amdgcn_logf(tot) + mm));
        st1_sc1(lse, rx, lo);
        st2_sc1(o, rx, so ^ 8192);
        st1_sc1(lse, rx, lo ^ 8192);
        __builtin_amdgcn_sched_barrier(0);
        STAMP(5);
        const int sn = s + 1 < NSEG - 1 ? s + 1 : s;
        kv_ensure(rx, sn >> 1, known, 4);
#pragma unroll
        for (int ks = 0; ks < KS; ++ks) K[ks] = ld_sc1(rx, (int)OFF_KP + (((sn * 16 + h) * 2 + u) * 14 + ks) * 1024 + lane * 16);
      }
    } else {
      Sched sd = {0u, 0u, 0, 0, 0, 0, 1};
#pragma nounroll
      for (int s = 0; s < NSEG - 1; ++s) {
        const int o0 = ex_off(s, 0) + goff;
        unsigned char* d0 = smem + L_MEM + (s & 1) * 7168 + goff;
        unsigned char* d1 = w == 7 ? smem + L_DUMMY : d0 + 1024;
        const int ot = (s > 0 && lane < NREP) ? o0 - goff + 7168 + lane * 128 : OOB;
        unsigned word;
        if (s > 1) wait_until(sd.tE2);
        else if (s == 1) { int kn = 0; kv_ensure(rx, 0, kn, 8); sleep_units(T_Q2); }
        STAMP(0);
        const int spin = gather_poll(rx, d0, d1, o0, w == 7 ? OOB : o0 + 1024, ot, word);
        BAR();
        STAMP(1);
        BAR();
        STAMPV(10, spin);
        sched_step(sd, word, lane, s);
        sd.nf += (s > 1 && spin) ? 1 : 0;
      }
      FAIL_DUMP(sd.nf);
    }
  }
  STAMP_DUMP();
}
DI void phase_kv(const Params& p, unsigned char* lds, rsrc_t rw, int cg, int par) {
  unsigned char* ws = p.ws;
  const int tid = threadIdx.x, lane = tid & 63, w = __builtin_amdgcn_readfirstlane(tid >> 6);
  const int nt0 = cg * 16 + 2 * w;
  v4u st[7];
  {
    const unsigned char* src = ws + OFF_XN + (size_t)par * 57344 + tid * 16;
#pragma unroll
    for (int i = 0; i < 7; ++i) st[i] = *(const v4u*)(src + i * 8192);
  }
  v4u W[2][KS];
#pragma unroll
  for (int q = 0; q < 2; ++q)
#pragma unroll
    for (int ks = 0; ks < KS; ++ks) W[q][ks] = *(const v4u*)(ws + OFF_WC + ((size_t)(nt0 + q) * 14 + ks) * 1024 + lane * 16);
#pragma unroll
  for (int i = 0; i < 7; ++i) *(v4u*)(lds + i * 8192 + tid * 16) = st[i];
#pragma unroll
  for (int ks = 0; ks < KS; ++ks) { KEEP4(W[0][ks]); KEEP4(W[1][ks]); }
  BAR();
  const bool isK = cg < 28;
  const int kbase = (int)OFF_KP + (((nt0 / 28) * 2) * 14 + ((nt0 % 28) >> 1)) * 1024 + lane * 16;
  const int vbase = (int)OFF_VP + (nt0 - 448) * 1024 + lane * 16;
  int pend = -1;
  unsigned aold = 0;
#pragma nounroll
  for (int it = 0; it < 16; ++it) {
    const int hb = par + 2 * it;
    const unsigned char* cur = lds + (it & 1) * 57344 + lane * 16;
    {
      const int so = (int)OFF_XN + (it + 1 < 16 ? hb + 2 : hb) * 57344;
#pragma unroll
      for (int i = 0; i < 7; ++i) st[i] = __builtin_amdgcn_raw_buffer_load_b128(rw, tid * 16, so + i * 8192, 0);
    }
    __builtin_amdgcn_sched_barrier(0);
    f32x4 acc[4][2];
#pragma unroll
    for (int rt = 0; rt < 4; ++rt) { acc[rt][0] = f32x4{0, 0, 0, 0}; acc[rt][1] = f32x4{0, 0, 0, 0}; }
    if (isK) {
      v4u af[2][4];
#pragma unroll
      for (int rt = 0; rt < 4; ++rt) af[0][rt] = *(const v4u*)(cur + (rt * 14) * 1024);
#pragma unroll
      for (int ks = 0; ks < KS; ++ks) {
        if (ks + 1 < KS) {
#pragma unroll
          for (int rt = 0; rt < 4; ++rt) af[(ks + 1) & 1][rt] = *(const v4u*)(cur + (rt * 14 + ks + 1) * 1024);
        }
        __builtin_amdgcn_sched_barrier(0);
#pragma unroll
        for (int rt = 0; rt < 4; ++rt) {
          acc[rt][0] = mfma32(W[0][ks], af[ks & 1][rt], acc[rt][0]);
          acc[rt][1] = mfma32(W[1][ks], af[ks & 1][rt], acc[rt][1]);
        }
        __builtin_amdgcn_sched_barrier(0);
      }
    } else {
      v4u af[2][4];
#pragma unroll
      for (int rt = 0; rt < 4; ++rt) af[0][rt] = *(const v4u*)(cur + (rt * 14) * 1024);
#pragma unroll
      for (int ks = 0; ks < KS; ++ks) {
        if (ks + 1 < KS) {
#pragma unroll
          for (int rt = 0; rt < 4; ++rt) af[(ks + 1) & 1][rt] = *(const v4u*)(cur + (rt * 14 + ks + 1) * 1024);
        }
        __builtin_amdgcn_sched_barrier(0);
#pragma unroll
        for (int rt = 0; rt < 4; ++rt) {
          acc[rt][0] = mfma32(af[ks & 1][rt], W[0][ks], acc[rt][0]);
          acc[rt][1] = mfma32(af[ks & 1][rt], W[1][ks], acc[rt][1]);
        }
        __builtin_amdgcn_sched_barrier(0);
      }
    }
    __builtin_amdgcn_sched_barrier(0);
#pragma unroll
    for (int i = 0; i < 7; ++i) *(v4u*)(lds + ((it + 1) & 1) * 57344 + i * 8192 + tid * 16) = st[i];
    DRAIN();
    if (pend >= 0 && lane == 0) arrive_finish(rw, aold, pend, 56u);
    pend = -1;
    __builtin_amdgcn_sched_barrier(0);
    if (isK) {
#pragma unroll
      for (int rt = 0; rt < 4; ++rt) st4_sc1(pk8(acc[rt][0], acc[rt][1]), rw, kbase + ((hb * 2 + (rt >> 1)) * 32 + (rt & 1)) * 14336);
    } else {
#pragma unroll
      for (int q = 0; q < 2; ++q)
#pragma unroll
        for (int sg = 0; sg < 2; ++sg) st4_sc1(pk8(acc[2 * sg][q], acc[2 * sg + 1][q]), rw, vbase + ((hb * 2 + sg) * 448 + q) * 1024);
    }
    if (it == 0 || it == 15) DRAIN();
    BAR();
    if (w == 0) TL(200 + it);
    if (w == (it & 7)) {
      if (it == 0 || it == 15) { if (lane == 0) arrive(ws, rw, CTR_KV + hb * 128, 56u); }
      if (it >= 2) {
        if (it == 15) { if (lane == 0) arrive(ws, rw, CTR_KV + (hb - 2) * 128, 56u); }
        else { pend = CTR_KV + (hb - 2) * 128; if (lane == 0) aold = arrive_issue(ws, pend); }
      }
    }
  }
}

DI void gate_wait(rsrc_t rw, int rt, int nap, int lag, int own) {
  int off = (int)OFF_PM + rt * 4096 + ((blockIdx.x >> 3) & 31) * 128;
  if (__builtin_amdgcn_readfirstlane(ld1_sc1(rw, off)) != SENT) {
    off = own; lag = 4; nap = 8;
    if (__builtin_amdgcn_readfirstlane(ld1_sc1(rw, off)) != SENT) return;
  }
#pragma nounroll
  for (int spin = 0; spin < SPIN_MAX; ++spin) {
    sleep_units(nap);
    asm volatile("" ::: "memory");
    if (__builtin_amdgcn_readfirstlane(ld1_sc1(rw, off)) != SENT) break;
  }
  sleep_units(lag);
}
#define LAG_A2 10
#define LAG_F1 65
#define LAG_F2 140
#define RETRY 6
#define NAPT 4
#define NAP 6

#define TLX(k) do { if (rt == 127 || rt == 96) TL((rt == 127 ? 130 : 140) + (k)); } while (0)
DI void stage_a1(const Params& p, rsrc_t rw, int h) {
  unsigned char* ws = p.ws;
  const int tid = threadIdx.x, lane = tid & 63, w = __builtin_amdgcn_readfirstlane(tid >> 6);
  const int kg = lane >> 4, n = lane & 15, v = w & 1;
  const float sc = 0.0625f * 1.4426950408889634f;
  const float NEG = -3.0e38f;
  int known = 0;
  ctr_wait(rw, CTR_FX, 1u, NAP);
#pragma nounroll
  for (int rt = w; rt < 128; rt += 8) {
    const int s = rt >> 1;
    v4u xf[KS], kf[KS], k1[KS];
    kv_ensure(rw, s >> 1, known, NAP);
    const int kb = (int)OFF_KP + ((s * 16 + h) * 2) * 14336 + lane * 16;
#pragma unroll
    for (int ks = 0; ks < KS; ++ks) {
      xf[ks] = *(const v4u*)(ws + OFF_XN + ((size_t)rt * 14 + ks) * 1024 + lane * 16);
      kf[ks] = ld_sc1(rw, kb + ks * 1024);
    }
#pragma unroll
    for (int ks = 0; ks < KS; ++ks) k1[ks] = ld_sc1(rw, v ? kb + 14336 + ks * 1024 : OOB);
    __builtin_amdgcn_sched_barrier(0);
    f32x4 a0 = {0, 0, 0, 0}, a1 = {0, 0, 0, 0}, am = {0, 0, 0, 0};
#pragma unroll
    for (int ks = 0; ks < KS; ++ks) a0 = mfma32(kf[ks], xf[ks], a0);
#pragma unroll
    for (int ks = 0; ks < KS; ++ks) a1 = mfma32(k1[ks], xf[ks], a1);
    float xs[8];
#pragma unroll
    for (int r = 0; r < 4; ++r) {
      const bool diag = 4 * kg + r <= n;
      xs[r] = (v || diag) ? rbf(a0[r]) : NEG;
      xs[4 + r] = (v && diag) ? rbf(a1[r]) : NEG;
    }
    float mx = NEG;
#pragma unroll
    for (int j = 0; j < 8; ++j) mx = fmaxf(mx, xs[j]);
    const int tb = (int)OFF_KM + (s * 16 + h) * 7168 + kg * 128 + (n & 7) * 16;
    TLX(3);
    ctr_wait(rw, CTR_M + (s < NSEG - 2 ? s : s - 2) * 128, 84u, NAP);
    TLX(0);
#pragma nounroll
    for (int spin = 0; spin < SPIN_MAX; ++spin) {
      asm volatile("" ::: "memory");
      TLX(1);
      unsigned mxu = 0;
#pragma unroll
      for (int ks = 0; ks < KS; ++ks) kf[ks] = ld_sc1(rw, tb + ks * 512);
      if (s < NSEG - 2) break;
#pragma unroll
      for (int ks = 0; ks < KS; ++ks) mxu = max(mxu, umax4(kf[ks]));
      if (__all(mxu != SENT)) break;
      sleep_units(3);
    }
#pragma unroll
    for (int ks = 0; ks < KS; ++ks) am = mfma32(kf[ks], xf[ks], am);
    float mm[4];
#pragma unroll
    for (int r = 0; r < 4; ++r) { mm[r] = rbf(am[r]); mx = fmaxf(mx, mm[r]); }
    mx = xmax32(xmax16(mx));
    const float nb = -mx * sc;
    float sum = 0.f;
#pragma unroll
    for (int j = 0; j < 8; ++j) { xs[j] = xs[j] == NEG ? 0.f : __builtin_amdgcn_exp2f(__builtin_fmaf(xs[j], sc, nb)); sum += xs[j]; }
#pragma unroll
    for (int r = 0; r < 4; ++r) { mm[r] = __builtin_amdgcn_exp2f(__builtin_fmaf(mm[r], sc, nb)); if (kg < 2) sum += mm[r]; }
    sum = xsum32(xsum16(sum));
    const float inv = 1.f / sum;
    st4_sc1(nosent4(v4u{pk2(xs[0] * inv, xs[1] * inv), pk2(xs[2] * inv, xs[3] * inv), pk2(xs[4] * inv, xs[5] * inv), pk2(xs[6] * inv, xs[7] * inv)}),
            rw, (int)OFF_PX + (rt * 16 + h) * 1024 + lane * 16);
    st2_sc1(nosent2(v2u{pk2(mm[0] * inv, mm[1] * inv), pk2(mm[2] * inv, mm[3] * inv)}), rw,
            kg < 2 ? (int)OFF_PM + (rt * 4 + (h >> 2)) * 1024 + ((h & 3) * 16 + n) * 16 + kg * 8 : OOB);
    TL(rt);
  }
}

DI void stage_a2(const Params& p, rsrc_t rw, int ct) {
  const int tid = threadIdx.x, lane = tid & 63, w = __builtin_amdgcn_readfirstlane(tid >> 6);
  const int kg = lane >> 4, n = lane & 15;
  const int c = 2 * ct + (kg >> 1);
  const int col = 32 * (c >> 2) + 16 * (kg & 1) + 4 * (c & 3);
  const int hoff = (c >> 2) * 1024 + ((c & 3) * 16 + n) * 16 + (kg & 1) * 8;
  int known = 0;
  ctr_wait(rw, CTR_FX, 1u, NAP);
#pragma nounroll
  for (int rt = w; rt < 128; rt += 8) {
    const int s = rt >> 1;
    const v2u xv = *(const v2u*)(p.x + (size_t)(rt * 16 + n) * 448 + col);
    kv_ensure(rw, s >> 1, known, NAP);
    v4u vf[16], yf[4];
#pragma unroll
    for (int h = 0; h < 16; ++h) vf[h] = ld_sc1(rw, (int)OFF_VP + (s * 448 + h * 28 + ct) * 1024 + lane * 16);
    TLX(3);
    ctr_wait(rw, CTR_M + (s < NSEG - 2 ? s : s - 2) * 128, 84u, NAP);
    const int pb = (int)OFF_PX + rt * 16384 + lane * 16, mb = (int)OFF_PM + rt * 4096 + lane * 16;
    gate_wait(rw, rt, NAPT, LAG_A2, (int)OFF_PX + (rt * 16 + (ct & 15)) * 1024);
    TLX(0);
    f32x4 acc;
#pragma nounroll
    for (int spin = 0; spin < SPIN_MAX; ++spin) {
      asm volatile("" ::: "memory");
      TLX(1);
      v4u px[16], pm[4];
#pragma unroll
      for (int q = 0; q < 4; ++q) yf[q] = ld_sc1(rw, (int)OFF_VM + ((s * 16 + 4 * q + kg) * 28 + ct) * 256 + n * 16);
#pragma unroll
      for (int h = 0; h < 16; ++h) px[h] = ld_sc1(rw, pb + h * 1024);
#pragma unroll
      for (int q = 0; q < 4; ++q) pm[q] = ld_sc1(rw, mb + q * 1024);
      unsigned mxu = 0;
      f32x4 a0 = {0, 0, 0, 0}, a1 = {0, 0, 0, 0};
#pragma unroll
      for (int h = 0; h < 16; h += 2) {
        mxu = umax3(mxu, umax4(px[h]), umax4(px[h + 1]));
        a0 = mfma32(vf[h], px[h], a0); a1 = mfma32(vf[h + 1], px[h + 1], a1);
      }
#pragma unroll
      for (int q = 0; q < 4; q += 2) {
        mxu = umax3(mxu, umax3(umax4(pm[q]), umax4(pm[q + 1]), umax4(yf[q])), umax4(yf[q + 1]));
        a0 = mfma32(yf[q], pm[q], a0); a1 = mfma32(yf[q + 1], pm[q + 1], a1);
      }
      acc = a0 + a1;
      if (__all(mxu != SENT)) break;
      sleep_units(RETRY);
    }
    const float h0 = bflo(xv.x) + rbf(acc.x), h1 = bfhi(xv.x) + rbf(acc.y), h2 = bflo(xv.y) + rbf(acc.z), h3 = bfhi(xv.y) + rbf(acc.w);
    st2_sc1(nosent2(v2u{pk2(h0, h1), pk2(h2, h3)}), rw, (int)OFF_HF + rt * 14336 + hoff);
    TL(rt);
  }
}

DI void stage_f1(const Params& p, unsigned char* lds, rsrc_t rw, int kf) {
  const int tid = threadIdx.x, lane = tid & 63, w = __builtin_amdgcn_readfirstlane(tid >> 6);
  const int kg = lane >> 4, n = lane & 15;
  for (int f = w; f < 56; f += 8) {
    const int t = f / 14, ks = f % 14;
    const u16* src = ((t >> 1) ? p.wu : p.wg) + 32 * kf + 16 * (t & 1) + n;
    unsigned e[8];
#pragma unroll
    for (int jj = 0; jj < 8; ++jj) e[jj] = src[(size_t)pidx(ks, kg, jj) * 1792];
    *(v4u*)(lds + f * 1024 + lane * 16) = v4u{e[0] | (e[1] << 16), e[2] | (e[3] << 16), e[4] | (e[5] << 16), e[6] | (e[7] << 16)};
  }
  BAR();
  const v4u ones = {0x3F803F80u, 0x3F803F80u, 0x3F803F80u, 0x3F803F80u};
  f32x4 cs[4];
#pragma unroll
  for (int t = 0; t < 4; ++t) {
    cs[t] = f32x4{0, 0, 0, 0};
#pragma unroll
    for (int ks = 0; ks < KS; ++ks) cs[t] = mfma32(*(const v4u*)(lds + (t * 14 + ks) * 1024 + lane * 16), ones, cs[t]);
  }
#pragma nounroll
  for (int rt = w; rt < 128; rt += 8) {
    const int hb = (int)OFF_HF + rt * 14336 + lane * 16;
    gate_wait(rw, rt, NAPT, LAG_F1, (int)OFF_HF + rt * 14336 + (kf >> 1) * 512);
    TLX(0);
    v4u hf[KS];
#pragma nounroll
    for (int spin = 0; spin < SPIN_MAX; ++spin) {
      asm volatile("" ::: "memory");
      TLX(1);
      unsigned mxu = 0;
#pragma unroll
      for (int ks = 0; ks < KS; ++ks) hf[ks] = ld_sc1(rw, hb + ks * 1024);
#pragma unroll
      for (int ks = 0; ks < KS; ++ks) mxu = max(mxu, umax4(hf[ks]));
      if (__all(mxu != SENT)) break;
      sleep_units(RETRY);
    }
    TLX(2);
    f32x4 acc[4], sm = {0, 0, 0, 0}, gr = {0, 0, 0, 0};
#pragma unroll
    for (int t = 0; t < 4; ++t) acc[t] = f32x4{0, 0, 0, 0};
    v4u wf[2][4];
#pragma unroll
    for (int t = 0; t < 4; ++t) wf[0][t] = *(const v4u*)(lds + (t * 14) * 1024 + lane * 16);
#pragma unroll
    for (int ks = 0; ks < KS; ++ks) {
      if (ks + 1 < KS) {
#pragma unroll
        for (int t = 0; t < 4; ++t) wf[(ks + 1) & 1][t] = *(const v4u*)(lds + (t * 14 + ks + 1) * 1024 + lane * 16);
      }
      sm = mfma32(ones, hf[ks], sm);
      gr = mfma32(hf[ks], hf[ks], gr);
      __builtin_amdgcn_sched_barrier(0);
#pragma unroll
      for (int t = 0; t < 4; ++t) acc[t] = mfma32(wf[ks & 1][t], hf[ks], acc[t]);
      __builtin_amdgcn_sched_barrier(0);
    }
    float dg = (kg == (n >> 2)) ? ((n & 2) ? ((n & 1) ? gr.w : gr.z) : ((n & 1) ? gr.y : gr.x)) : 0.f;
    dg = xsum32(xsum16(dg));
    const float mean = sm.x * (1.f / 448.f);
    const float var = fmaxf(dg * (1.f / 448.f) - mean * mean, 0.f);
    const float mb = rbf(mean), rden = 1.f / ln_den(var);
    f32x4 o0, o1;
#pragma unroll
    for (int r = 0; r < 4; ++r) {
      o0[r] = rbf((acc[0][r] - mb * cs[0][r]) * rden) * rbf((acc[2][r] - mb * cs[2][r]) * rden);
      o1[r] = rbf((acc[1][r] - mb * cs[1][r]) * rden) * rbf((acc[3][r] - mb * cs[3][r]) * rden);
    }
    st4_sc1(nosent4(pk8(o0, o1)), rw, (int)OFF_AF + (rt * 56 + kf) * 1024 + lane * 16);
    TL(rt);
  }
}

DI void stage_f2(const Params& p, unsigned char* lds, rsrc_t rw, int ct) {
  const int tid = threadIdx.x, lane = tid & 63, w = __builtin_amdgcn_readfirstlane(tid >> 6);
  const int kg = lane >> 4, n = lane & 15;
  for (int kf = w; kf < 56; kf += 8) {
    const u16* src = p.wd + 16 * ct + n;
    unsigned e[8];
#pragma unroll
    for (int jj = 0; jj < 8; ++jj) e[jj] = src[(size_t)pidx(kf, kg, jj) * 448];
    *(v4u*)(lds + kf * 1024 + lane * 16) = v4u{e[0] | (e[1] << 16), e[2] | (e[3] << 16), e[4] | (e[5] << 16), e[6] | (e[7] << 16)};
  }
  BAR();
#pragma nounroll
  for (int rt = w; rt < 128; rt += 8) {
    const int ab = (int)OFF_AF + rt * 57344 + lane * 16;
    gate_wait(rw, rt, NAPT, LAG_F2, (int)OFF_AF + rt * 57344 + (2 * ct) * 1024);
    TLX(0);
    v2u hv = v2u{0, 0};
    f32x4 a0, a1;
#pragma nounroll
    for (int spin = 0; spin < SPIN_MAX; ++spin) {
      asm volatile("" ::: "memory");
      TLX(1);
      a0 = f32x4{0, 0, 0, 0}; a1 = f32x4{0, 0, 0, 0};
      hv = __builtin_amdgcn_raw_buffer_load_b64(rw, (int)OFF_HF + rt * 14336 + (ct >> 1) * 1024 + lane * 16 + (ct & 1) * 8, 0, AUX_SC1);
      v4u af[3][14];
#pragma unroll
      for (int bb = 0; bb < 2; ++bb)
#pragma unroll
        for (int k = 0; k < 14; ++k) af[bb][k] = ld_sc1(rw, ab + (bb * 14 + k) * 1024);
      unsigned mxu = max(hv.x, hv.y);
#pragma unroll
      for (int bb = 0; bb < 4; ++bb) {
        if (bb + 2 < 4) {
#pragma unroll
          for (int k = 0; k < 14; ++k) af[(bb + 2) % 3][k] = ld_sc1(rw, ab + ((bb + 2) * 14 + k) * 1024);
        }
        __builtin_amdgcn_sched_barrier(0);
#pragma unroll
        for (int k = 0; k < 14; k += 2) {
          mxu = umax3(mxu, umax4(af[bb % 3][k]), umax4(af[bb % 3][k + 1]));
          a0 = mfma32(*(const v4u*)(lds + (bb * 14 + k) * 1024 + lane * 16), af[bb % 3][k], a0);
          a1 = mfma32(*(const v4u*)(lds + (bb * 14 + k + 1) * 1024 + lane * 16), af[bb % 3][k + 1], a1);
        }
        __builtin_amdgcn_sched_barrier(0);
      }
      if (__all(mxu != SENT)) break;
      sleep_units(RETRY);
    }
    const f32x4 d = a0 + a1;
    const float o0 = bflo(hv.x) + rbf(d.x), o1 = bfhi(hv.x) + rbf(d.y), o2 = bflo(hv.y) + rbf(d.z), o3 = bfhi(hv.y) + rbf(d.w);
    *(v2u*)(p.out + (size_t)(rt * 16 + n) * 448 + 16 * ct + 4 * kg) = v2u{pk2(o0, o1), pk2(o2, o3)};
    TL(rt);
  }
}

DI void arm_stage(const Params& p, rsrc_t rw, int k, int nwg, int off0, int nkb, int ctr) {
  const int tid = threadIdx.x, lane = tid & 63, w = __builtin_amdgcn_readfirstlane(tid >> 6);
  const v4u sent = {SENT, SENT, SENT, SENT};
  for (int i = k * 8 + w; i < nkb; i += nwg * 8) st4_sc1(sent, rw, off0 + i * 1024 + lane * 16);
  DRAIN();
  __syncthreads();
  if (tid == 0) arrive(p.ws, rw, ctr, (unsigned)nwg);
}

constexpr int MAIN_LDS = 114688;
__global__ void __launch_bounds__(512, 2) k_main(Params p) {
  extern __shared__ __attribute__((aligned(16))) unsigned char smem[];
  const rsrc_t rw = __builtin_amdgcn_make_buffer_rsrc((void*)p.ws, 0, (int)WS_END, 0x00020000);
  const int b = blockIdx.x;
  if (threadIdx.x < 64) TL(255);
  const int j = (b >> 3) * 4 + (b & 3);
  if ((b & 7) < 4) {
    if (j < N_CHAIN) chain_role(p, smem, rw, j);
    else { if (j == N_CHAIN) probe_place(p, rw); sleep_units(400); arm_stage(p, rw, j - N_CHAIN, 8, (int)OFF_HF, (int)((OFF_XN - OFF_HF) / 1024), CTR_FS + 128); }
    return;
  }
  if (j < 112) phase_kv(p, smem, rw, j % 56, j / 56);
  else { sleep_units(D_ARM); }
  if (j >= 112) arm_stage(p, rw, j - 112, 16, (int)OFF_PX, (int)((OFF_HF - OFF_PX) / 1024), CTR_FS);
  ctr_wait(rw, CTR_FS, 16u, 8);
  if (j < 112) ctr_wait(rw, CTR_FS + 128, 8u, 8);
  if (j < 56) stage_f1(p, smem, rw, j);
  else if (j < 84) stage_f2(p, smem, rw, j - 56);
  else if (j < 112) stage_a2(p, rw, j - 84);
  else stage_a1(p, rw, j - 112);
}

extern "C" void kernel_launch(void* const* d_in, const int* in_sizes, int n_in, void* d_out, int out_size, void* d_ws, size_t ws_size,
                              hipStream_t stream) {
  Params p;
  p.x = (const u16*)d_in[0]; p.mem0 = (const u16*)d_in[1]; p.Wq = (const u16*)d_in[2]; p.Wk = (const u16*)d_in[3];
  p.Wv = (const u16*)d_in[4]; p.Wo = (const u16*)d_in[5];
  p.wg = (const u16*)d_in[8]; p.wu = (const u16*)d_in[9]; p.wd = (const u16*)d_in[10];
  p.out = (u16*)d_out; p.ws = (unsigned char*)d_ws;
  (void)hipFuncSetAttribute((const void*)k_main, hipFuncAttributeMaxDynamicSharedMemorySize, MAIN_LDS);
  (void)hipFuncSetAttribute((const void*)k_prep, hipFuncAttributeMaxDynamicSharedMemorySize, PREP_LDS);
  k_prep<<<256, 512, PREP_LDS, stream>>>(p);
  k_main<<<256, 512, MAIN_LDS, stream>>>(p);
}
```

```cpp
#include <hip/hip_runtime.h>
#include <hip/hip_bf16.h>
#include <stdint.h>

typedef short bf16x8 __attribute__((ext_vector_type(8)));
typedef float f32x4 __attribute__((ext_vector_type(4)));
typedef float f32x2 __attribute__((ext_vector_type(2)));
typedef __bf16 bf2_t __attribute__((ext_vector_type(2)));
typedef unsigned v4u __attribute__((ext_vector_type(4)));
typedef unsigned v2u __attribute__((ext_vector_type(2)));
typedef unsigned short u16;
typedef __amdgpu_buffer_rsrc_t rsrc_t;
#define DI __device__ __forceinline__

constexpr int NSEG = 64, KS = 14;
constexpr unsigned SENT = 0xFFFFFFFFu;
constexpr int AUX_SC1 = 16;
constexpr int SPIN_MAX = 20000;
constexpr int OOB = 0x7FFFFFF0;

constexpr size_t OFF_CTR = 0;
constexpr int CTR_KV = 0, CTR_M = 4096, OFF_PTAB = 16384, CTR_FX = 16640, CTR_FS = 16896;
constexpr size_t OFF_EX = 65536;
constexpr size_t EX_STEP = 5 * 16384;
constexpr size_t OFF_PX = OFF_EX + 64 * EX_STEP;
constexpr size_t OFF_PM = OFF_PX + (size_t)128 * 16 * 1024;
constexpr size_t OFF_HF = OFF_PM + (size_t)128 * 4 * 1024;
constexpr size_t OFF_AF = OFF_HF + (size_t)128 * 14 * 1024;
constexpr size_t OFF_XN = OFF_AF + (size_t)128 * 56 * 1024;
constexpr size_t OFF_WC = OFF_XN + (size_t)128 * 14 * 1024;
constexpr size_t OFF_KP = OFF_WC + (size_t)896 * 14 * 1024;
constexpr size_t OFF_VP = OFF_KP + (size_t)64 * 16 * 2 * 14 * 1024;
constexpr size_t OFF_KM = OFF_VP + (size_t)64 * 448 * 1024;
constexpr size_t OFF_VM = OFF_KM + (size_t)64 * 16 * 14 * 512;
constexpr size_t WS_END = OFF_VM + (size_t)64 * 16 * 28 * 256;
constexpr size_t FILL_BYTES = OFF_XN - OFF_EX;
static_assert(WS_END < (size_t)200 * 1024 * 1024, "ws");
static_assert(FILL_BYTES % 16384 == 0, "fill");

DI f32x4 mfma32(v4u a, v4u b, f32x4 c) {
  return __builtin_amdgcn_mfma_f32_16x16x32_bf16(__builtin_bit_cast(bf16x8, a), __builtin_bit_cast(bf16x8, b), c, 0, 0, 0);
}
DI unsigned pk2(float a, float b) {
  f32x2 v = {a, b};
  return __builtin_bit_cast(unsigned, __builtin_convertvector(v, bf2_t));
}
DI v2u pk4(f32x4 a) { return v2u{pk2(a.x, a.y), pk2(a.z, a.w)}; }
DI v4u pk8(f32x4 a, f32x4 b) { return v4u{pk2(a.x, a.y), pk2(a.z, a.w), pk2(b.x, b.y), pk2(b.z, b.w)}; }
DI float bflo(unsigned u) { return __uint_as_float(u << 16); }
DI float bfhi(unsigned u) { return __uint_as_float(u & 0xFFFF0000u); }
DI float rbf(float a) { return bflo(pk2(a, 0.f) & 0xFFFFu); }
DI int picol(int c, int cc) { return 32 * (c >> 2) + 16 * (cc >> 2) + 4 * (c & 3) + (cc & 3); }
DI int pidx(int ks, int kg, int jj) { return 32 * ks + 16 * (jj >> 2) + 4 * kg + (jj & 3); }
DI float xsum16(float v) { auto r = __builtin_amdgcn_permlane16_swap(__float_as_uint(v), __float_as_uint(v), false, false);
  const unsigned a = r[0], b = r[1]; return __uint_as_float(a) + __uint_as_float(b); }
DI float xsum32(float v) { auto r = __builtin_amdgcn_permlane32_swap(__float_as_uint(v), __float_as_uint(v), false, false);
  const unsigned a = r[0], b = r[1]; return __uint_as_float(a) + __uint_as_float(b); }
DI float xmax16(float v) { auto r = __builtin_amdgcn_permlane16_swap(__float_as_uint(v), __float_as_uint(v), false, false);
  const unsigned a = r[0], b = r[1]; return fmaxf(__uint_as_float(a), __uint_as_float(b)); }
DI float xmax32(float v) { auto r = __builtin_amdgcn_permlane32_swap(__float_as_uint(v), __float_as_uint(v), false, false);
  const unsigned a = r[0], b = r[1]; return fmaxf(__uint_as_float(a), __uint_as_float(b)); }
DI unsigned nosent(unsigned v) { return min(v, 0xFFFFFFFEu); }
DI v2u nosent2(v2u o) { return v2u{nosent(o.x), nosent(o.y)}; }
DI v4u nosent4(v4u o) { return v4u{nosent(o.x), nosent(o.y), nosent(o.z), nosent(o.w)}; }
DI unsigned umax3(unsigned a, unsigned b, unsigned c) { return max(max(a, b), c); }
DI unsigned umax4(v4u r) { return max(umax3(r.x, r.y, r.z), r.w); }
#define KEEP4(x) asm volatile("" :: "v"(x))
#define BAR() do { asm volatile("s_waitcnt lgkmcnt(0)" ::: "memory"); __builtin_amdgcn_s_barrier(); asm volatile("" ::: "memory"); } while (0)
#define DRAIN() asm volatile("s_waitcnt vmcnt(0)" ::: "memory")
DI void sleep_units(int n) { for (int d = 0; d < n; ++d) __builtin_amdgcn_s_sleep(1); }
DI v4u ld_sc1(rsrc_t r, int off) { return __builtin_amdgcn_raw_buffer_load_b128(r, off, 0, AUX_SC1); }
DI unsigned ld1_sc1(rsrc_t r, int off) { return __builtin_amdgcn_raw_buffer_load_b32(r, off, 0, AUX_SC1); }
DI void st4_sc1(v4u v, rsrc_t r, int off) { __builtin_amdgcn_raw_buffer_store_b128(v, r, off, 0, AUX_SC1); }
DI void st2_sc1(v2u v, rsrc_t r, int off) { __builtin_amdgcn_raw_buffer_store_b64(v, r, off, 0, AUX_SC1); }
DI void st1_sc1(unsigned v, rsrc_t r, int off) { __builtin_amdgcn_raw_buffer_store_b32(v, r, off, 0, AUX_SC1); }
constexpr int FLAG_D = 32768;
DI unsigned arrive_issue(unsigned char* ws, int off) { return __hip_atomic_fetch_add((unsigned*)(ws + off), 1u, __ATOMIC_RELAXED, __HIP_MEMORY_SCOPE_AGENT); }
DI void arrive_finish(rsrc_t r, unsigned old, int off, unsigned total) { if (old == total - 1u) st1_sc1(1u, r, off + FLAG_D); }
DI void arrive(unsigned char* ws, rsrc_t r, int off, unsigned total) { arrive_finish(r, arrive_issue(ws, off), off, total); }
DI void ctr_wait(rsrc_t r, int off, unsigned want, int nap) {
  off += FLAG_D; want = 1u;
#pragma nounroll
  for (int spin = 0; spin < SPIN_MAX; ++spin) {
    asm volatile("" ::: "memory");
    const unsigned v = __builtin_amdgcn_readfirstlane(ld1_sc1(r, off));
    if (v >= want) break;
    sleep_units(nap);
  }
  asm volatile("" ::: "memory");
}

DI void kv_ensure(rsrc_t r, int hb, int& known, int nap) {
  if (hb < known) return;
  const int lane = threadIdx.x & 63;
#pragma nounroll
  for (int spin = 0; spin < SPIN_MAX; ++spin) {
    asm volatile("" ::: "memory");
    const unsigned v = ld1_sc1(r, lane < 32 ? CTR_KV + FLAG_D + lane * 128 : OOB);
    const unsigned long long m = __ballot(v != 0u);
    known = __builtin_ctzll(~m);
    if (hb < known) break;
    sleep_units(nap);
  }
  asm volatile("" ::: "memory");
}

struct Params {
  const u16 *x, *mem0, *Wq, *Wk, *Wv, *Wo, *wg, *wu, *wd;
  u16* out;
  unsigned char* ws;
};

DI float ln_den(float var) { return rbf(sqrtf(rbf(rbf(var) + 1e-5f))); }

#define TL(idx)
constexpr int PREP_LDS = 288 * 528;
constexpr int FILL_KB = (int)(FILL_BYTES / 1024);
__global__ void __launch_bounds__(512, 2) k_prep(Params p) {
  extern __shared__ __attribute__((aligned(16))) unsigned char lds[];
  const int tid = threadIdx.x, lane = tid & 63, kg = lane >> 4, n = lane & 15;
  const int w = __builtin_amdgcn_readfirstlane(tid >> 6);
  unsigned char* ws = p.ws;
#define TLP(idx)
  const int xg = blockIdx.x & 7, ix = blockIdx.x >> 3;
  const int b = ix < 28 ? (2 * xg + ix / 14) * 14 + ix % 14 : 224 + xg * 4 + (ix - 28), bb = b;
  TLP(0);
  const v4u sent = {SENT, SENT, SENT, SENT};
  if (bb < 224) {
    const int h = bb / 14, r = bb % 14, ab = r / 7, jb = r % 7;
    v4u sa[2][14], sb[2][4];
#pragma unroll
    for (int half = 0; half < 2; ++half) {
      const u16* asrc = (half ? p.Wk : p.Wv) + (size_t)(224 * ab) * 4096 + h * 256;
      if (half) {
        const u16* bsrc = p.Wq + (size_t)(64 * jb) * 4096 + h * 256;
#pragma unroll
        for (int i = 0; i < 4; ++i) { const int pc = i * 512 + tid; sb[1][i] = *(const v4u*)(bsrc + (size_t)(pc >> 5) * 4096 + (pc & 31) * 8); }
      } else {
        const u16* bsrc = p.Wo + (size_t)(h * 256) * 448 + 64 * jb;
#pragma unroll
        for (int i = 0; i < 4; ++i) { const int pc = i * 512 + tid; sb[0][i] = *(const v4u*)(bsrc + (size_t)(pc >> 3) * 448 + (pc & 7) * 8); }
      }
#pragma unroll
      for (int i = 0; i < 14; ++i) { const int pc = i * 512 + tid; sa[half][i] = *(const v4u*)(asrc + (size_t)(pc >> 5) * 4096 + (pc & 31) * 8); }
    }
    __builtin_amdgcn_sched_barrier(0);
    unsigned char* lb = lds + 224 * 528;
#pragma unroll
    for (int half = 0; half < 2; ++half) {
      if (half) {
        __syncthreads();
#pragma unroll
        for (int i = 0; i < 4; ++i) { const int pc = i * 512 + tid; *(v4u*)(lb + (pc >> 5) * 528 + (pc & 31) * 16) = sb[1][i]; }
      } else {
#pragma unroll
        for (int i = 0; i < 4; ++i) {
          const int pc = i * 512 + tid, e = pc >> 3, x0 = (pc & 7) * 8;
#pragma unroll
          for (int j = 0; j < 8; ++j) {
            const int x = x0 + j, m = x >> 5, xl = x & 31, cl = (xl >> 2) & 3;
            const int slot = (2 * m + (cl >> 1)) * 16 + (cl & 1) * 8 + (xl >> 4) * 4 + (xl & 3);
            const unsigned wv = sb[0][i][j >> 1];
            *(u16*)(lb + slot * 528 + e * 2) = (u16)((j & 1) ? (wv >> 16) : (wv & 0xFFFFu));
          }
        }
      }
#pragma unroll
      for (int i = 0; i < 14; ++i) { const int pc = i * 512 + tid; *(v4u*)(lds + (pc >> 5) * 528 + (pc & 31) * 16) = sa[half][i]; }
      __syncthreads();
      TLP(1 + 2 * half);
      if (w < 7) {
        const int ks = ab * 7 + w;
        const unsigned char* pa = lds + (32 * w + n) * 528 + kg * 16;
        const unsigned char* pb = lb + n * 528 + kg * 16;
        f32x4 acc[4][2];
#pragma unroll
        for (int t = 0; t < 4; ++t) { acc[t][0] = f32x4{0, 0, 0, 0}; acc[t][1] = f32x4{0, 0, 0, 0}; }
#pragma unroll
        for (int ke = 0; ke < 8; ++ke) {
          const v4u A0 = *(const v4u*)(pa + ke * 64), A1 = *(const v4u*)(pa + 16 * 528 + ke * 64);
#pragma unroll
          for (int t = 0; t < 4; ++t) {
            const v4u B = *(const v4u*)(pb + t * 16 * 528 + ke * 64);
            acc[t][0] = mfma32(A0, B, acc[t][0]); acc[t][1] = mfma32(A1, B, acc[t][1]);
          }
        }
#pragma unroll
        for (int t = 0; t < 4; ++t) {
          const int tile = (half ? 0 : 448) + h * 28 + jb * 4 + t;
          *(v4u*)(ws + OFF_WC + ((size_t)tile * 14 + ks) * 1024 + lane * 16) = pk8(acc[t][0], acc[t][1]);
        }
      }
      TLP(2 + 2 * half);
    }
    return;
  }
  {
    const int c = lane < 56 ? lane : 0;
    const int ks = c >> 2, kc = c & 3;
    v2u a4[8], b4[8];
#pragma unroll
    for (int rr = 0; rr < 8; ++rr) {
      const int row = (bb - 224) * 64 + w * 8 + rr;
      a4[rr] = *(const v2u*)(p.x + (size_t)row * 448 + 32 * ks + 4 * kc);
      b4[rr] = *(const v2u*)(p.x + (size_t)row * 448 + 32 * ks + 16 + 4 * kc);
    }
    v2u ma = v2u{0, 0}, mb2 = v2u{0, 0};
    const int pc = w * 64 + lane;
    if (b == 224 && w < 7) {
      const int cc = (pc >> 4) * 2 + ((pc >> 2) & 1), row = ((pc >> 3) & 1) * 4 + (pc & 3);
      ma = *(const v2u*)(p.mem0 + (size_t)row * 448 + 32 * (cc >> 2) + 4 * (cc & 3));
      mb2 = *(const v2u*)(p.mem0 + (size_t)row * 448 + 32 * (cc >> 2) + 16 + 4 * (cc & 3));
    }
    if (b == 225) {
#pragma unroll
      for (int i = 0; i < 8; ++i) *(v4u*)(ws + OFF_CTR + (size_t)(i * 512 + tid) * 16) = v4u{0, 0, 0, 0};
    }
    __builtin_amdgcn_sched_barrier(0);
    float v[8][8], s1[8], q1[8];
#pragma unroll
    for (int rr = 0; rr < 8; ++rr) {
      const v2u a = a4[rr], b2 = b4[rr];
      v[rr][0] = bflo(a.x); v[rr][1] = bfhi(a.x); v[rr][2] = bflo(a.y); v[rr][3] = bfhi(a.y);
      v[rr][4] = bflo(b2.x); v[rr][5] = bfhi(b2.x); v[rr][6] = bflo(b2.y); v[rr][7] = bfhi(b2.y);
      float s = 0.f;
#pragma unroll
      for (int j = 0; j < 8; ++j) s += v[rr][j];
      s1[rr] = lane < 56 ? s : 0.f;
    }
#pragma unroll
    for (int o = 1; o < 64; o <<= 1)
#pragma unroll
      for (int rr = 0; rr < 8; ++rr) s1[rr] += __shfl_xor(s1[rr], o);
#pragma unroll
    for (int rr = 0; rr < 8; ++rr) {
      s1[rr] *= (1.f / 448.f);
      float q = 0.f;
#pragma unroll
      for (int j = 0; j < 8; ++j) { const float d = v[rr][j] - s1[rr]; q += d * d; }
      q1[rr] = lane < 56 ? q : 0.f;
    }
#pragma unroll
    for (int o = 1; o < 64; o <<= 1)
#pragma unroll
      for (int rr = 0; rr < 8; ++rr) q1[rr] += __shfl_xor(q1[rr], o);
#pragma unroll
    for (int rr = 0; rr < 8; ++rr) {
      const int row = (bb - 224) * 64 + w * 8 + rr;
      const float den = ln_den(q1[rr] * (1.f / 448.f)), mb = rbf(s1[rr]);
      float o8[8];
#pragma unroll
      for (int j = 0; j < 8; ++j) o8[j] = rbf(v[rr][j] - mb) / den;
      if (lane < 56)
        *(v4u*)(ws + OFF_XN + ((size_t)(row >> 4) * 14 + ks) * 1024 + (kc * 16 + (row & 15)) * 16) =
            v4u{pk2(o8[0], o8[1]), pk2(o8[2], o8[3]), pk2(o8[4], o8[5]), pk2(o8[6], o8[7])};
    }
    if (b == 224) {
      if (w < 7) {
        const v4u vv = v4u{nosent(ma.x), nosent(ma.y), nosent(mb2.x), nosent(mb2.y)};
        *(v4u*)(ws + OFF_EX + pc * 16) = vv;
        *(v4u*)(ws + OFF_EX + 8192 + pc * 16) = vv;
      } else {
        *(v4u*)(ws + OFF_EX + 7168 + lane * 16) = sent;
        *(v4u*)(ws + OFF_EX + 8192 + 7168 + lane * 16) = sent;
      }
    }
  }
  TLP(4);
}
#define STAMP(k)
#define STAMPV(k, v)
#define STAMP_INIT()
#define STAMP_DUMP()
constexpr int L_MEM = 0;
constexpr int L_P1 = 16384;
constexpr int L_YB = 24576;
constexpr int N_S = 56, N_O = 56, N_X = 8, N_CHAIN = N_S + N_O + N_X;
constexpr int L_TAB2 = 22528;
constexpr int L_VOTE = 23808;
#define L_DUMMY (14336 + lane * 16)
#define ex_off(s, c) __builtin_amdgcn_readfirstlane(*(const int*)(smem + L_TAB2 + ((s) * 5 + (c)) * 4))
#define T_A 106
#define T_B 100
#define T_SW 26
#define T_DA 0
#define T_DB 0
#define T_R2 8
#define T_R1 0
#define T_Q1 15
#define T_Q2 70
#define PFD 4
#define T_EX 30
#define T_EXN 4
#define T_UP2 2
#define D_ARM 200
#define D_O 80
#define D_S 0
#define T_MIN 0
#define T_SW2 28
#define T_EX2 6
#define T_EX2B 4
#define NREP 4
#define T_OG 0
#define T_PEN 35
#define T_UP 2
#define T_OKN 1000
constexpr int L_SCH = 23872;
DI unsigned rt32() { unsigned long long t; asm volatile("s_memrealtime %0\n\ts_waitcnt lgkmcnt(0)" : "=s"(t) :: "memory"); return (unsigned)t; }
DI unsigned wait_until(unsigned t) {
  int d = (int)(t - rt32());
  if (d < -2) return t - d;
  if (d > 3000) d = 3000;
  if (d > 24) { sleep_units(((d - 16) * 3) >> 3); d = (int)(t - rt32()); }
  if (d > 0 && d < 64) sleep_units((d * 3) >> 3);
  return t;
}
struct Sched { unsigned tE1, tE2; int m1, m2, ok, nf, late; };
DI void sched_step(Sched& y, unsigned word, int lane, int s) {
  if (s == 0) { y.tE1 = rt32(); y.tE2 = y.tE1; return; }
  int dv = lane < NREP ? (int)((word & ~3u) - (y.tE1 << 2)) >> 2 : -0x40000000;
  unsigned fl = lane < NREP ? (word & 3u) : 0u;
  if (s > 1 && __all(lane >= NREP || word == (y.tE1 << 2))) { dv = 0; fl = 0; }
  else {
#pragma unroll
    for (int o = 1; o < NREP; o <<= 1) { dv = max(dv, __shfl_xor(dv, o)); fl |= (unsigned)__shfl_xor((int)fl, o); }
    dv = __builtin_amdgcn_readfirstlane(dv); fl = __builtin_amdgcn_readfirstlane(fl);
  }
  unsigned base = y.tE2;
  if (dv > 0 || s == 1) base = y.tE1 + dv + T_B + y.m2 + T_PEN;
  if (s > 2) {
    if (!y.late) {
      if (fl & 1u) { y.m2 += T_UP; y.ok = 0; }
      else if (fl & 2u) { y.m1 += T_UP; y.ok = 0; }
      else if (++y.ok >= T_OKN) { y.ok = 0; if (y.m1 > T_MIN) y.m1 -= 1; if (y.m2 > T_MIN) y.m2 -= 1; }
    } else if (dv > 0) { y.m1 += T_UP2; y.m2 += T_UP2; }
  }
  y.late = dv > 0 ? 1 : 0;
  if (s == T_SW) { y.m1 -= T_DA; y.m2 -= T_DB; }
  const int ex = s < T_EXN ? T_EX : (s < T_SW2 ? T_EX2 : 0), exb = s < T_EXN ? (T_EX >> 1) : (s < T_SW2 ? T_EX2B : 0);
  y.tE1 = base + T_A + y.m1 + ex;
  y.tE2 = y.tE1 + T_B + y.m2 + exb;
}
#define FAIL_DUMP(v)

DI int gather_poll(__amdgpu_buffer_rsrc_t rx, unsigned char* dst0, unsigned char* dst1, int o0, int o1, int ot, unsigned& word) {
  int spin = 0;
#pragma nounroll
  for (;;) {
    asm volatile("" ::: "memory");
    const v4u v0 = ld_sc1(rx, o0), v1 = ld_sc1(rx, o1);
    word = __builtin_amdgcn_raw_buffer_load_b32(rx, ot, 0, AUX_SC1);
    *(v4u*)dst0 = v0;
    *(v4u*)dst1 = v1;
    if (__builtin_expect(__all(umax3(umax4(v0), umax4(v1), word) != SENT), 1)) break;
    if (spin == 0) { o0 ^= 8192; o1 ^= 8192; ot ^= 8192; sleep_units(T_R2); }
    if (++spin >= SPIN_MAX) break;
    __builtin_amdgcn_s_sleep(2);
  }
  return spin;
}

DI void probe_place(const Params& p, const rsrc_t rx) {
  unsigned char* ws = p.ws;
  const int lane = threadIdx.x & 63, w = __builtin_amdgcn_readfirstlane(threadIdx.x >> 6);
  const unsigned a0 = (unsigned)(size_t)(ws + OFF_EX);
    if (w >= 4) {
      const int r = w - 4;
      int pr = -1;
      for (int k = 0; k < 64; ++k) if (pr < 0 && (int)(((a0 + (unsigned)k * 81920u) >> 21) - (a0 >> 21)) == r) pr = k * 5;
      unsigned res = 2;
      if (pr >= 0) {
        const int base = (int)OFF_EX + pr * 16384 + 7168;
        const v4u sent = {SENT, SENT, SENT, SENT};
        __builtin_amdgcn_raw_buffer_store_b128(sent, rx, base + lane * 16, 0, AUX_SC1);
        __builtin_amdgcn_raw_buffer_store_b128(sent, rx, base + 8192 + lane * 16, 0, AUX_SC1);
        asm volatile("s_waitcnt vmcnt(0)" ::: "memory");
        unsigned best[2] = {0xFFFFFFFFu, 0xFFFFFFFFu};
#pragma unroll
        for (int k = 0; k < 8; ++k) {
          unsigned long long t0, t1;
          asm volatile("s_memtime %0\n\ts_waitcnt lgkmcnt(0)" : "=s"(t0) :: "memory");
          unsigned v = __builtin_amdgcn_raw_buffer_load_b32(rx, base + (k & 1) * 8192 + (k >> 1) * 128, 0, AUX_SC1);
          asm volatile("s_waitcnt vmcnt(0)" :: "v"(v) : "memory");
          asm volatile("s_memtime %0\n\ts_waitcnt lgkmcnt(0)" : "=s"(t1) :: "memory");
          best[k & 1] = min(best[k & 1], (unsigned)(t1 - t0));
        }
        const unsigned b13 = ((a0 + (unsigned)pr * 16384u) >> 13) & 1;
        res = 2 + ((best[1] < best[0]) ? (b13 ^ 1) : b13);
      }
      if (lane == 0) __builtin_amdgcn_raw_buffer_store_b32(res, rx, (int)OFF_PTAB + r * 4, 0, AUX_SC1);
    }
}

DI void chain_role(const Params& p, unsigned char* smem, const rsrc_t rx, const int cu) {
  unsigned char* ws = p.ws;
  const int tid = threadIdx.x, lane = tid & 63, w = __builtin_amdgcn_readfirstlane(tid >> 6);
  const int kg = lane >> 4, n = lane & 15;
  const float sc = 0.0625f * 1.4426950408889634f;
  STAMP_INIT();
  auto startup = [&]() {
  {
    const v4u sent = {SENT, SENT, SENT, SENT};
    constexpr int EX_KB = (int)(64 * EX_STEP / 1024);
    for (int i = 16 + cu * 8 + w; i < EX_KB; i += N_CHAIN * 8) st4_sc1(sent, rx, (int)OFF_EX + i * 1024 + lane * 16);
    if (cu * 8 + w < 448) st4_sc1(sent, rx, (cu * 8 + w < 224 ? (int)OFF_KM + 62 * 114688 + (cu * 8 + w) * 1024 : (int)OFF_VM + 62 * 114688 + (cu * 8 + w - 224) * 1024) + lane * 16);
    DRAIN();
    __syncthreads();
    if (tid == 448) arrive(ws, rx, CTR_FX, (unsigned)N_CHAIN);
  }
  {
    const unsigned a0 = (unsigned)(size_t)(ws + OFF_EX);
    if (w == 0) {
      unsigned v = 0;
      for (int spin = 0; spin < SPIN_MAX; ++spin) {
        asm volatile("" ::: "memory");
        v = __builtin_amdgcn_raw_buffer_load_b32(rx, (int)OFF_PTAB + (lane & 3) * 4, 0, AUX_SC1);
        if (__all(v != 0)) break;
        __builtin_amdgcn_s_sleep(4);
      }
      ctr_wait(rx, CTR_FX, (unsigned)N_CHAIN, 4);
      if (lane < 4) *(unsigned*)(smem + L_VOTE + lane * 4) = v;
      if (lane < 2) *(unsigned*)(smem + L_SCH + lane * 4) = 0u;
    }
    __syncthreads();
    const unsigned a = a0 + (unsigned)tid * 16384u;
    const int reg = (int)((a >> 21) - (a0 >> 21)) & 3, b13 = (a >> 13) & 1;
    if (tid < 320) { const int nearbit = *(const int*)(smem + L_VOTE + reg * 4) & 1; *(int*)(smem + L_TAB2 + tid * 4) = (int)OFF_EX + tid * 16384 + ((nearbit != b13) ? 8192 : 0); }
    __syncthreads();
  }
  };
  const int lmoff = (kg >> 1) * 256 + ((n & 7) >> 2) * 128 + (kg & 1) * 64 + (n & 3) * 16;
  const int goff = (w - 4) * 2048 + lane * 16;
  if (cu < N_S) {
    const int c = cu, m7 = c / 7, r7 = c % 7;
    sleep_units(D_S);
    if (w < 4) {
      const int tile = 8 * c + 2 * w, hS = tile / 28, ksj = (tile % 28) >> 1;
      v4u Mf[2][KS];
#pragma unroll
      for (int q = 0; q < 2; ++q)
#pragma unroll
        for (int ks = 0; ks < KS; ++ks) Mf[q][ks] = *(const v4u*)(ws + OFF_WC + ((size_t)(tile + q) * 14 + ks) * 1024 + lane * 16);
      startup();
#pragma unroll
      for (int ks = 0; ks < KS; ++ks) { KEEP4(Mf[0][ks]); KEEP4(Mf[1][ks]); }
      unsigned char* pdst = smem + ((kg < 2 && n < 8) ? L_P1 + (kg * 8 + n) * 16 + w * 4 : L_DUMMY);
      const int kmo = (int)OFF_KM + ((hS * 14 + ksj) * 4 + kg) * 128 + n * 16;
#pragma nounroll
      for (int s = 0; s < NSEG; ++s) {
        const unsigned char* lm = smem + L_MEM + (s & 1) * 7168 + lmoff;
        BAR();
        STAMP(2);
        v4u mf[KS + 1];
#pragma unroll
        for (int ks = 0; ks < PFD; ++ks) mf[ks] = *(const v4u*)(lm + ks * 512);
        __builtin_amdgcn_sched_barrier(0);
        f32x4 a0 = {0, 0, 0, 0}, a1 = {0, 0, 0, 0};
#pragma unroll
        for (int ks = 0; ks < KS; ++ks) {
          if (ks + PFD < KS) mf[ks + PFD] = *(const v4u*)(lm + (ks + PFD) * 512);
          if (ks + PFD == KS) mf[KS] = *(const v4u*)(lm + ksj * 512);
          a0 = mfma32(Mf[0][ks], mf[ks], a0); a1 = mfma32(Mf[1][ks], mf[ks], a1);
          __builtin_amdgcn_sched_barrier(0);
        }
        const v4u piece = mf[KS];
        const v4u tb = pk8(a0, a1);
        const f32x4 d2 = mfma32(tb, piece, f32x4{0, 0, 0, 0});
        *(float*)(pdst) = d2.x; *(float*)(pdst + 256) = d2.y; *(float*)(pdst + 512) = d2.z; *(float*)(pdst + 768) = d2.w;
        STAMP(3);
        BAR();
        st4_sc1(nosent4(tb), rx, n < 8 ? kmo + s * (16 * 14 * 512) : OOB);
        DRAIN();
      }
      BAR();
    } else {
      startup();
      Sched sd = {0u, 0u, 0, 0, 0, 0, 1};
      const bool strad = r7 == 3;
      const int ii = lane & 7, key = ((lane >> 3) & 1) * 4 + (lane >> 4);
      const int hh = 2 * m7 + (r7 > 3 ? 1 : 0), pp = r7 > 3 ? r7 - 3 : r7;
      const int so1 = ((hh * 4 + pp) * 4 + (ii & 3)) * 32 + key * 4;
      const int so2 = (((2 * m7 + 1) * 4 + 0) * 4 + (ii & 3)) * 32 + key * 4;
      const unsigned char* psrc = smem + L_P1 + lane * 16;
#pragma nounroll
      for (int s = 0; s < NSEG; ++s) {
        const int o0 = ex_off(s, 0) + goff;
        const int eo2 = ex_off(s, 2), eo4 = ex_off(s, 4);
        const int eb = (ii >> 2) ? eo4 : eo2;
        const int sto1 = (w == 4 && s < NSEG - 1) ? eb + so1 : OOB;
        const int sto2 = (w == 4 && s < NSEG - 1 && strad) ? eb + so2 : OOB;
        unsigned char* d0 = smem + L_MEM + (s & 1) * 7168 + goff;
        unsigned char* d1 = w == 7 ? smem + L_DUMMY : d0 + 1024;
        const int ot = (s > 0 && lane < NREP) ? o0 - goff + 7168 + lane * 128 : OOB;
        unsigned word;
        KEEP4(sto1); KEEP4(sto2);
        if (s > 1) wait_until(sd.tE2);
        else if (s == 1) { int kn = 0; kv_ensure(rx, 0, kn, 8); sleep_units(T_Q2); }
        STAMP(0);
        const int spin = gather_poll(rx, d0, d1, o0, w == 7 ? OOB : o0 + 1024, ot, word);
        BAR();
        STAMP(1);
        if (w == 7) TL(s);
        BAR();
        const f32x4 a = *(const f32x4*)psrc;
        const float u0 = a.x + a.y, u1 = a.z + a.w;
        const float v1 = strad ? u0 : u0 + u1;
        st1_sc1(nosent(__float_as_uint(v1)), rx, sto1);
        st1_sc1(nosent(__float_as_uint(u1)), rx, sto2);
        st1_sc1(nosent(__float_as_uint(v1)), rx, sto1 ^ 8192);
        st1_sc1(nosent(__float_as_uint(u1)), rx, sto2 ^ 8192);
        __builtin_amdgcn_sched_barrier(0);
        STAMP(5);
        STAMPV(10, spin);
        sched_step(sd, word, lane, s);
        sd.nf += (s > 1 && spin) ? 1 : 0;
        if (w == 6 && lane == 0 && s > 0) arrive(ws, rx, CTR_M + (s - 1) * 128, 84u);
      }
      BAR();
      if (w == 6 && lane == 0) arrive(ws, rx, CTR_M + (NSEG - 1) * 128, 84u);
      FAIL_DUMP(sd.nf);
    }
  } else if (cu < N_S + N_O) {
    const int o_ = cu - N_S, rg = o_ / 28, ct = o_ % 28;
    sleep_units(D_O);
    v4u Nf[2][KS];
#pragma unroll
    for (int q = 0; q < 2; ++q)
#pragma unroll
      for (int ks = 0; ks < KS; ++ks) Nf[q][ks] = *(const v4u*)(ws + OFF_WC + ((size_t)(448 + (2 * w + q) * 28 + ct) * 14 + ks) * 1024 + lane * 16);
    startup();
#pragma unroll
    for (int ks = 0; ks < KS; ++ks) { KEEP4(Nf[0][ks]); KEEP4(Nf[1][ks]); }
    auto publish_y = [&](int s) {
      const int h = 4 * (w - 4) + kg;
      const unsigned char* ys = smem + L_YB + (h * 4) * 64 + n * 4;
      const v4u y = v4u{*(const unsigned*)(ys), *(const unsigned*)(ys + 64), *(const unsigned*)(ys + 128), *(const unsigned*)(ys + 192)};
      st4_sc1(nosent4(y), rx, rg == 0 ? (int)OFF_VM + ((s * 16 + h) * 28 + ct) * 256 + n * 16 : OOB);
    };
    auto compute_y = [&](int s) {
      const unsigned char* lm = smem + L_MEM + (s & 1) * 7168 + lmoff;
      v4u mf[KS];
#pragma unroll
      for (int ks = 0; ks < PFD; ++ks) mf[ks] = *(const v4u*)(lm + ks * 512);
      __builtin_amdgcn_sched_barrier(0);
      f32x4 a0 = {0, 0, 0, 0}, a1 = {0, 0, 0, 0};
#pragma unroll
      for (int ks = 0; ks < KS; ++ks) {
        if (ks + PFD < KS) mf[ks + PFD] = *(const v4u*)(lm + (ks + PFD) * 512);
        a0 = mfma32(mf[ks], Nf[0][ks], a0); a1 = mfma32(mf[ks], Nf[1][ks], a1);
        __builtin_amdgcn_sched_barrier(0);
      }
      v2u y0 = pk4(a0), y1 = pk4(a1);
      unsigned char* yd = smem + (kg < 2 ? L_YB + ((2 * w) * 4 + 2 * kg) * 64 + n * 4 : L_DUMMY);
      *(unsigned*)(yd) = y0.x; *(unsigned*)(yd + 64) = y0.y;
      *(unsigned*)(yd + 256) = y1.x; *(unsigned*)(yd + 320) = y1.y;
    };
    if (w < 4) {
      int nf = 0;
      v4u Vf[4];
      int known = 0;
      unsigned aold = 0;
      kv_ensure(rx, 0, known, 4);
#pragma unroll
      for (int g2 = 0; g2 < 4; ++g2) Vf[g2] = ld_sc1(rx, (int)OFF_VP + ((4 * w + g2) * 28 + ct) * 1024 + lane * 16);
      const int g = n >> 2, i4 = n & 3, hP = 4 * w + g;
      unsigned char* pdst = smem + L_P1 + (kg * 4 + i4) * 80 + (w * 4 + g) * 4;
      const unsigned char* ys = smem + L_YB + ((4 * w) * 4 + 2 * (kg & 1) + (kg >> 1)) * 64 + n * 4;
#pragma nounroll
      for (int s = 0; s < NSEG; ++s) {
        const int eb = ex_off(s, 1 + 2 * rg);
        int oe = eb + (hP * 4 + i4) * 64 + kg * 16;
        int ol = eb + 4096 + (hP * 4 + i4) * 4;
        int om = ex_off(s, 2 + 2 * rg) + ((hP * 4 + 2 * (kg >> 1)) * 4 + i4) * 32 + (kg & 1) * 16;
        KEEP4(oe); KEEP4(ol); KEEP4(om);
        BAR();
        STAMP(2);
        compute_y(s);
#pragma unroll
        for (int g2 = 0; g2 < 4; ++g2) KEEP4(Vf[g2]);
        if (w == 3 && lane == 0 && rg == 0 && s > 0) arrive_finish(rx, aold, CTR_M + (s - 1) * 128, 84u);
        if (s == NSEG - 1) break;
        unsigned tiss;
        for (;;) {
          const v2u tv = *(volatile const v2u*)(smem + L_SCH);
          tiss = __builtin_amdgcn_readfirstlane(tv.x);
          if (__builtin_amdgcn_readfirstlane(tv.y) == (unsigned)(s + 1)) break;
        }
        STAMP(3);
        if (s > 0) tiss = wait_until(tiss);
        else { sleep_units(T_Q1); tiss = rt32(); }
        STAMP(6);
        int spin = 0;
        f32x4 dres;
        v4u Yf = v4u{0, 0, 0, 0};
#pragma nounroll
        for (;;) {
          asm volatile("" ::: "memory");
          const v4u q0 = ld_sc1(rx, om), q1 = ld_sc1(rx, om + 128);
          const unsigned ls = __builtin_amdgcn_raw_buffer_load_b32(rx, ol, 0, AUX_SC1);
          const v4u ex = ld_sc1(rx, oe);
          __builtin_amdgcn_sched_barrier(0);
          if (spin == 0) {
            BAR();
            Yf = v4u{*(const unsigned*)(ys), *(const unsigned*)(ys + 256), *(const unsigned*)(ys + 512), *(const unsigned*)(ys + 768)};
            KEEP4(Yf);
          }
          __builtin_amdgcn_sched_barrier(0);
          const f32x4 sq = __builtin_bit_cast(f32x4, q0) + __builtin_bit_cast(f32x4, q1);
          auto ra = __builtin_amdgcn_permlane32_swap(__float_as_uint(sq.x), __float_as_uint(sq.z), false, false);
          auto rb = __builtin_amdgcn_permlane32_swap(__float_as_uint(sq.y), __float_as_uint(sq.w), false, false);
          const unsigned ra0 = ra[0], ra1 = ra[1], rb0 = rb[0], rb1 = rb[1];
          const float sm0 = __uint_as_float(ra0) + __uint_as_float(ra1), sm1 = __uint_as_float(rb0) + __uint_as_float(rb1);
          const float lse = __uint_as_float(ls);
          const float mx = xmax32(xmax16(fmaxf(sm0, sm1)));
          const float nb = -fmaxf(mx * sc, lse);
          const float e0 = __builtin_amdgcn_exp2f(__builtin_fmaf(sm0, sc, nb)), e1 = __builtin_amdgcn_exp2f(__builtin_fmaf(sm1, sc, nb));
          const float wx = __builtin_amdgcn_exp2f(lse + nb);
          const unsigned pk = pk2(e0, e1);
          const v4u pb = v4u{g == 0 ? pk : 0u, g == 1 ? pk : 0u, g == 2 ? pk : 0u, g == 3 ? pk : 0u};
          const f32x4 dx0 = mfma32(Vf[0], ex, f32x4{0, 0, 0, 0});
          const f32x4 dx1 = mfma32(Vf[1], ex, f32x4{0, 0, 0, 0});
          const f32x4 dx2 = mfma32(Vf[2], ex, f32x4{0, 0, 0, 0});
          const f32x4 dx3 = mfma32(Vf[3], ex, f32x4{0, 0, 0, 0});
          const f32x4 dm = mfma32(Yf, pb, f32x4{0, 0, 0, 0});
          const float inv = __builtin_amdgcn_rcpf(wx + xsum32(xsum16(e0 + e1)));
          const unsigned mxu = umax3(umax3(umax4(q0), umax4(q1), ls), umax4(ex), 0u);
          const f32x4 dxa = g < 2 ? (g == 0 ? dx0 : dx1) : (g == 2 ? dx2 : dx3);
          dres = (dxa * wx + dm) * inv;
          *(float*)(pdst) = dres.x; *(float*)(pdst + 1280) = dres.y; *(float*)(pdst + 2560) = dres.z; *(float*)(pdst + 3840) = dres.w;
          if (__builtin_expect(__all(mxu != SENT), 1)) break;
          if (spin == 0) { oe ^= 8192; ol ^= 8192; om ^= 8192; sleep_units(T_R1); } else __builtin_amdgcn_s_sleep(2);
          if (++spin >= SPIN_MAX) break;
          tiss = rt32();
        }
        *(unsigned*)(smem + L_SCH + 16 + w * 4) = (tiss << 2) | (spin ? 2u : 0u);
        DRAIN();
        STAMP(8);
        BAR();
        STAMPV(11, spin);
        nf += (s > 1 && spin) ? 1 : 0;
        if (w == 3 && lane == 0 && rg == 0) aold = arrive_issue(ws, CTR_M + s * 128);
        const int sn = s + 1 < NSEG - 1 ? s + 1 : s;
        kv_ensure(rx, sn >> 1, known, 4);
#pragma unroll
        for (int g2 = 0; g2 < 4; ++g2) Vf[g2] = ld_sc1(rx, (int)OFF_VP + (sn * 448 + (4 * w + g2) * 28 + ct) * 1024 + lane * 16);
      }
      BAR();
      BAR();
      FAIL_DUMP(nf);
    } else {
      Sched sd = {0u, 0u, 0, 0, 0, 0, 1};
      const unsigned char* pp = smem + L_P1 + lane * 80;
      const bool st = w == 4 && (kg & 1) == 0;
      const int eoff = ct * 256 + rg * 128 + ((((n >> 3) & 1) * 4 + (n & 3)) * 8 + ((n >> 2) & 1) * 4 + kg) * 2;
#pragma nounroll
      for (int s = 0; s < NSEG; ++s) {
        const int o0 = ex_off(s, 0) + goff;
        unsigned char* d0 = smem + L_MEM + (s & 1) * 7168 + goff;
        unsigned char* d1 = w == 7 ? smem + L_DUMMY : d0 + 1024;
        const int ot = (s > 0 && lane < NREP) ? o0 - goff + 7168 + lane * 128 : OOB;
        unsigned word;
        if (s > 1) wait_until(sd.tE2 + T_OG);
        else if (s == 1) sleep_units(8);
        STAMP(0);
        const int spin = gather_poll(rx, d0, d1, o0, w == 7 ? OOB : o0 + 1024, ot, word);
        __builtin_amdgcn_s_setprio(3);
        BAR();
        STAMP(1);
        sched_step(sd, word, lane, s);
        if (w == 4) *(volatile v2u*)(smem + L_SCH) = v2u{sd.tE1, (unsigned)(s + 1)};
        __builtin_amdgcn_s_setprio(0);

        sd.nf += (s > 1 && spin) ? 1 : 0;
        *(unsigned*)(smem + L_SCH + 32 + (w - 4) * 4) = (s > 1 && spin) ? 1u : 0u;
        compute_y(s);
        if (s == NSEG - 1) break;
        const int eon = ex_off(s + 1, 0);
        const int sto = st ? eon + eoff : ((w == 5 && lane == 0 && o_ % (56 / NREP) == 0) ? eon + 7168 + (o_ / (56 / NREP)) * 128 : OOB);
        BAR();
        publish_y(s);
        const v4u gf = *(const v4u*)(smem + L_SCH + 32);
        const unsigned gfl = gf.x | gf.y | gf.z | gf.w;
        const unsigned tref = sd.tE1 << 2;
        KEEP4(sto); KEEP4(gfl);
        DRAIN();
        if (w == 4) {
          BAR();
          STAMP(4);
          const f32x4 a = (*(const f32x4*)(pp) + *(const f32x4*)(pp + 16)) + (*(const f32x4*)(pp + 32) + *(const f32x4*)(pp + 48));
          const float v = (a.x + a.y) + (a.z + a.w);
          auto r = __builtin_amdgcn_permlane16_swap(__float_as_uint(v), __float_as_uint(v), false, false);
          const unsigned r0 = r[0], r1 = r[1];
          const unsigned ov = nosent(pk2(__uint_as_float(r0), __uint_as_float(r1)));
          st1_sc1(ov, rx, sto);
          st1_sc1(ov, rx, sto ^ 8192);
        } else {
          BAR();
          const v4u tw = *(const v4u*)(smem + L_SCH + 16);
          const unsigned tm = tref + (unsigned)max(max((int)(tw.x - tref), (int)(tw.y - tref)), max((int)(tw.z - tref), (int)(tw.w - tref)));
          const unsigned wd = nosent(tm | ((tw.x | tw.y | tw.z | tw.w) & 3u) | gfl);
          st1_sc1(wd, rx, sto);
          st1_sc1(wd, rx, sto ^ 8192);
        }
        __builtin_amdgcn_sched_barrier(0);
        STAMP(5);
        STAMPV(10, spin);
      }
      BAR();
      publish_y(NSEG - 1);
      DRAIN();
      BAR();
      if (w == 6 && lane == 0 && rg == 0) arrive(ws, rx, CTR_M + (NSEG - 1) * 128, 84u);
      FAIL_DUMP(w == 7 ? sd.m1 * 1000 + sd.m2 : sd.nf);
    }
  } else {
    startup();
    if (w < 4) {
      const int h = 2 * (cu - N_S - N_O) + (w >> 1), u = w & 1;
      v4u K[KS];
      int known = 0;
      kv_ensure(rx, 0, known, 4);
#pragma unroll
      for (int ks = 0; ks < KS; ++ks) K[ks] = ld_sc1(rx, (int)OFF_KP + ((h * 2 + u) * 14 + ks) * 1024 + lane * 16);
      unsigned char* xw = smem + (kg == 0 ? L_P1 + w * 128 + n * 8 : L_DUMMY);
      const unsigned char* xr = smem + L_P1 + (w ^ 1) * 128 + n * 8;
#pragma nounroll
      for (int s = 0; s < NSEG - 1; ++s) {
        const int eo1 = ex_off(s, 1), eo3 = ex_off(s, 3);
        const int eb = ((n >> 2) & 1) ? eo3 : eo1;
        const int so = n < 8 ? eb + (h * 4 + (n & 3)) * 64 + kg * 16 + u * 8 : OOB;
        const int lo = (lane < 8 && u == 0) ? eb + 4096 + (h * 4 + (n & 3)) * 4 : OOB;
        const unsigned char* lm = smem + L_MEM + (s & 1) * 7168 + lmoff;
#pragma unroll
        for (int ks = 0; ks < KS; ++ks) KEEP4(K[ks]);
        KEEP4(so); KEEP4(lo);
        BAR();
        STAMP(2);
        v4u mf[KS];
#pragma unroll
        for (int ks = 0; ks < PFD + 2; ++ks) mf[ks] = *(const v4u*)(lm + ks * 512);
        __builtin_amdgcn_sched_barrier(0);
        f32x4 a0 = {0, 0, 0, 0}, a1 = {0, 0, 0, 0};
#pragma unroll
        for (int ks = 0; ks < KS; ks += 2) {
          if (ks + PFD + 2 < KS) mf[ks + PFD + 2] = *(const v4u*)(lm + (ks + PFD + 2) * 512);
          if (ks + PFD + 3 < KS) mf[ks + PFD + 3] = *(const v4u*)(lm + (ks + PFD + 3) * 512);
          a0 = mfma32(K[ks], mf[ks], a0);
          a1 = mfma32(K[ks + 1], mf[ks + 1], a1);
          __builtin_amdgcn_sched_barrier(0);
        }
        a0 += a1;
        float xs[4] = {rbf(a0.x), rbf(a0.y), rbf(a0.z), rbf(a0.w)};
        const float mx = xmax32(xmax16(fmaxf(fmaxf(xs[0], xs[1]), fmaxf(xs[2], xs[3])))) * sc;
#pragma unroll
        for (int j = 0; j < 4; ++j) xs[j] = __builtin_amdgcn_exp2f(__builtin_fmaf(xs[j], sc, -mx));
        const float sum = xsum32(xsum16((xs[0] + xs[1]) + (xs[2] + xs[3])));
        *(f32x2*)xw = f32x2{mx, sum};
        BAR();
        const f32x2 ot = *(const f32x2*)xr;
        const float mm = fmaxf(mx, ot.x);
        const float fu = __builtin_amdgcn_exp2f(mx - mm), fo = __builtin_amdgcn_exp2f(ot.x - mm);
        const float pu = sum * fu, po = ot.y * fo;
        const float tot = u ? po + pu : pu + po;
        const float scl = fu * __builtin_amdgcn_rcpf(tot);
        const v2u o = nosent2(v2u{pk2(xs[0] * scl, xs[1] * scl), pk2(xs[2] * scl, xs[3] * scl)});
        st2_sc1(o, rx, so);
        const unsigned lse = nosent(__float_as_uint(__builtin_amdgcn_logf(tot) + mm));
        st1_sc1(lse, rx, lo);
        st2_sc1(o, rx, so ^ 8192);
        st1_sc1(lse, rx, lo ^ 8192);
        __builtin_amdgcn_sched_barrier(0);
        STAMP(5);
        const int sn = s + 1 < NSEG - 1 ? s + 1 : s;
        kv_ensure(rx, sn >> 1, known, 4);
#pragma unroll
        for (int ks = 0; ks < KS; ++ks) K[ks] = ld_sc1(rx, (int)OFF_KP + (((sn * 16 + h) * 2 + u) * 14 + ks) * 1024 + lane * 16);
      }
    } else {
      Sched sd = {0u, 0u, 0, 0, 0, 0, 1};
#pragma nounroll
      for (int s = 0; s < NSEG - 1; ++s) {
        const int o0 = ex_off(s, 0) + goff;
        unsigned char* d0 = smem + L_MEM + (s & 1) * 7168 + goff;
        unsigned char* d1 = w == 7 ? smem + L_DUMMY : d0 + 1024;
        const int ot = (s > 0 && lane < NREP) ? o0 - goff + 7168 + lane * 128 : OOB;
        unsigned word;
        if (s > 1) wait_until(sd.tE2);
        else if (s == 1) { int kn = 0; kv_ensure(rx, 0, kn, 8); sleep_units(T_Q2); }
        STAMP(0);
        const int spin = gather_poll(rx, d0, d1, o0, w == 7 ? OOB : o0 + 1024, ot, word);
        BAR();
        STAMP(1);
        BAR();
        STAMPV(10, spin);
        sched_step(sd, word, lane, s);
        sd.nf += (s > 1 && spin) ? 1 : 0;
      }
      FAIL_DUMP(sd.nf);
    }
  }
  STAMP_DUMP();
}
DI void phase_kv(const Params& p, unsigned char* lds, rsrc_t rw, int cg, int par) {
  unsigned char* ws = p.ws;
  const int tid = threadIdx.x, lane = tid & 63, w = __builtin_amdgcn_readfirstlane(tid >> 6);
  const int nt0 = cg * 16 + 2 * w;
  v4u st[7];
  {
    const unsigned char* src = ws + OFF_XN + (size_t)par * 57344 + tid * 16;
#pragma unroll
    for (int i = 0; i < 7; ++i) st[i] = *(const v4u*)(src + i * 8192);
  }
  v4u W[2][KS];
#pragma unroll
  for (int q = 0; q < 2; ++q)
#pragma unroll
    for (int ks = 0; ks < KS; ++ks) W[q][ks] = *(const v4u*)(ws + OFF_WC + ((size_t)(nt0 + q) * 14 + ks) * 1024 + lane * 16);
#pragma unroll
  for (int i = 0; i < 7; ++i) *(v4u*)(lds + i * 8192 + tid * 16) = st[i];
#pragma unroll
  for (int ks = 0; ks < KS; ++ks) { KEEP4(W[0][ks]); KEEP4(W[1][ks]); }
  BAR();
  const bool isK = cg < 28;
  const int kbase = (int)OFF_KP + (((nt0 / 28) * 2) * 14 + ((nt0 % 28) >> 1)) * 1024 + lane * 16;
  const int vbase = (int)OFF_VP + (nt0 - 448) * 1024 + lane * 16;
  int pend = -1;
  unsigned aold = 0;
#pragma nounroll
  for (int it = 0; it < 16; ++it) {
    const int hb = par + 2 * it;
    const unsigned char* cur = lds + (it & 1) * 57344 + lane * 16;
    {
      const int so = (int)OFF_XN + (it + 1 < 16 ? hb + 2 : hb) * 57344;
#pragma unroll
      for (int i = 0; i < 7; ++i) st[i] = __builtin_amdgcn_raw_buffer_load_b128(rw, tid * 16, so + i * 8192, 0);
    }
    __builtin_amdgcn_sched_barrier(0);
    f32x4 acc[4][2];
#pragma unroll
    for (int rt = 0; rt < 4; ++rt) { acc[rt][0] = f32x4{0, 0, 0, 0}; acc[rt][1] = f32x4{0, 0, 0, 0}; }
    if (isK) {
      v4u af[2][4];
#pragma unroll
      for (int rt = 0; rt < 4; ++rt) af[0][rt] = *(const v4u*)(cur + (rt * 14) * 1024);
#pragma unroll
      for (int ks = 0; ks < KS; ++ks) {
        if (ks + 1 < KS) {
#pragma unroll
          for (int rt = 0; rt < 4; ++rt) af[(ks + 1) & 1][rt] = *(const v4u*)(cur + (rt * 14 + ks + 1) * 1024);
        }
        __builtin_amdgcn_sched_barrier(0);
#pragma unroll
        for (int rt = 0; rt < 4; ++rt) {
          acc[rt][0] = mfma32(W[0][ks], af[ks & 1][rt], acc[rt][0]);
          acc[rt][1] = mfma32(W[1][ks], af[ks & 1][rt], acc[rt][1]);
        }
        __builtin_amdgcn_sched_barrier(0);
      }
    } else {
      v4u af[2][4];
#pragma unroll
      for (int rt = 0; rt < 4; ++rt) af[0][rt] = *(const v4u*)(cur + (rt * 14) * 1024);
#pragma unroll
      for (int ks = 0; ks < KS; ++ks) {
        if (ks + 1 < KS) {
#pragma unroll
          for (int rt = 0; rt < 4; ++rt) af[(ks + 1) & 1][rt] = *(const v4u*)(cur + (rt * 14 + ks + 1) * 1024);
        }
        __builtin_amdgcn_sched_barrier(0);
#pragma unroll
        for (int rt = 0; rt < 4; ++rt) {
          acc[rt][0] = mfma32(af[ks & 1][rt], W[0][ks], acc[rt][0]);
          acc[rt][1] = mfma32(af[ks & 1][rt], W[1][ks], acc[rt][1]);
        }
        __builtin_amdgcn_sched_barrier(0);
      }
    }
    __builtin_amdgcn_sched_barrier(0);
#pragma unroll
    for (int i = 0; i < 7; ++i) *(v4u*)(lds + ((it + 1) & 1) * 57344 + i * 8192 + tid * 16) = st[i];
    DRAIN();
    if (pend >= 0 && lane == 0) arrive_finish(rw, aold, pend, 56u);
    pend = -1;
    __builtin_amdgcn_sched_barrier(0);
    if (isK) {
#pragma unroll
      for (int rt = 0; rt < 4; ++rt) st4_sc1(pk8(acc[rt][0], acc[rt][1]), rw, kbase + ((hb * 2 + (rt >> 1)) * 32 + (rt & 1)) * 14336);
    } else {
#pragma unroll
      for (int q = 0; q < 2; ++q)
#pragma unroll
        for (int sg = 0; sg < 2; ++sg) st4_sc1(pk8(acc[2 * sg][q], acc[2 * sg + 1][q]), rw, vbase + ((hb * 2 + sg) * 448 + q) * 1024);
    }
    if (it == 0 || it == 15) DRAIN();
    BAR();
    if (w == 0) TL(200 + it);
    if (w == (it & 7)) {
      if (it == 0 || it == 15) { if (lane == 0) arrive(ws, rw, CTR_KV + hb * 128, 56u); }
      if (it >= 2) {
        if (it == 15) { if (lane == 0) arrive(ws, rw, CTR_KV + (hb - 2) * 128, 56u); }
        else { pend = CTR_KV + (hb - 2) * 128; if (lane == 0) aold = arrive_issue(ws, pend); }
      }
    }
  }
}

DI void gate_wait(rsrc_t rw, int rt, int nap, int lag, int own) {
  int off = (int)OFF_PM + rt * 4096 + ((blockIdx.x >> 3) & 31) * 128;
  if (__builtin_amdgcn_readfirstlane(ld1_sc1(rw, off)) != SENT) {
    off = own; lag = 4; nap = 8;
    if (__builtin_amdgcn_readfirstlane(ld1_sc1(rw, off)) != SENT) return;
  }
#pragma nounroll
  for (int spin = 0; spin < SPIN_MAX; ++spin) {
    sleep_units(nap);
    asm volatile("" ::: "memory");
    if (__builtin_amdgcn_readfirstlane(ld1_sc1(rw, off)) != SENT) break;
  }
  sleep_units(lag);
}
#define LAG_A2 10
#define LAG_F1 65
#define LAG_F2 140
#define RETRY 6
#define NAPT 4
#define NAP 6

#define TLX(k) do { if (rt == 127 || rt == 96) TL((rt == 127 ? 130 : 140) + (k)); } while (0)
DI void stage_a1(const Params& p, rsrc_t rw, int h) {
  unsigned char* ws = p.ws;
  const int tid = threadIdx.x, lane = tid & 63, w = __builtin_amdgcn_readfirstlane(tid >> 6);
  const int kg = lane >> 4, n = lane & 15, v = w & 1;
  const float sc = 0.0625f * 1.4426950408889634f;
  const float NEG = -3.0e38f;
  int known = 0;
  ctr_wait(rw, CTR_FX, 1u, NAP);
#pragma nounroll
  for (int rt = w; rt < 128; rt += 8) {
    const int s = rt >> 1;
    v4u xf[KS], kf[KS], k1[KS];
    kv_ensure(rw, s >> 1, known, NAP);
    const int kb = (int)OFF_KP + ((s * 16 + h) * 2) * 14336 + lane * 16;
#pragma unroll
    for (int ks = 0; ks < KS; ++ks) {
      xf[ks] = *(const v4u*)(ws + OFF_XN + ((size_t)rt * 14 + ks) * 1024 + lane * 16);
      kf[ks] = ld_sc1(rw, kb + ks * 1024);
    }
#pragma unroll
    for (int ks = 0; ks < KS; ++ks) k1[ks] = ld_sc1(rw, v ? kb + 14336 + ks * 1024 : OOB);
    __builtin_amdgcn_sched_barrier(0);
    f32x4 a0 = {0, 0, 0, 0}, a1 = {0, 0, 0, 0}, am = {0, 0, 0, 0};
#pragma unroll
    for (int ks = 0; ks < KS; ++ks) a0 = mfma32(kf[ks], xf[ks], a0);
#pragma unroll
    for (int ks = 0; ks < KS; ++ks) a1 = mfma32(k1[ks], xf[ks], a1);
    float xs[8];
#pragma unroll
    for (int r = 0; r < 4; ++r) {
      const bool diag = 4 * kg + r <= n;
      xs[r] = (v || diag) ? rbf(a0[r]) : NEG;
      xs[4 + r] = (v && diag) ? rbf(a1[r]) : NEG;
    }
    float mx = NEG;
#pragma unroll
    for (int j = 0; j < 8; ++j) mx = fmaxf(mx, xs[j]);
    const int tb = (int)OFF_KM + (s * 16 + h) * 7168 + kg * 128 + (n & 7) * 16;
    TLX(3);
    ctr_wait(rw, CTR_M + (s < NSEG - 2 ? s : s - 2) * 128, 84u, NAP);
    TLX(0);
#pragma nounroll
    for (int spin = 0; spin < SPIN_MAX; ++spin) {
      asm volatile("" ::: "memory");
      TLX(1);
      unsigned mxu = 0;
#pragma unroll
      for (int ks = 0; ks < KS; ++ks) kf[ks] = ld_sc1(rw, tb + ks * 512);
      if (s < NSEG - 2) break;
#pragma unroll
      for (int ks = 0; ks < KS; ++ks) mxu = max(mxu, umax4(kf[ks]));
      if (__all(mxu != SENT)) break;
      sleep_units(3);
    }
#pragma unroll
    for (int ks = 0; ks < KS; ++ks) am = mfma32(kf[ks], xf[ks], am);
    float mm[4];
#pragma unroll
    for (int r = 0; r < 4; ++r) { mm[r] = rbf(am[r]); mx = fmaxf(mx, mm[r]); }
    mx = xmax32(xmax16(mx));
    const float nb = -mx * sc;
    float sum = 0.f;
#pragma unroll
    for (int j = 0; j < 8; ++j) { xs[j] = xs[j] == NEG ? 0.f : __builtin_amdgcn_exp2f(__builtin_fmaf(xs[j], sc, nb)); sum += xs[j]; }
#pragma unroll
    for (int r = 0; r < 4; ++r) { mm[r] = __builtin_amdgcn_exp2f(__builtin_fmaf(mm[r], sc, nb)); if (kg < 2) sum += mm[r]; }
    sum = xsum32(xsum16(sum));
    const float inv = 1.f / sum;
    st4_sc1(nosent4(v4u{pk2(xs[0] * inv, xs[1] * inv), pk2(xs[2] * inv, xs[3] * inv), pk2(xs[4] * inv, xs[5] * inv), pk2(xs[6] * inv, xs[7] * inv)}),
            rw, (int)OFF_PX + (rt * 16 + h) * 1024 + lane * 16);
    st2_sc1(nosent2(v2u{pk2(mm[0] * inv, mm[1] * inv), pk2(mm[2] * inv, mm[3] * inv)}), rw,
            kg < 2 ? (int)OFF_PM + (rt * 4 + (h >> 2)) * 1024 + ((h & 3) * 16 + n) * 16 + kg * 8 : OOB);
    TL(rt);
  }
}

DI void stage_a2(const Params& p, rsrc_t rw, int ct) {
  const int tid = threadIdx.x, lane = tid & 63, w = __builtin_amdgcn_readfirstlane(tid >> 6);
  const int kg = lane >> 4, n = lane & 15;
  const int c = 2 * ct + (kg >> 1);
  const int col = 32 * (c >> 2) + 16 * (kg & 1) + 4 * (c & 3);
  const int hoff = (c >> 2) * 1024 + ((c & 3) * 16 + n) * 16 + (kg & 1) * 8;
  int known = 0;
  ctr_wait(rw, CTR_FX, 1u, NAP);
#pragma nounroll
  for (int rt = w; rt < 128; rt += 8) {
    const int s = rt >> 1;
    const v2u xv = *(const v2u*)(p.x + (size_t)(rt * 16 + n) * 448 + col);
    kv_ensure(rw, s >> 1, known, NAP);
    v4u vf[16], yf[4];
#pragma unroll
    for (int h = 0; h < 16; ++h) vf[h] = ld_sc1(rw, (int)OFF_VP + (s * 448 + h * 28 + ct) * 1024 + lane * 16);
    TLX(3);
    ctr_wait(rw, CTR_M + (s < NSEG - 2 ? s : s - 2) * 128, 84u, NAP);
    const int pb = (int)OFF_PX + rt * 16384 + lane * 16, mb = (int)OFF_PM + rt * 4096 + lane * 16;
    gate_wait(rw, rt, NAPT, LAG_A2, (int)OFF_PX + (rt * 16 + (ct & 15)) * 1024);
    TLX(0);
    f32x4 acc;
#pragma nounroll
    for (int spin = 0; spin < SPIN_MAX; ++spin) {
      asm volatile("" ::: "memory");
      TLX(1);
      v4u px[16], pm[4];
#pragma unroll
      for (int q = 0; q < 4; ++q) yf[q] = ld_sc1(rw, (int)OFF_VM + ((s * 16 + 4 * q + kg) * 28 + ct) * 256 + n * 16);
#pragma unroll
      for (int h = 0; h < 16; ++h) px[h] = ld_sc1(rw, pb + h * 1024);
#pragma unroll
      for (int q = 0; q < 4; ++q) pm[q] = ld_sc1(rw, mb + q * 1024);
      unsigned mxu = 0;
      f32x4 a0 = {0, 0, 0, 0}, a1 = {0, 0, 0, 0};
#pragma unroll
      for (int h = 0; h < 16; h += 2) {
        mxu = umax3(mxu, umax4(px[h]), umax4(px[h + 1]));
        a0 = mfma32(vf[h], px[h], a0); a1 = mfma32(vf[h + 1], px[h + 1], a1);
      }
#pragma unroll
      for (int q = 0; q < 4; q += 2) {
        mxu = umax3(mxu, umax3(umax4(pm[q]), umax4(pm[q + 1]), umax4(yf[q])), umax4(yf[q + 1]));
        a0 = mfma32(yf[q], pm[q], a0); a1 = mfma32(yf[q + 1], pm[q + 1], a1);
      }
      acc = a0 + a1;
      if (__all(mxu != SENT)) break;
      sleep_units(RETRY);
    }
    const float h0 = bflo(xv.x) + rbf(acc.x), h1 = bfhi(xv.x) + rbf(acc.y), h2 = bflo(xv.y) + rbf(acc.z), h3 = bfhi(xv.y) + rbf(acc.w);
    st2_sc1(nosent2(v2u{pk2(h0, h1), pk2(h2, h3)}), rw, (int)OFF_HF + rt * 14336 + hoff);
    TL(rt);
  }
}

DI void stage_f1(const Params& p, unsigned char* lds, rsrc_t rw, int kf) {
  const int tid = threadIdx.x, lane = tid & 63, w = __builtin_amdgcn_readfirstlane(tid >> 6);
  const int kg = lane >> 4, n = lane & 15;
  for (int f = w; f < 56; f += 8) {
    const int t = f / 14, ks = f % 14;
    const u16* src = ((t >> 1) ? p.wu : p.wg) + 32 * kf + 16 * (t & 1) + n;
    unsigned e[8];
#pragma unroll
    for (int jj = 0; jj < 8; ++jj) e[jj] = src[(size_t)pidx(ks, kg, jj) * 1792];
    *(v4u*)(lds + f * 1024 + lane * 16) = v4u{e[0] | (e[1] << 16), e[2] | (e[3] << 16), e[4] | (e[5] << 16), e[6] | (e[7] << 16)};
  }
  BAR();
  const v4u ones = {0x3F803F80u, 0x3F803F80u, 0x3F803F80u, 0x3F803F80u};
  f32x4 cs[4];
#pragma unroll
  for (int t = 0; t < 4; ++t) {
    cs[t] = f32x4{0, 0, 0, 0};
#pragma unroll
    for (int ks = 0; ks < KS; ++ks) cs[t] = mfma32(*(const v4u*)(lds + (t * 14 + ks) * 1024 + lane * 16), ones, cs[t]);
  }
#pragma nounroll
  for (int rt = w; rt < 128; rt += 8) {
    const int hb = (int)OFF_HF + rt * 14336 + lane * 16;
    gate_wait(rw, rt, NAPT, LAG_F1, (int)OFF_HF + rt * 14336 + (kf >> 1) * 512);
    TLX(0);
    v4u hf[KS];
#pragma nounroll
    for (int spin = 0; spin < SPIN_MAX; ++spin) {
      asm volatile("" ::: "memory");
      TLX(1);
      unsigned mxu = 0;
#pragma unroll
      for (int ks = 0; ks < KS; ++ks) hf[ks] = ld_sc1(rw, hb + ks * 1024);
#pragma unroll
      for (int ks = 0; ks < KS; ++ks) mxu = max(mxu, umax4(hf[ks]));
      if (__all(mxu != SENT)) break;
      sleep_units(RETRY);
    }
    TLX(2);
    f32x4 acc[4], sm = {0, 0, 0, 0}, gr = {0, 0, 0, 0};
#pragma unroll
    for (int t = 0; t < 4; ++t) acc[t] = f32x4{0, 0, 0, 0};
    v4u wf[2][4];
#pragma unroll
    for (int t = 0; t < 4; ++t) wf[0][t] = *(const v4u*)(lds + (t * 14) * 1024 + lane * 16);
#pragma unroll
    for (int ks = 0; ks < KS; ++ks) {
      if (ks + 1 < KS) {
#pragma unroll
        for (int t = 0; t < 4; ++t) wf[(ks + 1) & 1][t] = *(const v4u*)(lds + (t * 14 + ks + 1) * 1024 + lane * 16);
      }
      sm = mfma32(ones, hf[ks], sm);
      gr = mfma32(hf[ks], hf[ks], gr);
      __builtin_amdgcn_sched_barrier(0);
#pragma unroll
      for (int t = 0; t < 4; ++t) acc[t] = mfma32(wf[ks & 1][t], hf[ks], acc[t]);
      __builtin_amdgcn_sched_barrier(0);
    }
    float dg = (kg == (n >> 2)) ? ((n & 2) ? ((n & 1) ? gr.w : gr.z) : ((n & 1) ? gr.y : gr.x)) : 0.f;
    dg = xsum32(xsum16(dg));
    const float mean = sm.x * (1.f / 448.f);
    const float var = fmaxf(dg * (1.f / 448.f) - mean * mean, 0.f);
    const float mb = rbf(mean), rden = 1.f / ln_den(var);
    f32x4 o0, o1;
#pragma unroll
    for (int r = 0; r < 4; ++r) {
      o0[r] = rbf((acc[0][r] - mb * cs[0][r]) * rden) * rbf((acc[2][r] - mb * cs[2][r]) * rden);
      o1[r] = rbf((acc[1][r] - mb * cs[1][r]) * rden) * rbf((acc[3][r] - mb * cs[3][r]) * rden);
    }
    st4_sc1(nosent4(pk8(o0, o1)), rw, (int)OFF_AF + (rt * 56 + kf) * 1024 + lane * 16);
    TL(rt);
  }
}

DI void stage_f2(const Params& p, unsigned char* lds, rsrc_t rw, int ct) {
  const int tid = threadIdx.x, lane = tid & 63, w = __builtin_amdgcn_readfirstlane(tid >> 6);
  const int kg = lane >> 4, n = lane & 15;
  for (int kf = w; kf < 56; kf += 8) {
    const u16* src = p.wd + 16 * ct + n;
    unsigned e[8];
#pragma unroll
    for (int jj = 0; jj < 8; ++jj) e[jj] = src[(size_t)pidx(kf, kg, jj) * 448];
    *(v4u*)(lds + kf * 1024 + lane * 16) = v4u{e[0] | (e[1] << 16), e[2] | (e[3] << 16), e[4] | (e[5] << 16), e[6] | (e[7] << 16)};
  }
  BAR();
#pragma nounroll
  for (int rt = w; rt < 128; rt += 8) {
    const int ab = (int)OFF_AF + rt * 57344 + lane * 16;
    gate_wait(rw, rt, NAPT, LAG_F2, (int)OFF_AF + rt * 57344 + (2 * ct) * 1024);
    TLX(0);
    v2u hv = v2u{0, 0};
    f32x4 a0, a1;
#pragma nounroll
    for (int spin = 0; spin < SPIN_MAX; ++spin) {
      asm volatile("" ::: "memory");
      TLX(1);
      a0 = f32x4{0, 0, 0, 0}; a1 = f32x4{0, 0, 0, 0};
      hv = __builtin_amdgcn_raw_buffer_load_b64(rw, (int)OFF_HF + rt * 14336 + (ct >> 1) * 1024 + lane * 16 + (ct & 1) * 8, 0, AUX_SC1);
      v4u af[3][14];
#pragma unroll
      for (int bb = 0; bb < 2; ++bb)
#pragma unroll
        for (int k = 0; k < 14; ++k) af[bb][k] = ld_sc1(rw, ab + (bb * 14 + k) * 1024);
      unsigned mxu = max(hv.x, hv.y);
#pragma unroll
      for (int bb = 0; bb < 4; ++bb) {
        if (bb + 2 < 4) {
#pragma unroll
          for (int k = 0; k < 14; ++k) af[(bb + 2) % 3][k] = ld_sc1(rw, ab + ((bb + 2) * 14 + k) * 1024);
        }
        __builtin_amdgcn_sched_barrier(0);
#pragma unroll
        for (int k = 0; k < 14; k += 2) {
          mxu = umax3(mxu, umax4(af[bb % 3][k]), umax4(af[bb % 3][k + 1]));
          a0 = mfma32(*(const v4u*)(lds + (bb * 14 + k) * 1024 + lane * 16), af[bb % 3][k], a0);
          a1 = mfma32(*(const v4u*)(lds + (bb * 14 + k + 1) * 1024 + lane * 16), af[bb % 3][k + 1], a1);
        }
        __builtin_amdgcn_sched_barrier(0);
      }
      if (__all(mxu != SENT)) break;
      sleep_units(RETRY);
    }
    const f32x4 d = a0 + a1;
    const float o0 = bflo(hv.x) + rbf(d.x), o1 = bfhi(hv.x) + rbf(d.y), o2 = bflo(hv.y) + rbf(d.z), o3 = bfhi(hv.y) + rbf(d.w);
    *(v2u*)(p.out + (size_t)(rt * 16 + n) * 448 + 16 * ct + 4 * kg) = v2u{pk2(o0, o1), pk2(o2, o3)};
    TL(rt);
  }
}

DI void arm_stage(const Params& p, rsrc_t rw, int k, int nwg, int off0, int nkb, int ctr) {
  const int tid = threadIdx.x, lane = tid & 63, w = __builtin_amdgcn_readfirstlane(tid >> 6);
  const v4u sent = {SENT, SENT, SENT, SENT};
  for (int i = k * 8 + w; i < nkb; i += nwg * 8) st4_sc1(sent, rw, off0 + i * 1024 + lane * 16);
  DRAIN();
  __syncthreads();
  if (tid == 0) arrive(p.ws, rw, ctr, (unsigned)nwg);
}

constexpr int MAIN_LDS = 114688;
__global__ void __launch_bounds__(512, 2) k_main(Params p) {
  extern __shared__ __attribute__((aligned(16))) unsigned char smem[];
  const rsrc_t rw = __builtin_amdgcn_make_buffer_rsrc((void*)p.ws, 0, (int)WS_END, 0x00020000);
  const int b = blockIdx.x;
  if (threadIdx.x < 64) TL(255);
  const int j = (b >> 3) * 4 + (b & 3);
  if ((b & 7) < 4) {
    if (j < N_CHAIN) chain_role(p, smem, rw, j);
    else { if (j == N_CHAIN) probe_place(p, rw); sleep_units(400); arm_stage(p, rw, j - N_CHAIN, 8, (int)OFF_HF, (int)((OFF_XN - OFF_HF) / 1024), CTR_FS + 128); }
    return;
  }
  if (j < 112) phase_kv(p, smem, rw, j % 56, j / 56);
  else { sleep_units(D_ARM); }
  if (j >= 112) arm_stage(p, rw, j - 112, 16, (int)OFF_PX, (int)((OFF_HF - OFF_PX) / 1024), CTR_FS);
  ctr_wait(rw, CTR_FS, 16u, 8);
  if (j < 112) ctr_wait(rw, CTR_FS + 128, 8u, 8);
  if (j < 56) stage_f1(p, smem, rw, j);
  else if (j < 84) stage_f2(p, smem, rw, j - 56);
  else if (j < 112) stage_a2(p, rw, j - 84);
  else stage_a1(p, rw, j - 112);
}

extern "C" void kernel_launch(void* const* d_in, const int* in_sizes, int n_in, void* d_out, int out_size, void* d_ws, size_t ws_size,
                              hipStream_t stream) {
  Params p;
  p.x = (const u16*)d_in[0]; p.mem0 = (const u16*)d_in[1]; p.Wq = (const u16*)d_in[2]; p.Wk = (const u16*)d_in[3];
  p.Wv = (const u16*)d_in[4]; p.Wo = (const u16*)d_in[5];
  p.wg = (const u16*)d_in[8]; p.wu = (const u16*)d_in[9]; p.wd = (const u16*)d_in[10];
  p.out = (u16*)d_out; p.ws = (unsigned char*)d_ws;
  (void)hipFuncSetAttribute((const void*)k_main, hipFuncAttributeMaxDynamicSharedMemorySize, MAIN_LDS);
  (void)hipFuncSetAttribute((const void*)k_prep, hipFuncAttributeMaxDynamicSharedMemorySize, PREP_LDS);
  k_prep<<<256, 512, PREP_LDS, stream>>>(p);
  k_main<<<256, 512, MAIN_LDS, stream>>>(p);
}
```

```cpp
#include <hip/hip_runtime.h>
#include <hip/hip_bf16.h>
#include <stdint.h>

typedef short bf16x8 __attribute__((ext_vector_type(8)));
typedef float f32x4 __attribute__((ext_vector_type(4)));
typedef float f32x2 __attribute__((ext_vector_type(2)));
typedef __bf16 bf2_t __attribute__((ext_vector_type(2)));
typedef unsigned v4u __attribute__((ext_vector_type(4)));
typedef unsigned v2u __attribute__((ext_vector_type(2)));
typedef unsigned short u16;
typedef __amdgpu_buffer_rsrc_t rsrc_t;
#define DI __device__ __forceinline__

constexpr int NSEG = 64, KS = 14;
constexpr unsigned SENT = 0xFFFFFFFFu;
constexpr int AUX_SC1 = 16;
constexpr int SPIN_MAX = 20000;
constexpr int OOB = 0x7FFFFFF0;

constexpr size_t OFF_CTR = 0;
constexpr int CTR_KV = 0, CTR_M = 4096, OFF_PTAB = 16384, CTR_FX = 16640, CTR_FS = 16896;
constexpr size_t OFF_EX = 65536;
constexpr size_t EX_STEP = 5 * 16384;
constexpr size_t OFF_PX = OFF_EX + 64 * EX_STEP;
constexpr size_t OFF_PM = OFF_PX + (size_t)128 * 16 * 1024;
constexpr size_t OFF_HF = OFF_PM + (size_t)128 * 4 * 1024;
constexpr size_t OFF_AF = OFF_HF + (size_t)128 * 14 * 1024;
constexpr size_t OFF_XN = OFF_AF + (size_t)128 * 56 * 1024;
constexpr size_t OFF_WC = OFF_XN + (size_t)128 * 14 * 1024;
constexpr size_t OFF_KP = OFF_WC + (size_t)896 * 14 * 1024;
constexpr size_t OFF_VP = OFF_KP + (size_t)64 * 16 * 2 * 14 * 1024;
constexpr size_t OFF_KM = OFF_VP + (size_t)64 * 448 * 1024;
constexpr size_t OFF_VM = OFF_KM + (size_t)64 * 16 * 14 * 512;
constexpr size_t WS_END = OFF_VM + (size_t)64 * 16 * 28 * 256;
constexpr size_t FILL_BYTES = OFF_XN - OFF_EX;
static_assert(WS_END < (size_t)200 * 1024 * 1024, "ws");
static_assert(FILL_BYTES % 16384 == 0, "fill");

DI f32x4 mfma32(v4u a, v4u b, f32x4 c) {
  return __builtin_amdgcn_mfma_f32_16x16x32_bf16(__builtin_bit_cast(bf16x8, a), __builtin_bit_cast(bf16x8, b), c, 0, 0, 0);
}
DI unsigned pk2(float a, float b) {
  f32x2 v = {a, b};
  return __builtin_bit_cast(unsigned, __builtin_convertvector(v, bf2_t));
}
DI v2u pk4(f32x4 a) { return v2u{pk2(a.x, a.y), pk2(a.z, a.w)}; }
DI v4u pk8(f32x4 a, f32x4 b) { return v4u{pk2(a.x, a.y), pk2(a.z, a.w), pk2(b.x, b.y), pk2(b.z, b.w)}; }
DI float bflo(unsigned u) { return __uint_as_float(u << 16); }
DI float bfhi(unsigned u) { return __uint_as_float(u & 0xFFFF0000u); }
DI float rbf(float a) { return bflo(pk2(a, 0.f) & 0xFFFFu); }
DI int picol(int c, int cc) { return 32 * (c >> 2) + 16 * (cc >> 2) + 4 * (c & 3) + (cc & 3); }
DI int pidx(int ks, int kg, int jj) { return 32 * ks + 16 * (jj >> 2) + 4 * kg + (jj & 3); }
DI float xsum16(float v) { auto r = __builtin_amdgcn_permlane16_swap(__float_as_uint(v), __float_as_uint(v), false, false);
  const unsigned a = r[0], b = r[1]; return __uint_as_float(a) + __uint_as_float(b); }
DI float xsum32(float v) { auto r = __builtin_amdgcn_permlane32_swap(__float_as_uint(v), __float_as_uint(v), false, false);
  const unsigned a = r[0], b = r[1]; return __uint_as_float(a) + __uint_as_float(b); }
DI float xmax16(float v) { auto r = __builtin_amdgcn_permlane16_swap(__float_as_uint(v), __float_as_uint(v), false, false);
  const unsigned a = r[0], b = r[1]; return fmaxf(__uint_as_float(a), __uint_as_float(b)); }
DI float xmax32(float v) { auto r = __builtin_amdgcn_permlane32_swap(__float_as_uint(v), __float_as_uint(v), false, false);
  const unsigned a = r[0], b = r[1]; return fmaxf(__uint_as_float(a), __uint_as_float(b)); }
DI unsigned nosent(unsigned v) { return min(v, 0xFFFFFFFEu); }
DI v2u nosent2(v2u o) { return v2u{nosent(o.x), nosent(o.y)}; }
DI v4u nosent4(v4u o) { return v4u{nosent(o.x), nosent(o.y), nosent(o.z), nosent(o.w)}; }
DI unsigned umax3(unsigned a, unsigned b, unsigned c) { return max(max(a, b), c); }
DI unsigned umax4(v4u r) { return max(umax3(r.x, r.y, r.z), r.w); }
#define KEEP4(x) asm volatile("" :: "v"(x))
#define BAR() do { asm volatile("s_waitcnt lgkmcnt(0)" ::: "memory"); __builtin_amdgcn_s_barrier(); asm volatile("" ::: "memory"); } while (0)
#define DRAIN() asm volatile("s_waitcnt vmcnt(0)" ::: "memory")
DI void sleep_units(int n) { for (int d = 0; d < n; ++d) __builtin_amdgcn_s_sleep(1); }
DI v4u ld_sc1(rsrc_t r, int off) { return __builtin_amdgcn_raw_buffer_load_b128(r, off, 0, AUX_SC1); }
DI unsigned ld1_sc1(rsrc_t r, int off) { return __builtin_amdgcn_raw_buffer_load_b32(r, off, 0, AUX_SC1); }
DI void st4_sc1(v4u v, rsrc_t r, int off) { __builtin_amdgcn_raw_buffer_store_b128(v, r, off, 0, AUX_SC1); }
DI void st2_sc1(v2u v, rsrc_t r, int off) { __builtin_amdgcn_raw_buffer_store_b64(v, r, off, 0, AUX_SC1); }
DI void st1_sc1(unsigned v, rsrc_t r, int off) { __builtin_amdgcn_raw_buffer_store_b32(v, r, off, 0, AUX_SC1); }
constexpr int FLAG_D = 32768;
DI unsigned arrive_issue(unsigned char* ws, int off) { return __hip_atomic_fetch_add((unsigned*)(ws + off), 1u, __ATOMIC_RELAXED, __HIP_MEMORY_SCOPE_AGENT); }
DI void arrive_finish(rsrc_t r, unsigned old, int off, unsigned total) { if (old == total - 1u) st1_sc1(1u, r, off + FLAG_D); }
DI void arrive(unsigned char* ws, rsrc_t r, int off, unsigned total) { arrive_finish(r, arrive_issue(ws, off), off, total); }
DI void ctr_wait(rsrc_t r, int off, unsigned want, int nap) {
  off += FLAG_D; want = 1u;
#pragma nounroll
  for (int spin = 0; spin < SPIN_MAX; ++spin) {
    asm volatile("" ::: "memory");
    const unsigned v = __builtin_amdgcn_readfirstlane(ld1_sc1(r, off));
    if (v >= want) break;
    sleep_units(nap);
  }
  asm volatile("" ::: "memory");
}

DI void kv_ensure(rsrc_t r, int hb, int& known, int nap) {
  if (hb < known) return;
  const int lane = threadIdx.x & 63;
#pragma nounroll
  for (int spin = 0; spin < SPIN_MAX; ++spin) {
    asm volatile("" ::: "memory");
    const unsigned v = ld1_sc1(r, lane < 32 ? CTR_KV + FLAG_D + lane * 128 : OOB);
    const unsigned long long m = __ballot(v != 0u);
    known = __builtin_ctzll(~m);
    if (hb < known) break;
    sleep_units(nap);
  }
  asm volatile("" ::: "memory");
}

struct Params {
  const u16 *x, *mem0, *Wq, *Wk, *Wv, *Wo, *wg, *wu, *wd;
  u16* out;
  unsigned char* ws;
};

DI float ln_den(float var) { return rbf(sqrtf(rbf(rbf(var) + 1e-5f))); }

#define TL(idx)
constexpr int PREP_LDS = 288 * 528;
constexpr int FILL_KB = (int)(FILL_BYTES / 1024);
__global__ void __launch_bounds__(512, 2) k_prep(Params p) {
  extern __shared__ __attribute__((aligned(16))) unsigned char lds[];
  const int tid = threadIdx.x, lane = tid & 63, kg = lane >> 4, n = lane & 15;
  const int w = __builtin_amdgcn_readfirstlane(tid >> 6);
  unsigned char* ws = p.ws;
#define TLP(idx)
  const int xg = blockIdx.x & 7, ix = blockIdx.x >> 3;
  const int b = ix < 28 ? (2 * xg + ix / 14) * 14 + ix % 14 : 224 + xg * 4 + (ix - 28), bb = b;
  TLP(0);
  const v4u sent = {SENT, SENT, SENT, SENT};
  if (bb < 224) {
    const int h = bb / 14, r = bb % 14, ab = r / 7, jb = r % 7;
    v4u sa[2][14], sb[2][4];
#pragma unroll
    for (int half = 0; half < 2; ++half) {
      const u16* asrc = (half ? p.Wk : p.Wv) + (size_t)(224 * ab) * 4096 + h * 256;
      if (half) {
        const u16* bsrc = p.Wq + (size_t)(64 * jb) * 4096 + h * 256;
#pragma unroll
        for (int i = 0; i < 4; ++i) { const int pc = i * 512 + tid; sb[1][i] = *(const v4u*)(bsrc + (size_t)(pc >> 5) * 4096 + (pc & 31) * 8); }
      } else {
        const u16* bsrc = p.Wo + (size_t)(h * 256) * 448 + 64 * jb;
#pragma unroll
        for (int i = 0; i < 4; ++i) { const int pc = i * 512 + tid; sb[0][i] = *(const v4u*)(bsrc + (size_t)(pc >> 3) * 448 + (pc & 7) * 8); }
      }
#pragma unroll
      for (int i = 0; i < 14; ++i) { const int pc = i * 512 + tid; sa[half][i] = *(const v4u*)(asrc + (size_t)(pc >> 5) * 4096 + (pc & 31) * 8); }
    }
    __builtin_amdgcn_sched_barrier(0);
    unsigned char* lb = lds + 224 * 528;
#pragma unroll
    for (int half = 0; half < 2; ++half) {
      if (half) {
        __syncthreads();
#pragma unroll
        for (int i = 0; i < 4; ++i) { const int pc = i * 512 + tid; *(v4u*)(lb + (pc >> 5) * 528 + (pc & 31) * 16) = sb[1][i]; }
      } else {
#pragma unroll
        for (int i = 0; i < 4; ++i) {
          const int pc = i * 512 + tid, e = pc >> 3, x0 = (pc & 7) * 8;
#pragma unroll
          for (int j = 0; j < 8; ++j) {
            const int x = x0 + j, m = x >> 5, xl = x & 31, cl = (xl >> 2) & 3;
            const int slot = (2 * m + (cl >> 1)) * 16 + (cl & 1) * 8 + (xl >> 4) * 4 + (xl & 3);
            const unsigned wv = sb[0][i][j >> 1];
            *(u16*)(lb + slot * 528 + e * 2) = (u16)((j & 1) ? (wv >> 16) : (wv & 0xFFFFu));
          }
        }
      }
#pragma unroll
      for (int i = 0; i < 14; ++i) { const int pc = i * 512 + tid; *(v4u*)(lds + (pc >> 5) * 528 + (pc & 31) * 16) = sa[half][i]; }
      __syncthreads();
      TLP(1 + 2 * half);
      if (w < 7) {
        const int ks = ab * 7 + w;
        const unsigned char* pa = lds + (32 * w + n) * 528 + kg * 16;
        const unsigned char* pb = lb + n * 528 + kg * 16;
        f32x4 acc[4][2];
#pragma unroll
        for (int t = 0; t < 4; ++t) { acc[t][0] = f32x4{0, 0, 0, 0}; acc[t][1] = f32x4{0, 0, 0, 0}; }
#pragma unroll
        for (int ke = 0; ke < 8; ++ke) {
          const v4u A0 = *(const v4u*)(pa + ke * 64), A1 = *(const v4u*)(pa + 16 * 528 + ke * 64);
#pragma unroll
          for (int t = 0; t < 4; ++t) {
            const v4u B = *(const v4u*)(pb + t * 16 * 528 + ke * 64);
            acc[t][0] = mfma32(A0, B, acc[t][0]); acc[t][1] = mfma32(A1, B, acc[t][1]);
          }
        }
#pragma unroll
        for (int t = 0; t < 4; ++t) {
          const int tile = (half ? 0 : 448) + h * 28 + jb * 4 + t;
          *(v4u*)(ws + OFF_WC + ((size_t)tile * 14 + ks) * 1024 + lane * 16) = pk8(acc[t][0], acc[t][1]);
        }
      }
      TLP(2 + 2 * half);
    }
    return;
  }
  {
    const int c = lane < 56 ? lane : 0;
    const int ks = c >> 2, kc = c & 3;
    v2u a4[8], b4[8];
#pragma unroll
    for (int rr = 0; rr < 8; ++rr) {
      const int row = (bb - 224) * 64 + w * 8 + rr;
      a4[rr] = *(const v2u*)(p.x + (size_t)row * 448 + 32 * ks + 4 * kc);
      b4[rr] = *(const v2u*)(p.x + (size_t)row * 448 + 32 * ks + 16 + 4 * kc);
    }
    v2u ma = v2u{0, 0}, mb2 = v2u{0, 0};
    const int pc = w * 64 + lane;
    if (b == 224 && w < 7) {
      const int cc = (pc >> 4) * 2 + ((pc >> 2) & 1), row = ((pc >> 3) & 1) * 4 + (pc & 3);
      ma = *(const v2u*)(p.mem0 + (size_t)row * 448 + 32 * (cc >> 2) + 4 * (cc & 3));
      mb2 = *(const v2u*)(p.mem0 + (size_t)row * 448 + 32 * (cc >> 2) + 16 + 4 * (cc & 3));
    }
    if (b == 225) {
#pragma unroll
      for (int i = 0; i < 8; ++i) *(v4u*)(ws + OFF_CTR + (size_t)(i * 512 + tid) * 16) = v4u{0, 0, 0, 0};
    }
    __builtin_amdgcn_sched_barrier(0);
    float v[8][8], s1[8], q1[8];
#pragma unroll
    for (int rr = 0; rr < 8; ++rr) {
      const v2u a = a4[rr], b2 = b4[rr];
      v[rr][0] = bflo(a.x); v[rr][1] = bfhi(a.x); v[rr][2] = bflo(a.y); v[rr][3] = bfhi(a.y);
      v[rr][4] = bflo(b2.x); v[rr][5] = bfhi(b2.x); v[rr][6] = bflo(b2.y); v[rr][7] = bfhi(b2.y);
      float s = 0.f;
#pragma unroll
      for (int j = 0; j < 8; ++j) s += v[rr][j];
      s1[rr] = lane < 56 ? s : 0.f;
    }
#pragma unroll
    for (int o = 1; o < 64; o <<= 1)
#pragma unroll
      for (int rr = 0; rr < 8; ++rr) s1[rr] += __shfl_xor(s1[rr], o);
#pragma unroll
    for (int rr = 0; rr < 8; ++rr) {
      s1[rr] *= (1.f / 448.f);
      float q = 0.f;
#pragma unroll
      for (int j = 0; j < 8; ++j) { const float d = v[rr][j] - s1[rr]; q += d * d; }
      q1[rr] = lane < 56 ? q : 0.f;
    }
#pragma unroll
    for (int o = 1; o < 64; o <<= 1)
#pragma unroll
      for (int rr = 0; rr < 8; ++rr) q1[rr] += __shfl_xor(q1[rr], o);
#pragma unroll
    for (int rr = 0; rr < 8; ++rr) {
      const int row = (bb - 224) * 64 + w * 8 + rr;
      const float den = ln_den(q1[rr] * (1.f / 448.f)), mb = rbf(s1[rr]);
      float o8[8];
#pragma unroll
      for (int j = 0; j < 8; ++j) o8[j] = rbf(v[rr][j] - mb) / den;
      if (lane < 56)
        *(v4u*)(ws + OFF_XN + ((size_t)(row >> 4) * 14 + ks) * 1024 + (kc * 16 + (row & 15)) * 16) =
            v4u{pk2(o8[0], o8[1]), pk2(o8[2], o8[3]), pk2(o8[4], o8[5]), pk2(o8[6], o8[7])};
    }
    if (b == 224) {
      if (w < 7) {
        const v4u vv = v4u{nosent(ma.x), nosent(ma.y), nosent(mb2.x), nosent(mb2.y)};
        *(v4u*)(ws + OFF_EX + pc * 16) = vv;
        *(v4u*)(ws + OFF_EX + 8192 + pc * 16) = vv;
      } else {
        *(v4u*)(ws + OFF_EX + 7168 + lane * 16) = sent;
        *(v4u*)(ws + OFF_EX + 8192 + 7168 + lane * 16) = sent;
      }
    }
  }
  TLP(4);
}
#define STAMP(k)
#define STAMPV(k, v)
#define STAMP_INIT()
#define STAMP_DUMP()
constexpr int L_MEM = 0;
constexpr int L_P1 = 16384;
constexpr int L_YB = 24576;
constexpr int N_S = 56, N_O = 56, N_X = 8, N_CHAIN = N_S + N_O + N_X;
constexpr int L_TAB2 = 22528;
constexpr int L_VOTE = 23808;
#define L_DUMMY (14336 + lane * 16)
#define ex_off(s, c) __builtin_amdgcn_readfirstlane(*(const int*)(smem + L_TAB2 + ((s) * 5 + (c)) * 4))
#define T_A 106
#define T_B 100
#define T_SW 26
#define T_DA 0
#define T_DB 0
#define T_R2 8
#define T_R1 0
#define T_Q1 15
#define T_Q2 70
#define PFD 4
#define T_EX 30
#define T_EXN 4
#define T_UP2 2
#define D_ARM 200
#define D_O 80
#define D_S 0
#define T_MIN 0
#define T_TOL 5
#define NREP 4
#define T_OG 0
#define T_PEN 35
#define T_UP 2
#define T_OKN 1000
constexpr int L_SCH = 23872;
DI unsigned rt32() { unsigned long long t; asm volatile("s_memrealtime %0\n\ts_waitcnt lgkmcnt(0)" : "=s"(t) :: "memory"); return (unsigned)t; }
DI unsigned wait_until(unsigned t) {
  int d = (int)(t - rt32());
  if (d < -T_TOL) return t - d;
  if (d > 3000) d = 3000;
  if (d > 24) { sleep_units(((d - 16) * 3) >> 3); d = (int)(t - rt32()); }
  if (d > 0 && d < 64) sleep_units((d * 3) >> 3);
  return t;
}
struct Sched { unsigned tE1, tE2; int m1, m2, ok, nf, late; };
DI void sched_step(Sched& y, unsigned word, int lane, int s) {
  if (s == 0) { y.tE1 = rt32(); y.tE2 = y.tE1; return; }
  int dv = lane < NREP ? (int)((word & ~3u) - (y.tE1 << 2)) >> 2 : -0x40000000;
  unsigned fl = lane < NREP ? (word & 3u) : 0u;
  if (s > 1 && __all(lane >= NREP || word == (y.tE1 << 2))) { dv = 0; fl = 0; }
  else {
#pragma unroll
    for (int o = 1; o < NREP; o <<= 1) { dv = max(dv, __shfl_xor(dv, o)); fl |= (unsigned)__shfl_xor((int)fl, o); }
    dv = __builtin_amdgcn_readfirstlane(dv); fl = __builtin_amdgcn_readfirstlane(fl);
  }
  unsigned base = y.tE2;
  if (dv > 0 || s == 1) base = y.tE1 + dv + T_B + y.m2 + T_PEN;
  if (s > 2) {
    if (!y.late) {
      if (fl & 1u) { y.m2 += T_UP; y.ok = 0; }
      else if (fl & 2u) { y.m1 += T_UP; y.ok = 0; }
      else if (++y.ok >= T_OKN) { y.ok = 0; if (y.m1 > T_MIN) y.m1 -= 1; if (y.m2 > T_MIN) y.m2 -= 1; }
    } else if (dv > 0) { y.m1 += T_UP2; y.m2 += T_UP2; }
  }
  y.late = dv > 0 ? 1 : 0;
  if (s == T_SW) { y.m1 -= T_DA; y.m2 -= T_DB; }
  const int ex = s < T_EXN ? T_EX : 0;
  y.tE1 = base + T_A + y.m1 + ex;
  y.tE2 = y.tE1 + T_B + y.m2 + (ex >> 1);
}
#define FAIL_DUMP(v)

DI int gather_poll(__amdgpu_buffer_rsrc_t rx, unsigned char* dst0, unsigned char* dst1, int o0, int o1, int ot, unsigned& word) {
  int spin = 0;
#pragma nounroll
  for (;;) {
    asm volatile("" ::: "memory");
    const v4u v0 = ld_sc1(rx, o0), v1 = ld_sc1(rx, o1);
    word = __builtin_amdgcn_raw_buffer_load_b32(rx, ot, 0, AUX_SC1);
    *(v4u*)dst0 = v0;
    *(v4u*)dst1 = v1;
    if (__builtin_expect(__all(umax3(umax4(v0), umax4(v1), word) != SENT), 1)) break;
    if (spin == 0) { o0 ^= 8192; o1 ^= 8192; ot ^= 8192; sleep_units(T_R2); }
    if (++spin >= SPIN_MAX) break;
    __builtin_amdgcn_s_sleep(2);
  }
  return spin;
}

DI void probe_place(const Params& p, const rsrc_t rx) {
  unsigned char* ws = p.ws;
  const int lane = threadIdx.x & 63, w = __builtin_amdgcn_readfirstlane(threadIdx.x >> 6);
  const unsigned a0 = (unsigned)(size_t)(ws + OFF_EX);
    if (w >= 4) {
      const int r = w - 4;
      int pr = -1;
      for (int k = 0; k < 64; ++k) if (pr < 0 && (int)(((a0 + (unsigned)k * 81920u) >> 21) - (a0 >> 21)) == r) pr = k * 5;
      unsigned res = 2;
      if (pr >= 0) {
        const int base = (int)OFF_EX + pr * 16384 + 7168;
        const v4u sent = {SENT, SENT, SENT, SENT};
        __builtin_amdgcn_raw_buffer_store_b128(sent, rx, base + lane * 16, 0, AUX_SC1);
        __builtin_amdgcn_raw_buffer_store_b128(sent, rx, base + 8192 + lane * 16, 0, AUX_SC1);
        asm volatile("s_waitcnt vmcnt(0)" ::: "memory");
        unsigned best[2] = {0xFFFFFFFFu, 0xFFFFFFFFu};
#pragma unroll
        for (int k = 0; k < 8; ++k) {
          unsigned long long t0, t1;
          asm volatile("s_memtime %0\n\ts_waitcnt lgkmcnt(0)" : "=s"(t0) :: "memory");
          unsigned v = __builtin_amdgcn_raw_buffer_load_b32(rx, base + (k & 1) * 8192 + (k >> 1) * 128, 0, AUX_SC1);
          asm volatile("s_waitcnt vmcnt(0)" :: "v"(v) : "memory");
          asm volatile("s_memtime %0\n\ts_waitcnt lgkmcnt(0)" : "=s"(t1) :: "memory");
          best[k & 1] = min(best[k & 1], (unsigned)(t1 - t0));
        }
        const unsigned b13 = ((a0 + (unsigned)pr * 16384u) >> 13) & 1;
        res = 2 + ((best[1] < best[0]) ? (b13 ^ 1) : b13);
      }
      if (lane == 0) __builtin_amdgcn_raw_buffer_store_b32(res, rx, (int)OFF_PTAB + r * 4, 0, AUX_SC1);
    }
}

DI void chain_role(const Params& p, unsigned char* smem, const rsrc_t rx, const int cu) {
  unsigned char* ws = p.ws;
  const int tid = threadIdx.x, lane = tid & 63, w = __builtin_amdgcn_readfirstlane(tid >> 6);
  const int kg = lane >> 4, n = lane & 15;
  const float sc = 0.0625f * 1.4426950408889634f;
  __attribute__((address_space(3))) unsigned char* smem3 = (__attribute__((address_space(3))) unsigned char*)smem;
  STAMP_INIT();
  auto startup = [&]() {
  {
    const v4u sent = {SENT, SENT, SENT, SENT};
    constexpr int EX_KB = (int)(64 * EX_STEP / 1024);
    for (int i = 16 + cu * 8 + w; i < EX_KB; i += N_CHAIN * 8) st4_sc1(sent, rx, (int)OFF_EX + i * 1024 + lane * 16);
    if (cu * 8 + w < 448) st4_sc1(sent, rx, (cu * 8 + w < 224 ? (int)OFF_KM + 62 * 114688 + (cu * 8 + w) * 1024 : (int)OFF_VM + 62 * 114688 + (cu * 8 + w - 224) * 1024) + lane * 16);
    DRAIN();
    __syncthreads();
    if (tid == 448) arrive(ws, rx, CTR_FX, (unsigned)N_CHAIN);
  }
  {
    const unsigned a0 = (unsigned)(size_t)(ws + OFF_EX);
    if (w == 0) {
      unsigned v = 0;
      for (int spin = 0; spin < SPIN_MAX; ++spin) {
        asm volatile("" ::: "memory");
        v = __builtin_amdgcn_raw_buffer_load_b32(rx, (int)OFF_PTAB + (lane & 3) * 4, 0, AUX_SC1);
        if (__all(v != 0)) break;
        __builtin_amdgcn_s_sleep(4);
      }
      ctr_wait(rx, CTR_FX, (unsigned)N_CHAIN, 4);
      if (lane < 4) *(unsigned*)(smem + L_VOTE + lane * 4) = v;
      if (lane < 2) *(unsigned*)(smem + L_SCH + lane * 4) = 0u;
    }
    __syncthreads();
    const unsigned a = a0 + (unsigned)tid * 16384u;
    const int reg = (int)((a >> 21) - (a0 >> 21)) & 3, b13 = (a >> 13) & 1;
    if (tid < 320) { const int nearbit = *(const int*)(smem + L_VOTE + reg * 4) & 1; *(int*)(smem + L_TAB2 + tid * 4) = (int)OFF_EX + tid * 16384 + ((nearbit != b13) ? 8192 : 0); }
    __syncthreads();
  }
  };
  const int lmoff = (kg >> 1) * 256 + ((n & 7) >> 2) * 128 + (kg & 1) * 64 + (n & 3) * 16;
  const int goff = (w - 4) * 2048 + lane * 16;
  if (cu < N_S) {
    const int c = cu, m7 = c / 7, r7 = c % 7;
    sleep_units(D_S);
    if (w < 4) {
      const int tile = 8 * c + 2 * w, hS = tile / 28, ksj = (tile % 28) >> 1;
      v4u Mf[2][KS];
#pragma unroll
      for (int q = 0; q < 2; ++q)
#pragma unroll
        for (int ks = 0; ks < KS; ++ks) Mf[q][ks] = *(const v4u*)(ws + OFF_WC + ((size_t)(tile + q) * 14 + ks) * 1024 + lane * 16);
      startup();
#pragma unroll
      for (int ks = 0; ks < KS; ++ks) { KEEP4(Mf[0][ks]); KEEP4(Mf[1][ks]); }
      unsigned char* pdst = smem + ((kg < 2 && n < 8) ? L_P1 + (kg * 8 + n) * 16 + w * 4 : L_DUMMY);
      const int kmo = (int)OFF_KM + ((hS * 14 + ksj) * 4 + kg) * 128 + n * 16;
#pragma nounroll
      for (int s = 0; s < NSEG; ++s) {
        const unsigned char* lm = smem + L_MEM + (s & 1) * 7168 + lmoff;
        BAR();
        STAMP(2);
        v4u mf[KS + 1];
#pragma unroll
        for (int ks = 0; ks < PFD; ++ks) mf[ks] = *(const v4u*)(lm + ks * 512);
        __builtin_amdgcn_sched_barrier(0);
        f32x4 a0 = {0, 0, 0, 0}, a1 = {0, 0, 0, 0};
#pragma unroll
        for (int ks = 0; ks < KS; ++ks) {
          if (ks + PFD < KS) mf[ks + PFD] = *(const v4u*)(lm + (ks + PFD) * 512);
          if (ks + PFD == KS) mf[KS] = *(const v4u*)(lm + ksj * 512);
          a0 = mfma32(Mf[0][ks], mf[ks], a0); a1 = mfma32(Mf[1][ks], mf[ks], a1);
          __builtin_amdgcn_sched_barrier(0);
        }
        const v4u piece = mf[KS];
        const v4u tb = pk8(a0, a1);
        const f32x4 d2 = mfma32(tb, piece, f32x4{0, 0, 0, 0});
        *(float*)(pdst) = d2.x; *(float*)(pdst + 256) = d2.y; *(float*)(pdst + 512) = d2.z; *(float*)(pdst + 768) = d2.w;
        STAMP(3);
        BAR();
        st4_sc1(nosent4(tb), rx, n < 8 ? kmo + s * (16 * 14 * 512) : OOB);
        DRAIN();
      }
      BAR();
    } else {
      startup();
      Sched sd = {0u, 0u, 0, 0, 0, 0, 1};
      const bool strad = r7 == 3;
      const int ii = lane & 7, key = ((lane >> 3) & 1) * 4 + (lane >> 4);
      const int hh = 2 * m7 + (r7 > 3 ? 1 : 0), pp = r7 > 3 ? r7 - 3 : r7;
      const int so1 = ((hh * 4 + pp) * 4 + (ii & 3)) * 32 + key * 4;
      const int so2 = (((2 * m7 + 1) * 4 + 0) * 4 + (ii & 3)) * 32 + key * 4;
      const unsigned char* psrc = smem + L_P1 + lane * 16;
#pragma nounroll
      for (int s = 0; s < NSEG; ++s) {
        const int o0 = ex_off(s, 0) + goff;
        const int eo2 = ex_off(s, 2), eo4 = ex_off(s, 4);
        const int eb = (ii >> 2) ? eo4 : eo2;
        const int sto1 = (w == 4 && s < NSEG - 1) ? eb + so1 : OOB;
        const int sto2 = (w == 4 && s < NSEG - 1 && strad) ? eb + so2 : OOB;
        unsigned char* d0 = smem + L_MEM + (s & 1) * 7168 + goff;
        unsigned char* d1 = w == 7 ? smem + L_DUMMY : d0 + 1024;
        const int ot = (s > 0 && lane < NREP) ? o0 - goff + 7168 + lane * 128 : OOB;
        unsigned word;
        KEEP4(sto1); KEEP4(sto2);
        if (s > 1) wait_until(sd.tE2);
        else if (s == 1) { int kn = 0; kv_ensure(rx, 0, kn, 8); sleep_units(T_Q2); }
        STAMP(0);
        const int spin = gather_poll(rx, d0, d1, o0, w == 7 ? OOB : o0 + 1024, ot, word);
        BAR();
        STAMP(1);
        if (w == 7) TL(s);
        BAR();
        const f32x4 a = *(const f32x4*)psrc;
        const float u0 = a.x + a.y, u1 = a.z + a.w;
        const float v1 = strad ? u0 : u0 + u1;
        st1_sc1(nosent(__float_as_uint(v1)), rx, sto1);
        st1_sc1(nosent(__float_as_uint(u1)), rx, sto2);
        st1_sc1(nosent(__float_as_uint(v1)), rx, sto1 ^ 8192);
        st1_sc1(nosent(__float_as_uint(u1)), rx, sto2 ^ 8192);
        __builtin_amdgcn_sched_barrier(0);
        STAMP(5);
        STAMPV(10, spin);
        sched_step(sd, word, lane, s);
        sd.nf += (s > 1 && spin) ? 1 : 0;
        if (w == 6 && lane == 0 && s > 0) arrive(ws, rx, CTR_M + (s - 1) * 128, 84u);
      }
      BAR();
      if (w == 6 && lane == 0) arrive(ws, rx, CTR_M + (NSEG - 1) * 128, 84u);
      FAIL_DUMP(sd.nf);
    }
  } else if (cu < N_S + N_O) {
    const int o_ = cu - N_S, rg = o_ / 28, ct = o_ % 28;
    sleep_units(D_O);
    v4u Nf[2][KS];
#pragma unroll
    for (int q = 0; q < 2; ++q)
#pragma unroll
      for (int ks = 0; ks < KS; ++ks) Nf[q][ks] = *(const v4u*)(ws + OFF_WC + ((size_t)(448 + (2 * w + q) * 28 + ct) * 14 + ks) * 1024 + lane * 16);
    startup();
#pragma unroll
    for (int ks = 0; ks < KS; ++ks) { KEEP4(Nf[0][ks]); KEEP4(Nf[1][ks]); }
    auto publish_y = [&](int s) {
      const int h = 4 * (w - 4) + kg;
      const unsigned char* ys = smem + L_YB + (h * 4) * 64 + n * 4;
      const v4u y = v4u{*(const unsigned*)(ys), *(const unsigned*)(ys + 64), *(const unsigned*)(ys + 128), *(const unsigned*)(ys + 192)};
      st4_sc1(nosent4(y), rx, rg == 0 ? (int)OFF_VM + ((s * 16 + h) * 28 + ct) * 256 + n * 16 : OOB);
    };
    auto compute_y = [&](int s) {
      const unsigned char* lm = smem + L_MEM + (s & 1) * 7168 + lmoff;
      v4u mf[KS];
#pragma unroll
      for (int ks = 0; ks < PFD; ++ks) mf[ks] = *(const v4u*)(lm + ks * 512);
      __builtin_amdgcn_sched_barrier(0);
      f32x4 a0 = {0, 0, 0, 0}, a1 = {0, 0, 0, 0};
#pragma unroll
      for (int ks = 0; ks < KS; ++ks) {
        if (ks + PFD < KS) mf[ks + PFD] = *(const v4u*)(lm + (ks + PFD) * 512);
        a0 = mfma32(mf[ks], Nf[0][ks], a0); a1 = mfma32(mf[ks], Nf[1][ks], a1);
        __builtin_amdgcn_sched_barrier(0);
      }
      v2u y0 = pk4(a0), y1 = pk4(a1);
      unsigned char* yd = smem + (kg < 2 ? L_YB + ((2 * w) * 4 + 2 * kg) * 64 + n * 4 : L_DUMMY);
      *(unsigned*)(yd) = y0.x; *(unsigned*)(yd + 64) = y0.y;
      *(unsigned*)(yd + 256) = y1.x; *(unsigned*)(yd + 320) = y1.y;
    };
    if (w < 4) {
      int nf = 0;
      v4u Vf[4];
      int known = 0;
      unsigned aold = 0;
      kv_ensure(rx, 0, known, 4);
#pragma unroll
      for (int g2 = 0; g2 < 4; ++g2) Vf[g2] = ld_sc1(rx, (int)OFF_VP + ((4 * w + g2) * 28 + ct) * 1024 + lane * 16);
      const int g = n >> 2, i4 = n & 3, hP = 4 * w + g;
      unsigned char* pdst = smem + L_P1 + (kg * 4 + i4) * 80 + (w * 4 + g) * 4;
      const unsigned char* ys = smem + L_YB + ((4 * w) * 4 + 2 * (kg & 1) + (kg >> 1)) * 64 + n * 4;
#pragma nounroll
      for (int s = 0; s < NSEG; ++s) {
        const int eb = ex_off(s, 1 + 2 * rg);
        int oe = eb + (hP * 4 + i4) * 64 + kg * 16;
        int ol = eb + 4096 + (hP * 4 + i4) * 4;
        int om = ex_off(s, 2 + 2 * rg) + ((hP * 4 + 2 * (kg >> 1)) * 4 + i4) * 32 + (kg & 1) * 16;
        KEEP4(oe); KEEP4(ol); KEEP4(om);
        BAR();
        STAMP(2);
        compute_y(s);
#pragma unroll
        for (int g2 = 0; g2 < 4; ++g2) KEEP4(Vf[g2]);
        if (w == 3 && lane == 0 && rg == 0 && s > 0) arrive_finish(rx, aold, CTR_M + (s - 1) * 128, 84u);
        if (s == NSEG - 1) break;
        unsigned tiss;
        for (;;) {
          const v2u tv = *(volatile const __attribute__((address_space(3))) v2u*)(smem3 + L_SCH);
          tiss = __builtin_amdgcn_readfirstlane(tv.x);
          if (__builtin_amdgcn_readfirstlane(tv.y) == (unsigned)(s + 1)) break;
        }
        STAMP(3);
        if (s > 0) tiss = wait_until(tiss);
        else { sleep_units(T_Q1); tiss = rt32(); }
        STAMP(6);
        int spin = 0;
        f32x4 dres;
        v4u Yf = v4u{0, 0, 0, 0};
#pragma nounroll
        for (;;) {
          asm volatile("" ::: "memory");
          const v4u q0 = ld_sc1(rx, om), q1 = ld_sc1(rx, om + 128);
          const unsigned ls = __builtin_amdgcn_raw_buffer_load_b32(rx, ol, 0, AUX_SC1);
          const v4u ex = ld_sc1(rx, oe);
          __builtin_amdgcn_sched_barrier(0);
          if (spin == 0) {
            BAR();
            Yf = v4u{*(const unsigned*)(ys), *(const unsigned*)(ys + 256), *(const unsigned*)(ys + 512), *(const unsigned*)(ys + 768)};
            KEEP4(Yf);
          }
          __builtin_amdgcn_sched_barrier(0);
          const f32x4 sq = __builtin_bit_cast(f32x4, q0) + __builtin_bit_cast(f32x4, q1);
          auto ra = __builtin_amdgcn_permlane32_swap(__float_as_uint(sq.x), __float_as_uint(sq.z), false, false);
          auto rb = __builtin_amdgcn_permlane32_swap(__float_as_uint(sq.y), __float_as_uint(sq.w), false, false);
          const unsigned ra0 = ra[0], ra1 = ra[1], rb0 = rb[0], rb1 = rb[1];
          const float sm0 = __uint_as_float(ra0) + __uint_as_float(ra1), sm1 = __uint_as_float(rb0) + __uint_as_float(rb1);
          const float lse = __uint_as_float(ls);
          const float mx = xmax32(xmax16(fmaxf(sm0, sm1)));
          const float nb = -fmaxf(mx * sc, lse);
          const float e0 = __builtin_amdgcn_exp2f(__builtin_fmaf(sm0, sc, nb)), e1 = __builtin_amdgcn_exp2f(__builtin_fmaf(sm1, sc, nb));
          const float wx = __builtin_amdgcn_exp2f(lse + nb);
          const unsigned pk = pk2(e0, e1);
          const v4u pb = v4u{g == 0 ? pk : 0u, g == 1 ? pk : 0u, g == 2 ? pk : 0u, g == 3 ? pk : 0u};
          const f32x4 dx0 = mfma32(Vf[0], ex, f32x4{0, 0, 0, 0});
          const f32x4 dx1 = mfma32(Vf[1], ex, f32x4{0, 0, 0, 0});
          const f32x4 dx2 = mfma32(Vf[2], ex, f32x4{0, 0, 0, 0});
          const f32x4 dx3 = mfma32(Vf[3], ex, f32x4{0, 0, 0, 0});
          const f32x4 dm = mfma32(Yf, pb, f32x4{0, 0, 0, 0});
          const float inv = __builtin_amdgcn_rcpf(wx + xsum32(xsum16(e0 + e1)));
          const unsigned mxu = umax3(umax3(umax4(q0), umax4(q1), ls), umax4(ex), 0u);
          const f32x4 dxa = g < 2 ? (g == 0 ? dx0 : dx1) : (g == 2 ? dx2 : dx3);
          dres = (dxa * wx + dm) * inv;
          *(float*)(pdst) = dres.x; *(float*)(pdst + 1280) = dres.y; *(float*)(pdst + 2560) = dres.z; *(float*)(pdst + 3840) = dres.w;
          if (__builtin_expect(__all(mxu != SENT), 1)) break;
          if (spin == 0) { oe ^= 8192; ol ^= 8192; om ^= 8192; sleep_units(T_R1); } else __builtin_amdgcn_s_sleep(2);
          if (++spin >= SPIN_MAX) break;
          tiss = rt32();
        }
        *(unsigned*)(smem + L_SCH + 16 + w * 4) = (tiss << 2) | (spin ? 2u : 0u);
        DRAIN();
        STAMP(8);
        BAR();
        STAMPV(11, spin);
        nf += (s > 1 && spin) ? 1 : 0;
        if (w == 3 && lane == 0 && rg == 0) aold = arrive_issue(ws, CTR_M + s * 128);
        const int sn = s + 1 < NSEG - 1 ? s + 1 : s;
        kv_ensure(rx, sn >> 1, known, 4);
#pragma unroll
        for (int g2 = 0; g2 < 4; ++g2) Vf[g2] = ld_sc1(rx, (int)OFF_VP + (sn * 448 + (4 * w + g2) * 28 + ct) * 1024 + lane * 16);
      }
      BAR();
      BAR();
      FAIL_DUMP(nf);
    } else {
      Sched sd = {0u, 0u, 0, 0, 0, 0, 1};
      const unsigned char* pp = smem + L_P1 + lane * 80;
      const bool st = w == 4 && (kg & 1) == 0;
      const int eoff = ct * 256 + rg * 128 + ((((n >> 3) & 1) * 4 + (n & 3)) * 8 + ((n >> 2) & 1) * 4 + kg) * 2;
#pragma nounroll
      for (int s = 0; s < NSEG; ++s) {
        const int o0 = ex_off(s, 0) + goff;
        unsigned char* d0 = smem + L_MEM + (s & 1) * 7168 + goff;
        unsigned char* d1 = w == 7 ? smem + L_DUMMY : d0 + 1024;
        const int ot = (s > 0 && lane < NREP) ? o0 - goff + 7168 + lane * 128 : OOB;
        unsigned word;
        if (s > 1) wait_until(sd.tE2 + T_OG);
        else if (s == 1) sleep_units(8);
        STAMP(0);
        const int spin = gather_poll(rx, d0, d1, o0, w == 7 ? OOB : o0 + 1024, ot, word);
        __builtin_amdgcn_s_setprio(3);
        BAR();
        STAMP(1);
        sched_step(sd, word, lane, s);
        if (w == 4) *(volatile __attribute__((address_space(3))) v2u*)(smem3 + L_SCH) = v2u{sd.tE1, (unsigned)(s + 1)};
        __builtin_amdgcn_s_setprio(0);

        sd.nf += (s > 1 && spin) ? 1 : 0;
        *(unsigned*)(smem + L_SCH + 32 + (w - 4) * 4) = (s > 1 && spin) ? 1u : 0u;
        compute_y(s);
        if (s == NSEG - 1) break;
        const int eon = ex_off(s + 1, 0);
        const int sto = st ? eon + eoff : ((w == 5 && lane == 0 && o_ % (56 / NREP) == 0) ? eon + 7168 + (o_ / (56 / NREP)) * 128 : OOB);
        BAR();
        publish_y(s);
        const v4u gf = *(const v4u*)(smem + L_SCH + 32);
        const unsigned gfl = gf.x | gf.y | gf.z | gf.w;
        const unsigned tref = sd.tE1 << 2;
        KEEP4(sto); KEEP4(gfl);
        DRAIN();
        if (w == 4) {
          BAR();
          STAMP(4);
          const f32x4 a = (*(const f32x4*)(pp) + *(const f32x4*)(pp + 16)) + (*(const f32x4*)(pp + 32) + *(const f32x4*)(pp + 48));
          const float v = (a.x + a.y) + (a.z + a.w);
          auto r = __builtin_amdgcn_permlane16_swap(__float_as_uint(v), __float_as_uint(v), false, false);
          const unsigned r0 = r[0], r1 = r[1];
          const unsigned ov = nosent(pk2(__uint_as_float(r0), __uint_as_float(r1)));
          st1_sc1(ov, rx, sto);
          st1_sc1(ov, rx, sto ^ 8192);
        } else {
          BAR();
          const v4u tw = *(const v4u*)(smem + L_SCH + 16);
          const unsigned tm = tref + (unsigned)max(max((int)(tw.x - tref), (int)(tw.y - tref)), max((int)(tw.z - tref), (int)(tw.w - tref)));
          const unsigned wd = nosent(tm | ((tw.x | tw.y | tw.z | tw.w) & 3u) | gfl);
          st1_sc1(wd, rx, sto);
          st1_sc1(wd, rx, sto ^ 8192);
        }
        __builtin_amdgcn_sched_barrier(0);
        STAMP(5);
        STAMPV(10, spin);
      }
      BAR();
      publish_y(NSEG - 1);
      DRAIN();
      BAR();
      if (w == 6 && lane == 0 && rg == 0) arrive(ws, rx, CTR_M + (NSEG - 1) * 128, 84u);
      FAIL_DUMP(w == 7 ? sd.m1 * 1000 + sd.m2 : sd.nf);
    }
  } else {
    startup();
    if (w < 4) {
      const int h = 2 * (cu - N_S - N_O) + (w >> 1), u = w & 1;
      v4u K[KS];
      int known = 0;
      kv_ensure(rx, 0, known, 4);
#pragma unroll
      for (int ks = 0; ks < KS; ++ks) K[ks] = ld_sc1(rx, (int)OFF_KP + ((h * 2 + u) * 14 + ks) * 1024 + lane * 16);
      unsigned char* xw = smem + (kg == 0 ? L_P1 + w * 128 + n * 8 : L_DUMMY);
      const unsigned char* xr = smem + L_P1 + (w ^ 1) * 128 + n * 8;
#pragma nounroll
      for (int s = 0; s < NSEG - 1; ++s) {
        const int eo1 = ex_off(s, 1), eo3 = ex_off(s, 3);
        const int eb = ((n >> 2) & 1) ? eo3 : eo1;
        const int so = n < 8 ? eb + (h * 4 + (n & 3)) * 64 + kg * 16 + u * 8 : OOB;
        const int lo = (lane < 8 && u == 0) ? eb + 4096 + (h * 4 + (n & 3)) * 4 : OOB;
        const unsigned char* lm = smem + L_MEM + (s & 1) * 7168 + lmoff;
#pragma unroll
        for (int ks = 0; ks < KS; ++ks) KEEP4(K[ks]);
        KEEP4(so); KEEP4(lo);
        BAR();
        STAMP(2);
        v4u mf[KS];
#pragma unroll
        for (int ks = 0; ks < PFD + 2; ++ks) mf[ks] = *(const v4u*)(lm + ks * 512);
        __builtin_amdgcn_sched_barrier(0);
        f32x4 a0 = {0, 0, 0, 0}, a1 = {0, 0, 0, 0};
#pragma unroll
        for (int ks = 0; ks < KS; ks += 2) {
          if (ks + PFD + 2 < KS) mf[ks + PFD + 2] = *(const v4u*)(lm + (ks + PFD + 2) * 512);
          if (ks + PFD + 3 < KS) mf[ks + PFD + 3] = *(const v4u*)(lm + (ks + PFD + 3) * 512);
          a0 = mfma32(K[ks], mf[ks], a0);
          a1 = mfma32(K[ks + 1], mf[ks + 1], a1);
          __builtin_amdgcn_sched_barrier(0);
        }
        a0 += a1;
        float xs[4] = {rbf(a0.x), rbf(a0.y), rbf(a0.z), rbf(a0.w)};
        const float mx = xmax32(xmax16(fmaxf(fmaxf(xs[0], xs[1]), fmaxf(xs[2], xs[3])))) * sc;
#pragma unroll
        for (int j = 0; j < 4; ++j) xs[j] = __builtin_amdgcn_exp2f(__builtin_fmaf(xs[j], sc, -mx));
        const float sum = xsum32(xsum16((xs[0] + xs[1]) + (xs[2] + xs[3])));
        *(f32x2*)xw = f32x2{mx, sum};
        BAR();
        const f32x2 ot = *(const f32x2*)xr;
        const float mm = fmaxf(mx, ot.x);
        const float fu = __builtin_amdgcn_exp2f(mx - mm), fo = __builtin_amdgcn_exp2f(ot.x - mm);
        const float pu = sum * fu, po = ot.y * fo;
        const float tot = u ? po + pu : pu + po;
        const float scl = fu * __builtin_amdgcn_rcpf(tot);
        const v2u o = nosent2(v2u{pk2(xs[0] * scl, xs[1] * scl), pk2(xs[2] * scl, xs[3] * scl)});
        st2_sc1(o, rx, so);
        const unsigned lse = nosent(__float_as_uint(__builtin_amdgcn_logf(tot) + mm));
        st1_sc1(lse, rx, lo);
        st2_sc1(o, rx, so ^ 8192);
        st1_sc1(lse, rx, lo ^ 8192);
        __builtin_amdgcn_sched_barrier(0);
        STAMP(5);
        const int sn = s + 1 < NSEG - 1 ? s + 1 : s;
        kv_ensure(rx, sn >> 1, known, 4);
#pragma unroll
        for (int ks = 0; ks < KS; ++ks) K[ks] = ld_sc1(rx, (int)OFF_KP + (((sn * 16 + h) * 2 + u) * 14 + ks) * 1024 + lane * 16);
      }
    } else {
      Sched sd = {0u, 0u, 0, 0, 0, 0, 1};
#pragma nounroll
      for (int s = 0; s < NSEG - 1; ++s) {
        const int o0 = ex_off(s, 0) + goff;
        unsigned char* d0 = smem + L_MEM + (s & 1) * 7168 + goff;
        unsigned char* d1 = w == 7 ? smem + L_DUMMY : d0 + 1024;
        const int ot = (s > 0 && lane < NREP) ? o0 - goff + 7168 + lane * 128 : OOB;
        unsigned word;
        if (s > 1) wait_until(sd.tE2);
        else if (s == 1) { int kn = 0; kv_ensure(rx, 0, kn, 8); sleep_units(T_Q2); }
        STAMP(0);
        const int spin = gather_poll(rx, d0, d1, o0, w == 7 ? OOB : o0 + 1024, ot, word);
        BAR();
        STAMP(1);
        BAR();
        STAMPV(10, spin);
        sched_step(sd, word, lane, s);
        sd.nf += (s > 1 && spin) ? 1 : 0;
      }
      FAIL_DUMP(sd.nf);
    }
  }
  STAMP_DUMP();
}
DI void phase_kv(const Params& p, unsigned char* lds, rsrc_t rw, int cg, int par) {
  unsigned char* ws = p.ws;
  const int tid = threadIdx.x, lane = tid & 63, w = __builtin_amdgcn_readfirstlane(tid >> 6);
  const int nt0 = cg * 16 + 2 * w;
  v4u st[7];
  {
    const unsigned char* src = ws + OFF_XN + (size_t)par * 57344 + tid * 16;
#pragma unroll
    for (int i = 0; i < 7; ++i) st[i] = *(const v4u*)(src + i * 8192);
  }
  v4u W[2][KS];
#pragma unroll
  for (int q = 0; q < 2; ++q)
#pragma unroll
    for (int ks = 0; ks < KS; ++ks) W[q][ks] = *(const v4u*)(ws + OFF_WC + ((size_t)(nt0 + q) * 14 + ks) * 1024 + lane * 16);
#pragma unroll
  for (int i = 0; i < 7; ++i) *(v4u*)(lds + i * 8192 + tid * 16) = st[i];
#pragma unroll
  for (int ks = 0; ks < KS; ++ks) { KEEP4(W[0][ks]); KEEP4(W[1][ks]); }
  BAR();
  const bool isK = cg < 28;
  const int kbase = (int)OFF_KP + (((nt0 / 28) * 2) * 14 + ((nt0 % 28) >> 1)) * 1024 + lane * 16;
  const int vbase = (int)OFF_VP + (nt0 - 448) * 1024 + lane * 16;
  int pend = -1;
  unsigned aold = 0;
#pragma nounroll
  for (int it = 0; it < 16; ++it) {
    const int hb = par + 2 * it;
    const unsigned char* cur = lds + (it & 1) * 57344 + lane * 16;
    {
      const int so = (int)OFF_XN + (it + 1 < 16 ? hb + 2 : hb) * 57344;
#pragma unroll
      for (int i = 0; i < 7; ++i) st[i] = __builtin_amdgcn_raw_buffer_load_b128(rw, tid * 16, so + i * 8192, 0);
    }
    __builtin_amdgcn_sched_barrier(0);
    f32x4 acc[4][2];
#pragma unroll
    for (int rt = 0; rt < 4; ++rt) { acc[rt][0] = f32x4{0, 0, 0, 0}; acc[rt][1] = f32x4{0, 0, 0, 0}; }
    if (isK) {
      v4u af[2][4];
#pragma unroll
      for (int rt = 0; rt < 4; ++rt) af[0][rt] = *(const v4u*)(cur + (rt * 14) * 1024);
#pragma unroll
      for (int ks = 0; ks < KS; ++ks) {
        if (ks + 1 < KS) {
#pragma unroll
          for (int rt = 0; rt < 4; ++rt) af[(ks + 1) & 1][rt] = *(const v4u*)(cur + (rt * 14 + ks + 1) * 1024);
        }
        __builtin_amdgcn_sched_barrier(0);
#pragma unroll
        for (int rt = 0; rt < 4; ++rt) {
          acc[rt][0] = mfma32(W[0][ks], af[ks & 1][rt], acc[rt][0]);
          acc[rt][1] = mfma32(W[1][ks], af[ks & 1][rt], acc[rt][1]);
        }
        __builtin_amdgcn_sched_barrier(0);
      }
    } else {
      v4u af[2][4];
#pragma unroll
      for (int rt = 0; rt < 4; ++rt) af[0][rt] = *(const v4u*)(cur + (rt * 14) * 1024);
#pragma unroll
      for (int ks = 0; ks < KS; ++ks) {
        if (ks + 1 < KS) {
#pragma unroll
          for (int rt = 0; rt < 4; ++rt) af[(ks + 1) & 1][rt] = *(const v4u*)(cur + (rt * 14 + ks + 1) * 1024);
        }
        __builtin_amdgcn_sched_barrier(0);
#pragma unroll
        for (int rt = 0; rt < 4; ++rt) {
          acc[rt][0] = mfma32(af[ks & 1][rt], W[0][ks], acc[rt][0]);
          acc[rt][1] = mfma32(af[ks & 1][rt], W[1][ks], acc[rt][1]);
        }
        __builtin_amdgcn_sched_barrier(0);
      }
    }
    __builtin_amdgcn_sched_barrier(0);
#pragma unroll
    for (int i = 0; i < 7; ++i) *(v4u*)(lds + ((it + 1) & 1) * 57344 + i * 8192 + tid * 16) = st[i];
    DRAIN();
    if (pend >= 0 && lane == 0) arrive_finish(rw, aold, pend, 56u);
    pend = -1;
    __builtin_amdgcn_sched_barrier(0);
    if (isK) {
#pragma unroll
      for (int rt = 0; rt < 4; ++rt) st4_sc1(pk8(acc[rt][0], acc[rt][1]), rw, kbase + ((hb * 2 + (rt >> 1)) * 32 + (rt & 1)) * 14336);
    } else {
#pragma unroll
      for (int q = 0; q < 2; ++q)
#pragma unroll
        for (int sg = 0; sg < 2; ++sg) st4_sc1(pk8(acc[2 * sg][q], acc[2 * sg + 1][q]), rw, vbase + ((hb * 2 + sg) * 448 + q) * 1024);
    }
    if (it == 0 || it == 15) DRAIN();
    BAR();
    if (w == 0) TL(200 + it);
    if (w == (it & 7)) {
      if (it == 0 || it == 15) { if (lane == 0) arrive(ws, rw, CTR_KV + hb * 128, 56u); }
      if (it >= 2) {
        if (it == 15) { if (lane == 0) arrive(ws, rw, CTR_KV + (hb - 2) * 128, 56u); }
        else { pend = CTR_KV + (hb - 2) * 128; if (lane == 0) aold = arrive_issue(ws, pend); }
      }
    }
  }
}

DI void gate_wait(rsrc_t rw, int rt, int nap, int lag, int own) {
  int off = (int)OFF_PM + rt * 4096 + ((blockIdx.x >> 3) & 31) * 128;
  if (__builtin_amdgcn_readfirstlane(ld1_sc1(rw, off)) != SENT) {
    off = own; lag = 4; nap = 8;
    if (__builtin_amdgcn_readfirstlane(ld1_sc1(rw, off)) != SENT) return;
  }
#pragma nounroll
  for (int spin = 0; spin < SPIN_MAX; ++spin) {
    sleep_units(nap);
    asm volatile("" ::: "memory");
    if (__builtin_amdgcn_readfirstlane(ld1_sc1(rw, off)) != SENT) break;
  }
  sleep_units(lag);
}
#define LAG_A2 10
#define LAG_F1 65
#define LAG_F2 140
#define RETRY 6
#define NAPT 4
#define NAP 6

#define TLX(k) do { if (rt == 127 || rt == 96) TL((rt == 127 ? 130 : 140) + (k)); } while (0)
DI void stage_a1(const Params& p, rsrc_t rw, int h) {
  unsigned char* ws = p.ws;
  const int tid = threadIdx.x, lane = tid & 63, w = __builtin_amdgcn_readfirstlane(tid >> 6);
  const int kg = lane >> 4, n = lane & 15, v = w & 1;
  const float sc = 0.0625f * 1.4426950408889634f;
  const float NEG = -3.0e38f;
  int known = 0;
  ctr_wait(rw, CTR_FX, 1u, NAP);
#pragma nounroll
  for (int rt = w; rt < 128; rt += 8) {
    const int s = rt >> 1;
    v4u xf[KS], kf[KS], k1[KS];
    kv_ensure(rw, s >> 1, known, NAP);
    const int kb = (int)OFF_KP + ((s * 16 + h) * 2) * 14336 + lane * 16;
#pragma unroll
    for (int ks = 0; ks < KS; ++ks) {
      xf[ks] = *(const v4u*)(ws + OFF_XN + ((size_t)rt * 14 + ks) * 1024 + lane * 16);
      kf[ks] = ld_sc1(rw, kb + ks * 1024);
    }
#pragma unroll
    for (int ks = 0; ks < KS; ++ks) k1[ks] = ld_sc1(rw, v ? kb + 14336 + ks * 1024 : OOB);
    __builtin_amdgcn_sched_barrier(0);
    f32x4 a0 = {0, 0, 0, 0}, a1 = {0, 0, 0, 0}, am = {0, 0, 0, 0};
#pragma unroll
    for (int ks = 0; ks < KS; ++ks) a0 = mfma32(kf[ks], xf[ks], a0);
#pragma unroll
    for (int ks = 0; ks < KS; ++ks) a1 = mfma32(k1[ks], xf[ks], a1);
    float xs[8];
#pragma unroll
    for (int r = 0; r < 4; ++r) {
      const bool diag = 4 * kg + r <= n;
      xs[r] = (v || diag) ? rbf(a0[r]) : NEG;
      xs[4 + r] = (v && diag) ? rbf(a1[r]) : NEG;
    }
    float mx = NEG;
#pragma unroll
    for (int j = 0; j < 8; ++j) mx = fmaxf(mx, xs[j]);
    const int tb = (int)OFF_KM + (s * 16 + h) * 7168 + kg * 128 + (n & 7) * 16;
    TLX(3);
    ctr_wait(rw, CTR_M + (s < NSEG - 2 ? s : s - 2) * 128, 84u, NAP);
    TLX(0);
#pragma nounroll
    for (int spin = 0; spin < SPIN_MAX; ++spin) {
      asm volatile("" ::: "memory");
      TLX(1);
      unsigned mxu = 0;
#pragma unroll
      for (int ks = 0; ks < KS; ++ks) kf[ks] = ld_sc1(rw, tb + ks * 512);
      if (s < NSEG - 2) break;
#pragma unroll
      for (int ks = 0; ks < KS; ++ks) mxu = max(mxu, umax4(kf[ks]));
      if (__all(mxu != SENT)) break;
      sleep_units(3);
    }
#pragma unroll
    for (int ks = 0; ks < KS; ++ks) am = mfma32(kf[ks], xf[ks], am);
    float mm[4];
#pragma unroll
    for (int r = 0; r < 4; ++r) { mm[r] = rbf(am[r]); mx = fmaxf(mx, mm[r]); }
    mx = xmax32(xmax16(mx));
    const float nb = -mx * sc;
    float sum = 0.f;
#pragma unroll
    for (int j = 0; j < 8; ++j) { xs[j] = xs[j] == NEG ? 0.f : __builtin_amdgcn_exp2f(__builtin_fmaf(xs[j], sc, nb)); sum += xs[j]; }
#pragma unroll
    for (int r = 0; r < 4; ++r) { mm[r] = __builtin_amdgcn_exp2f(__builtin_fmaf(mm[r], sc, nb)); if (kg < 2) sum += mm[r]; }
    sum = xsum32(xsum16(sum));
    const float inv = 1.f / sum;
    st4_sc1(nosent4(v4u{pk2(xs[0] * inv, xs[1] * inv), pk2(xs[2] * inv, xs[3] * inv), pk2(xs[4] * inv, xs[5] * inv), pk2(xs[6] * inv, xs[7] * inv)}),
            rw, (int)OFF_PX + (rt * 16 + h) * 1024 + lane * 16);
    st2_sc1(nosent2(v2u{pk2(mm[0] * inv, mm[1] * inv), pk2(mm[2] * inv, mm[3] * inv)}), rw,
            kg < 2 ? (int)OFF_PM + (rt * 4 + (h >> 2)) * 1024 + ((h & 3) * 16 + n) * 16 + kg * 8 : OOB);
    TL(rt);
  }
}

DI void stage_a2(const Params& p, rsrc_t rw, int ct) {
  const int tid = threadIdx.x, lane = tid & 63, w = __builtin_amdgcn_readfirstlane(tid >> 6);
  const int kg = lane >> 4, n = lane & 15;
  const int c = 2 * ct + (kg >> 1);
  const int col = 32 * (c >> 2) + 16 * (kg & 1) + 4 * (c & 3);
  const int hoff = (c >> 2) * 1024 + ((c & 3) * 16 + n) * 16 + (kg & 1) * 8;
  int known = 0;
  ctr_wait(rw, CTR_FX, 1u, NAP);
#pragma nounroll
  for (int rt = w; rt < 128; rt += 8) {
    const int s = rt >> 1;
    const v2u xv = *(const v2u*)(p.x + (size_t)(rt * 16 + n) * 448 + col);
    kv_ensure(rw, s >> 1, known, NAP);
    v4u vf[16], yf[4];
#pragma unroll
    for (int h = 0; h < 16; ++h) vf[h] = ld_sc1(rw, (int)OFF_VP + (s * 448 + h * 28 + ct) * 1024 + lane * 16);
    TLX(3);
    ctr_wait(rw, CTR_M + (s < NSEG - 2 ? s : s - 2) * 128, 84u, NAP);
    const int pb = (int)OFF_PX + rt * 16384 + lane * 16, mb = (int)OFF_PM + rt * 4096 + lane * 16;
    gate_wait(rw, rt, NAPT, LAG_A2, (int)OFF_PX + (rt * 16 + (ct & 15)) * 1024);
    TLX(0);
    f32x4 acc;
#pragma nounroll
    for (int spin = 0; spin < SPIN_MAX; ++spin) {
      asm volatile("" ::: "memory");
      TLX(1);
      v4u px[16], pm[4];
#pragma unroll
      for (int q = 0; q < 4; ++q) yf[q] = ld_sc1(rw, (int)OFF_VM + ((s * 16 + 4 * q + kg) * 28 + ct) * 256 + n * 16);
#pragma unroll
      for (int h = 0; h < 16; ++h) px[h] = ld_sc1(rw, pb + h * 1024);
#pragma unroll
      for (int q = 0; q < 4; ++q) pm[q] = ld_sc1(rw, mb + q * 1024);
      unsigned mxu = 0;
      f32x4 a0 = {0, 0, 0, 0}, a1 = {0, 0, 0, 0};
#pragma unroll
      for (int h = 0; h < 16; h += 2) {
        mxu = umax3(mxu, umax4(px[h]), umax4(px[h + 1]));
        a0 = mfma32(vf[h], px[h], a0); a1 = mfma32(vf[h + 1], px[h + 1], a1);
      }
#pragma unroll
      for (int q = 0; q < 4; q += 2) {
        mxu = umax3(mxu, umax3(umax4(pm[q]), umax4(pm[q + 1]), umax4(yf[q])), umax4(yf[q + 1]));
        a0 = mfma32(yf[q], pm[q], a0); a1 = mfma32(yf[q + 1], pm[q + 1], a1);
      }
      acc = a0 + a1;
      if (__all(mxu != SENT)) break;
      sleep_units(RETRY);
    }
    const float h0 = bflo(xv.x) + rbf(acc.x), h1 = bfhi(xv.x) + rbf(acc.y), h2 = bflo(xv.y) + rbf(acc.z), h3 = bfhi(xv.y) + rbf(acc.w);
    st2_sc1(nosent2(v2u{pk2(h0, h1), pk2(h2, h3)}), rw, (int)OFF_HF + rt * 14336 + hoff);
    TL(rt);
  }
}

DI void stage_f1(const Params& p, unsigned char* lds, rsrc_t rw, int kf) {
  const int tid = threadIdx.x, lane = tid & 63, w = __builtin_amdgcn_readfirstlane(tid >> 6);
  const int kg = lane >> 4, n = lane & 15;
  for (int f = w; f < 56; f += 8) {
    const int t = f / 14, ks = f % 14;
    const u16* src = ((t >> 1) ? p.wu : p.wg) + 32 * kf + 16 * (t & 1) + n;
    unsigned e[8];
#pragma unroll
    for (int jj = 0; jj < 8; ++jj) e[jj] = src[(size_t)pidx(ks, kg, jj) * 1792];
    *(v4u*)(lds + f * 1024 + lane * 16) = v4u{e[0] | (e[1] << 16), e[2] | (e[3] << 16), e[4] | (e[5] << 16), e[6] | (e[7] << 16)};
  }
  BAR();
  const v4u ones = {0x3F803F80u, 0x3F803F80u, 0x3F803F80u, 0x3F803F80u};
  f32x4 cs[4];
#pragma unroll
  for (int t = 0; t < 4; ++t) {
    cs[t] = f32x4{0, 0, 0, 0};
#pragma unroll
    for (int ks = 0; ks < KS; ++ks) cs[t] = mfma32(*(const v4u*)(lds + (t * 14 + ks) * 1024 + lane * 16), ones, cs[t]);
  }
#pragma nounroll
  for (int rt = w; rt < 128; rt += 8) {
    const int hb = (int)OFF_HF + rt * 14336 + lane * 16;
    gate_wait(rw, rt, NAPT, LAG_F1, (int)OFF_HF + rt * 14336 + (kf >> 1) * 512);
    TLX(0);
    v4u hf[KS];
#pragma nounroll
    for (int spin = 0; spin < SPIN_MAX; ++spin) {
      asm volatile("" ::: "memory");
      TLX(1);
      unsigned mxu = 0;
#pragma unroll
      for (int ks = 0; ks < KS; ++ks) hf[ks] = ld_sc1(rw, hb + ks * 1024);
#pragma unroll
      for (int ks = 0; ks < KS; ++ks) mxu = max(mxu, umax4(hf[ks]));
      if (__all(mxu != SENT)) break;
      sleep_units(RETRY);
    }
    TLX(2);
    f32x4 acc[4], sm = {0, 0, 0, 0}, gr = {0, 0, 0, 0};
#pragma unroll
    for (int t = 0; t < 4; ++t) acc[t] = f32x4{0, 0, 0, 0};
    v4u wf[2][4];
#pragma unroll
    for (int t = 0; t < 4; ++t) wf[0][t] = *(const v4u*)(lds + (t * 14) * 1024 + lane * 16);
#pragma unroll
    for (int ks = 0; ks < KS; ++ks) {
      if (ks + 1 < KS) {
#pragma unroll
        for (int t = 0; t < 4; ++t) wf[(ks + 1) & 1][t] = *(const v4u*)(lds + (t * 14 + ks + 1) * 1024 + lane * 16);
      }
      sm = mfma32(ones, hf[ks], sm);
      gr = mfma32(hf[ks], hf[ks], gr);
      __builtin_amdgcn_sched_barrier(0);
#pragma unroll
      for (int t = 0; t < 4; ++t) acc[t] = mfma32(wf[ks & 1][t], hf[ks], acc[t]);
      __builtin_amdgcn_sched_barrier(0);
    }
    float dg = (kg == (n >> 2)) ? ((n & 2) ? ((n & 1) ? gr.w : gr.z) : ((n & 1) ? gr.y : gr.x)) : 0.f;
    dg = xsum32(xsum16(dg));
    const float mean = sm.x * (1.f / 448.f);
    const float var = fmaxf(dg * (1.f / 448.f) - mean * mean, 0.f);
    const float mb = rbf(mean), rden = 1.f / ln_den(var);
    f32x4 o0, o1;
#pragma unroll
    for (int r = 0; r < 4; ++r) {
      o0[r] = rbf((acc[0][r] - mb * cs[0][r]) * rden) * rbf((acc[2][r] - mb * cs[2][r]) * rden);
      o1[r] = rbf((acc[1][r] - mb * cs[1][r]) * rden) * rbf((acc[3][r] - mb * cs[3][r]) * rden);
    }
    st4_sc1(nosent4(pk8(o0, o1)), rw, (int)OFF_AF + (rt * 56 + kf) * 1024 + lane * 16);
    TL(rt);
  }
}

DI void stage_f2(const Params& p, unsigned char* lds, rsrc_t rw, int ct) {
  const int tid = threadIdx.x, lane = tid & 63, w = __builtin_amdgcn_readfirstlane(tid >> 6);
  const int kg = lane >> 4, n = lane & 15;
  for (int kf = w; kf < 56; kf += 8) {
    const u16* src = p.wd + 16 * ct + n;
    unsigned e[8];
#pragma unroll
    for (int jj = 0; jj < 8; ++jj) e[jj] = src[(size_t)pidx(kf, kg, jj) * 448];
    *(v4u*)(lds + kf * 1024 + lane * 16) = v4u{e[0] | (e[1] << 16), e[2] | (e[3] << 16), e[4] | (e[5] << 16), e[6] | (e[7] << 16)};
  }
  BAR();
#pragma nounroll
  for (int rt = w; rt < 128; rt += 8) {
    const int ab = (int)OFF_AF + rt * 57344 + lane * 16;
    gate_wait(rw, rt, NAPT, LAG_F2, (int)OFF_AF + rt * 57344 + (2 * ct) * 1024);
    TLX(0);
    v2u hv = v2u{0, 0};
    f32x4 a0, a1;
#pragma nounroll
    for (int spin = 0; spin < SPIN_MAX; ++spin) {
      asm volatile("" ::: "memory");
      TLX(1);
      a0 = f32x4{0, 0, 0, 0}; a1 = f32x4{0, 0, 0, 0};
      hv = __builtin_amdgcn_raw_buffer_load_b64(rw, (int)OFF_HF + rt * 14336 + (ct >> 1) * 1024 + lane * 16 + (ct & 1) * 8, 0, AUX_SC1);
      v4u af[3][14];
#pragma unroll
      for (int bb = 0; bb < 2; ++bb)
#pragma unroll
        for (int k = 0; k < 14; ++k) af[bb][k] = ld_sc1(rw, ab + (bb * 14 + k) * 1024);
      unsigned mxu = max(hv.x, hv.y);
#pragma unroll
      for (int bb = 0; bb < 4; ++bb) {
        if (bb + 2 < 4) {
#pragma unroll
          for (int k = 0; k < 14; ++k) af[(bb + 2) % 3][k] = ld_sc1(rw, ab + ((bb + 2) * 14 + k) * 1024);
        }
        __builtin_amdgcn_sched_barrier(0);
#pragma unroll
        for (int k = 0; k < 14; k += 2) {
          mxu = umax3(mxu, umax4(af[bb % 3][k]), umax4(af[bb % 3][k + 1]));
          a0 = mfma32(*(const v4u*)(lds + (bb * 14 + k) * 1024 + lane * 16), af[bb % 3][k], a0);
          a1 = mfma32(*(const v4u*)(lds + (bb * 14 + k + 1) * 1024 + lane * 16), af[bb % 3][k + 1], a1);
        }
        __builtin_amdgcn_sched_barrier(0);
      }
      if (__all(mxu != SENT)) break;
      sleep_units(RETRY);
    }
    const f32x4 d = a0 + a1;
    const float o0 = bflo(hv.x) + rbf(d.x), o1 = bfhi(hv.x) + rbf(d.y), o2 = bflo(hv.y) + rbf(d.z), o3 = bfhi(hv.y) + rbf(d.w);
    *(v2u*)(p.out + (size_t)(rt * 16 + n) * 448 + 16 * ct + 4 * kg) = v2u{pk2(o0, o1), pk2(o2, o3)};
    TL(rt);
  }
}

DI void arm_stage(const Params& p, rsrc_t rw, int k, int nwg, int off0, int nkb, int ctr) {
  const int tid = threadIdx.x, lane = tid & 63, w = __builtin_amdgcn_readfirstlane(tid >> 6);
  const v4u sent = {SENT, SENT, SENT, SENT};
  for (int i = k * 8 + w; i < nkb; i += nwg * 8) st4_sc1(sent, rw, off0 + i * 1024 + lane * 16);
  DRAIN();
  __syncthreads();
  if (tid == 0) arrive(p.ws, rw, ctr, (unsigned)nwg);
}

constexpr int MAIN_LDS = 114688;
__global__ void __launch_bounds__(512, 2) k_main(Params p) {
  extern __shared__ __attribute__((aligned(16))) unsigned char smem[];
  const rsrc_t rw = __builtin_amdgcn_make_buffer_rsrc((void*)p.ws, 0, (int)WS_END, 0x00020000);
  const int b = blockIdx.x;
  if (threadIdx.x < 64) TL(255);
  const int j = (b >> 3) * 4 + (b & 3);
  if ((b & 7) < 4) {
    if (j < N_CHAIN) chain_role(p, smem, rw, j);
    else { if (j == N_CHAIN) probe_place(p, rw); sleep_units(400); arm_stage(p, rw, j - N_CHAIN, 8, (int)OFF_HF, (int)((OFF_XN - OFF_HF) / 1024), CTR_FS + 128); }
    return;
  }
  if (j < 112) phase_kv(p, smem, rw, j % 56, j / 56);
  else { sleep_units(D_ARM); }
  if (j >= 112) arm_stage(p, rw, j - 112, 16, (int)OFF_PX, (int)((OFF_HF - OFF_PX) / 1024), CTR_FS);
  ctr_wait(rw, CTR_FS, 16u, 8);
  if (j < 112) ctr_wait(rw, CTR_FS + 128, 8u, 8);
  if (j < 56) stage_f1(p, smem, rw, j);
  else if (j < 84) stage_f2(p, smem, rw, j - 56);
  else if (j < 112) stage_a2(p, rw, j - 84);
  else stage_a1(p, rw, j - 112);
}

extern "C" void kernel_launch(void* const* d_in, const int* in_sizes, int n_in, void* d_out, int out_size, void* d_ws, size_t ws_size,
                              hipStream_t stream) {
  Params p;
  p.x = (const u16*)d_in[0]; p.mem0 = (const u16*)d_in[1]; p.Wq = (const u16*)d_in[2]; p.Wk = (const u16*)d_in[3];
  p.Wv = (const u16*)d_in[4]; p.Wo = (const u16*)d_in[5];
  p.wg = (const u16*)d_in[8]; p.wu = (const u16*)d_in[9]; p.wd = (const u16*)d_in[10];
  p.out = (u16*)d_out; p.ws = (unsigned char*)d_ws;
  (void)hipFuncSetAttribute((const void*)k_main, hipFuncAttributeMaxDynamicSharedMemorySize, MAIN_LDS);
  (void)hipFuncSetAttribute((const void*)k_prep, hipFuncAttributeMaxDynamicSharedMemorySize, PREP_LDS);
  k_prep<<<256, 512, PREP_LDS, stream>>>(p);
  k_main<<<256, 512, MAIN_LDS, stream>>>(p);
}
```
